```python
import torch

def _softmax(x, axis=-1):
    return torch.softmax(x, dim=axis)
def _gelu(x):
    return torch.nn.functional.gelu(x, approximate="tanh")
def _silu(x):
    return torch.nn.functional.silu(x)
def _sigmoid(x):
    return torch.sigmoid(x)
def _layernorm(x, axis=-1, eps=1e-5):
    m = x.mean(dim=axis, keepdim=True)
    v = x.var(dim=axis, keepdim=True, unbiased=False)
    return (x - m) / torch.sqrt(v + eps)
def _scatter_add(tgt, idx, src, axis=0):
    return tgt.clone().index_add_(axis, idx, src)
def _segment_sum(src, seg, n):
    out = torch.zeros((n,) + src.shape[1:], dtype=src.dtype)
    return out.index_add_(0, seg, src)
def _pad(x, pw):
    flat = tuple(v for pair in reversed(pw) for v in pair)
    return torch.nn.functional.pad(x, flat)

kernel_name = 'gated_mix__sliding_block_cached__swiglu_ffn__decode'

def setup_inputs():

    _w = (lambda a: (lambda t: t.to(torch.bfloat16) if t.is_floating_point() else t)(torch.as_tensor(a)))
    def _A_setup():
        B, Sq, D = 2, 1, 512
        Hq, Hkv, Sk, Dh = 32, 8, 16384, 128
        assert Hq % Hkv == 0, (Hq, Hkv)
        return {
            "x": _w((lambda *s: torch.randn(*s, dtype=torch.bfloat16) * 0.05)(B, Sq, D)),
            "k_cache": _w((lambda *s: torch.randn(*s, dtype=torch.bfloat16) * 6.4)(B, Hkv, Sk, Dh)),
            "v_cache": _w((lambda *s: torch.randn(*s, dtype=torch.bfloat16) * 0.4)(B, Hkv, Sk, Dh)),
            "wq": _w((lambda *s: torch.randn(*s, dtype=torch.bfloat16) * 6.4)(D, Hq * Dh) * (D ** -0.5)),
            "wo": _w((lambda *s: torch.randn(*s, dtype=torch.bfloat16) * 0.4)(Hq * Dh, D) * ((Hq * Dh) ** -0.5)),
        }
    _A_in = _A_setup()

    def _B_setup():
        B, S, D, F = 2, 1, 512, 1792
        return {
            "x": _w((lambda *s: torch.randn(*s, dtype=torch.bfloat16) * 0.05)(B, S, D)),
            "w_gate": _w((lambda *s: torch.randn(*s, dtype=torch.bfloat16) * 0.05)(D, F)),
            "w_up": _w((lambda *s: torch.randn(*s, dtype=torch.bfloat16) * 0.05)(D, F)),
            "w_down": _w((lambda *s: torch.randn(*s, dtype=torch.bfloat16) * 0.05)(F, D)),
        }
    _B_in = _B_setup()

    return {
        "A_x": _A_in["x"],
        "A_k_cache": _A_in["k_cache"],
        "A_v_cache": _A_in["v_cache"],
        "A_wq": _A_in["wq"],
        "A_wo": _A_in["wo"],
        "B_x": _B_in["x"],
        "B_w_gate": _B_in["w_gate"],
        "B_w_up": _B_in["w_up"],
        "B_w_down": _B_in["w_down"],
        "gate_w": _w((lambda *s: torch.randn(*s, dtype=torch.bfloat16) * 0.05)(512) * 16.0),
    }

def reference(A_x, A_k_cache, A_v_cache, A_wq, A_wo, B_x, B_w_gate, B_w_up, B_w_down, gate_w):
    def _A_ref(x, k_cache, v_cache, wq, wo):
        B, Sq, D = x.shape
        Hq, Dh = 32, 128
        Hkv, Sk = k_cache.shape[1], k_cache.shape[2]
        W = min(4096, max(256, Sk // 4))
        rep = Hq // Hkv
        q = (lambda x, *d: x.permute(*d))((x @ wq).reshape(B, Sq, Hq, Dh), 0, 2, 1, 3)
        k = (lambda x, n, axis: torch.repeat_interleave(x, n, dim=axis))(k_cache[:, :, Sk - W:, :], rep, axis=1)
        v = (lambda x, n, axis: torch.repeat_interleave(x, n, dim=axis))(v_cache[:, :, Sk - W:, :], rep, axis=1)
        scores = torch.einsum("bhqd,bhkd->bhqk", q, k) * (Dh ** -0.5)
        p = _softmax(scores, axis=-1)
        o = torch.einsum("bhqk,bhkd->bhqd", p, v)
        return (lambda x, *d: x.permute(*d))(o, 0, 2, 1, 3).reshape(B, Sq, Hq * Dh) @ wo

    def _B_ref(x, w_gate, w_up, w_down):
        g = _silu(x @ w_gate)
        u = x @ w_up
        return (g * u) @ w_down

    x = A_x
    g = _sigmoid((x * gate_w).sum(-1, keepdims=True))
    a = _A_ref(x, A_k_cache, A_v_cache, A_wq, A_wo)
    b = _B_ref(x, B_w_gate, B_w_up, B_w_down)
    return g * a + (1 - g) * b
```

```cpp
#include <hip/hip_runtime.h>
#include <hip/hip_bf16.h>
#include <stdint.h>

typedef unsigned short u16;
typedef unsigned v4u __attribute__((ext_vector_type(4)));

__device__ __forceinline__ float bf2f(u16 b) { return __builtin_bit_cast(float, (unsigned)b << 16); }
__device__ __forceinline__ float rbf(float f) { return __bfloat162float(__float2bfloat16(f)); }

constexpr int D = 512, HQ = 32, HKV = 8, SK = 16384, DH = 128, W = 4096, F = 1792;
constexpr int NS = 16;

__global__ void __launch_bounds__(256) k1(const u16* x, const u16* wq, const u16* wg, const u16* wu, float* q, float* h) {
  __shared__ float xs[2][D];
  __shared__ float red[2][2][4][64];
  int tid = threadIdx.x, lane = tid & 63, w = tid >> 6;
  for (int i = tid; i < 2 * D; i += 256) xs[i / D][i % D] = bf2f(x[i]);
  __syncthreads();
  int blk = blockIdx.x;
  if (blk < 64) {
    int col = blk * 64 + lane;
    float a0 = 0, a1 = 0;
    for (int k = w * 128; k < w * 128 + 128; ++k) { float wv = bf2f(wq[(size_t)k * 4096 + col]); a0 += xs[0][k] * wv; a1 += xs[1][k] * wv; }
    red[0][0][w][lane] = a0; red[0][1][w][lane] = a1;
    __syncthreads();
    if (tid < 128) { int b = tid >> 6; float s = red[0][b][0][lane] + red[0][b][1][lane] + red[0][b][2][lane] + red[0][b][3][lane]; q[b * 4096 + col] = rbf(s); }
  } else {
    int col = (blk - 64) * 64 + lane;
    float g0 = 0, g1 = 0, u0 = 0, u1 = 0;
    for (int k = w * 128; k < w * 128 + 128; ++k) { float gv = bf2f(wg[(size_t)k * F + col]), uv = bf2f(wu[(size_t)k * F + col]);
      g0 += xs[0][k] * gv; g1 += xs[1][k] * gv; u0 += xs[0][k] * uv; u1 += xs[1][k] * uv; }
    red[0][0][w][lane] = g0; red[0][1][w][lane] = g1; red[1][0][w][lane] = u0; red[1][1][w][lane] = u1;
    __syncthreads();
    if (tid < 128) { int b = tid >> 6;
      float g = rbf(red[0][b][0][lane] + red[0][b][1][lane] + red[0][b][2][lane] + red[0][b][3][lane]);
      float u = rbf(red[1][b][0][lane] + red[1][b][1][lane] + red[1][b][2][lane] + red[1][b][3][lane]);
      float s = rbf(g / (1.f + __expf(-g)));
      h[b * F + col] = rbf(s * u); }
  }
}

__global__ void __launch_bounds__(256) k2(const u16* kc, const u16* vc, const float* q, float* po, float* pml) {
  __shared__ float qs[4][DH];
  __shared__ float ps[4][256];
  __shared__ float red[4][4];
  __shared__ float ored[2][4][DH];
  int tid = threadIdx.x, lane = tid & 63, w = tid >> 6;
  int s = blockIdx.x % NS, kvh = (blockIdx.x / NS) % HKV, b = blockIdx.x / (NS * HKV);
  for (int i = tid; i < 4 * DH; i += 256) qs[i / DH][i % DH] = q[b * 4096 + kvh * 512 + i];
  __syncthreads();
  size_t base = ((size_t)(b * HKV + kvh) * SK + (SK - W) + s * 256) * DH;
  const v4u* kp = (const v4u*)(kc + base + (size_t)tid * DH);
  float sc[4] = {0, 0, 0, 0};
  for (int c = 0; c < 16; ++c) { v4u kv = kp[c];
    for (int j = 0; j < 4; ++j) { float lo = __builtin_bit_cast(float, kv[j] << 16), hi = __builtin_bit_cast(float, kv[j] & 0xffff0000u);
      for (int g = 0; g < 4; ++g) sc[g] += lo * qs[g][c * 8 + 2 * j] + hi * qs[g][c * 8 + 2 * j + 1]; } }
  float m[4];
  for (int g = 0; g < 4; ++g) { sc[g] *= 0.08838834764831845f; float t = sc[g];
    for (int o = 32; o > 0; o >>= 1) t = fmaxf(t, __shfl_xor(t, o));
    if (lane == 0) red[g][w] = t; }
  __syncthreads();
  for (int g = 0; g < 4; ++g) m[g] = fmaxf(fmaxf(red[g][0], red[g][1]), fmaxf(red[g][2], red[g][3]));
  __syncthreads();
  for (int g = 0; g < 4; ++g) { float p = __expf(sc[g] - m[g]); ps[g][tid] = p; float t = p;
    for (int o = 32; o > 0; o >>= 1) t += __shfl_xor(t, o);
    if (lane == 0) red[g][w] = t; }
  __syncthreads();
  int d = tid & 127, half = tid >> 7;
  float o[4] = {0, 0, 0, 0};
  for (int k = half * 128; k < half * 128 + 128; ++k) { float v = bf2f(vc[base + (size_t)k * DH + d]);
    for (int g = 0; g < 4; ++g) o[g] += ps[g][k] * v; }
  for (int g = 0; g < 4; ++g) ored[half][g][d] = o[g];
  __syncthreads();
  for (int i = tid; i < 512; i += 256) { int g = i / DH, dd = i % DH;
    po[((size_t)(b * HQ + kvh * 4 + g) * NS + s) * DH + dd] = ored[0][g][dd] + ored[1][g][dd]; }
  if (tid < 4) { int g = tid; pml[((b * HQ + kvh * 4 + g) * NS + s) * 2] = m[g]; pml[((b * HQ + kvh * 4 + g) * NS + s) * 2 + 1] = red[g][0] + red[g][1] + red[g][2] + red[g][3]; }
}

__global__ void __launch_bounds__(256) k3(const float* po, const float* pml, const u16* wo, const float* h, const u16* wd, float* part) {
  __shared__ float os[2][128];
  int tid = threadIdx.x;
  int blk = blockIdx.x;
  const u16* wp; int nrows;
  if (blk < 32) {
    int b = tid >> 7, d = tid & 127;
    const float* ml = pml + (b * HQ + blk) * NS * 2;
    float M = -INFINITY; for (int s = 0; s < NS; ++s) M = fmaxf(M, ml[2 * s]);
    float L = 0, o = 0; for (int s = 0; s < NS; ++s) { float wt = __expf(ml[2 * s] - M); L += wt * ml[2 * s + 1]; o += wt * po[((size_t)(b * HQ + blk) * NS + s) * DH + d]; }
    os[b][d] = rbf(o / L);
    wp = wo + (size_t)blk * 128 * D; nrows = 128;
  } else {
    int c = blk - 32; int b = tid >> 7, d = tid & 127;
    os[b][d] = h[b * F + c * 128 + d];
    wp = wd + (size_t)c * 128 * D; nrows = 128;
  }
  __syncthreads();
  float a[2][2] = {{0, 0}, {0, 0}};
  for (int k = 0; k < nrows; ++k) { unsigned wv = *(const unsigned*)(wp + (size_t)k * D + 2 * tid);
    float lo = __builtin_bit_cast(float, wv << 16), hi = __builtin_bit_cast(float, wv & 0xffff0000u);
    a[0][0] += os[0][k] * lo; a[0][1] += os[0][k] * hi; a[1][0] += os[1][k] * lo; a[1][1] += os[1][k] * hi; }
  for (int b = 0; b < 2; ++b) { part[(blk * 2 + b) * D + 2 * tid] = a[b][0]; part[(blk * 2 + b) * D + 2 * tid + 1] = a[b][1]; }
}

__global__ void __launch_bounds__(512) k4(const float* part, const u16* x, const u16* gw, __hip_bfloat16* out) {
  __shared__ float red[8];
  int b = blockIdx.x, tid = threadIdx.x;
  float t = rbf(bf2f(x[b * D + tid]) * bf2f(gw[tid]));
  for (int o = 32; o > 0; o >>= 1) t += __shfl_xor(t, o);
  if ((tid & 63) == 0) red[tid >> 6] = t;
  __syncthreads();
  float s = 0; for (int i = 0; i < 8; ++i) s += red[i];
  s = rbf(s);
  float g = rbf(1.f / (1.f + __expf(-s)));
  float a = 0, bb = 0;
  for (int i = 0; i < 32; ++i) a += part[(i * 2 + b) * D + tid];
  for (int i = 32; i < 46; ++i) bb += part[(i * 2 + b) * D + tid];
  a = rbf(a); bb = rbf(bb);
  float r = rbf(g * a) + rbf(rbf(1.f - g) * bb);
  out[b * D + tid] = __float2bfloat16(r);
}

extern "C" void kernel_launch(void* const* d_in, const int* in_sizes, int n_in,
                              void* d_out, int out_size, void* d_ws, size_t ws_size,
                              hipStream_t stream) {
  const u16* x = (const u16*)d_in[0]; const u16* kc = (const u16*)d_in[1]; const u16* vc = (const u16*)d_in[2];
  const u16* wq = (const u16*)d_in[3]; const u16* wo = (const u16*)d_in[4];
  const u16* wg = (const u16*)d_in[6]; const u16* wu = (const u16*)d_in[7]; const u16* wd = (const u16*)d_in[8]; const u16* gw = (const u16*)d_in[9];
  float* ws = (float*)d_ws;
  float* q = ws; float* h = ws + 8192; float* pml = ws + 16384; float* po = ws + 32768; float* part = ws + 32768 + 2 * HQ * NS * DH;
  k1<<<64 + 28, 256, 0, stream>>>(x, wq, wg, wu, q, h);
  k2<<<256, 256, 0, stream>>>(kc, vc, q, po, pml);
  k3<<<46, 256, 0, stream>>>(po, pml, wo, h, wd, part);
  k4<<<2, 512, 0, stream>>>(part, x, gw, (__hip_bfloat16*)d_out);
}
```

```cpp
#include <hip/hip_runtime.h>
#include <hip/hip_bf16.h>
#include <stdint.h>

#define STAMP 0
#define YLAYOUT 0
#define NT_KV 1
#define NT_WQ 1
#define NT_WO 1
#define NT_GU 1

typedef unsigned short u16;
typedef unsigned long long u64;
typedef unsigned v4u __attribute__((ext_vector_type(4)));
typedef unsigned v2u __attribute__((ext_vector_type(2)));
typedef short v4s __attribute__((ext_vector_type(4)));
typedef float f32x4 __attribute__((ext_vector_type(4)));
typedef float f32x2 __attribute__((ext_vector_type(2)));
typedef __bf16 v8bf __attribute__((ext_vector_type(8)));
typedef __bf16 v2bf __attribute__((ext_vector_type(2)));
#define GAS __attribute__((address_space(1)))
#define LAS __attribute__((address_space(3)))
#define SB() __builtin_amdgcn_sched_barrier(0)

constexpr unsigned WS_G = 0, WS_Q = 4096, WS_ST = WS_Q + 32768, WS_O = 65536, WS_Y = WS_O + 524288, WS_STAMP = 4u << 20;
constexpr unsigned L_K = 0, L_V = 65536, L_W = 131072, L_X = L_W + 16384, L_Q = L_X + 2048, QSTR = 320, L_P = L_Q + 4 * QSTR, PSTR = 576,
                   L_A = L_P + 4 * PSTR, L_MX = L_A + 128, L_SUM = L_MX + 64, L_GATE = L_SUM + 64, L_FS = L_GATE + 16, L_FC = L_FS + 16, L_END = L_FC + 16;
constexpr int SPIN_CAP = 1 << 18;

__device__ __forceinline__ float asf(unsigned u) { return __builtin_bit_cast(float, u); }
__device__ __forceinline__ unsigned asu(float f) { return __builtin_bit_cast(unsigned, f); }
__device__ __forceinline__ unsigned pk_bf16(float lo, float hi) { f32x2 x = {lo, hi}; return __builtin_bit_cast(unsigned, __builtin_convertvector(x, v2bf)); }
__device__ __forceinline__ float rbf(float f) { return asf(pk_bf16(f, f) << 16); }
template <int CTRL> __device__ __forceinline__ float dppf(float x) { return asf(__builtin_amdgcn_mov_dpp(asu(x), CTRL, 0xf, 0xf, true)); }
template <int CTRL> __device__ __forceinline__ unsigned dppu(unsigned x) { return __builtin_amdgcn_mov_dpp(x, CTRL, 0xf, 0xf, true); }
constexpr int XOR1 = 0xB1, XOR2 = 0x4E, HMIR = 0x141, ROR4 = 0x124, ROR8 = 0x128, SHL8 = 0x108;
__device__ __forceinline__ float x16sum(float x) { auto s = __builtin_amdgcn_permlane16_swap(asu(x), asu(x), false, false); return asf(s[0]) + asf(s[1]); }
__device__ __forceinline__ float x32sum(float x) { auto s = __builtin_amdgcn_permlane32_swap(asu(x), asu(x), false, false); return asf(s[0]) + asf(s[1]); }
__device__ __forceinline__ float x16max(float x) { auto s = __builtin_amdgcn_permlane16_swap(asu(x), asu(x), false, false); return fmaxf(asf(s[0]), asf(s[1])); }
__device__ __forceinline__ float x32max(float x) { auto s = __builtin_amdgcn_permlane32_swap(asu(x), asu(x), false, false); return fmaxf(asf(s[0]), asf(s[1])); }
__device__ __forceinline__ f32x4 mfma(v4u a, v4u b, f32x4 c) { return __builtin_amdgcn_mfma_f32_16x16x32_bf16(__builtin_bit_cast(v8bf, a), __builtin_bit_cast(v8bf, b), c, 0, 0, 0); }
__device__ __forceinline__ v2u trread(LAS char* p) { return __builtin_bit_cast(v2u, __builtin_amdgcn_ds_read_tr16_b64_v4i16((LAS v4s*)p)); }
__device__ __forceinline__ unsigned tagf(float v, unsigned t) { return ((asu(v) + 0x800u) & 0xFFFFF000u) | t; }
__device__ __forceinline__ float untag(unsigned w) { return asf(w & 0xFFFFF000u); }
__device__ __forceinline__ unsigned bad12(v4u g, unsigned t) { return ((g.x ^ t) | (g.y ^ t) | (g.z ^ t) | (g.w ^ t)) & 0xFFFu; }

#define DMA_S(NTSTR, voff, sbase, ldst) do { unsigned keep_; asm volatile("s_mov_b32 %0, m0\n\ts_mov_b32 m0, %3\n\ts_nop 0\n\tglobal_load_lds_dwordx4 %1, %2" NTSTR "\n\ts_mov_b32 m0, %0" \
    : "=&s"(keep_) : "v"(voff), "s"(sbase), "s"(ldst) : "memory"); } while (0)
#define DMA_V(NTSTR, vaddr, ldst) do { unsigned keep_; asm volatile("s_mov_b32 %0, m0\n\ts_mov_b32 m0, %2\n\ts_nop 0\n\tglobal_load_lds_dwordx4 %1, off" NTSTR "\n\ts_mov_b32 m0, %0" \
    : "=&s"(keep_) : "v"(vaddr), "s"(ldst) : "memory"); } while (0)
#define KVS " nt"
#define WQS " nt"
#define WOS " nt"
#define GUS " nt"
#define VMCNT(n) asm volatile("s_waitcnt vmcnt(" #n ")" ::: "memory")
#define LGKM0() asm volatile("s_waitcnt lgkmcnt(0)" ::: "memory")
__device__ __forceinline__ void ld_sc1(v4u& d, unsigned voff, const char* sbase) { asm volatile("global_load_dwordx4 %0, %1, %2 sc1" : "=v"(d) : "v"(voff), "s"(sbase) : "memory"); }
__device__ __forceinline__ void st_sc1(unsigned voff, v4u d, char* sbase) { asm volatile("global_store_dwordx4 %0, %1, %2 sc1\n\ts_nop 1" ::"v"(voff), "v"(d), "s"(sbase) : "memory"); }

#define ST(i)

__device__ __forceinline__ void lds_set(LAS char* L, unsigned off, unsigned v) { *(volatile LAS unsigned*)(L + off) = v; }
__device__ __forceinline__ void wait_s(LAS char* L, unsigned s) {
  for (int it = 0; it < SPIN_CAP; ++it) {
    v4u f = *(volatile LAS v4u*)(L + L_FS);
    unsigned m = min(min(f.x, f.y), min(f.z, f.w));
    if (__builtin_amdgcn_readfirstlane(m) >= s) break;
  }
  asm volatile("" ::: "memory");
}
__device__ __forceinline__ void wait_c(LAS char* L, unsigned s, bool sleep) {
  for (int it = 0; it < SPIN_CAP; ++it) {
    unsigned f = *(volatile LAS unsigned*)(L + L_FC);
    if (__builtin_amdgcn_readfirstlane(f) >= s) break;
    if (sleep) __builtin_amdgcn_s_sleep(1);
  }
  asm volatile("" ::: "memory");
}

__global__ void __launch_bounds__(320) fused_decode(const u16* __restrict__ x, const u16* __restrict__ kc, const u16* __restrict__ vc, const u16* __restrict__ wq,
                                                    const u16* __restrict__ wo, const u16* __restrict__ wg, const u16* __restrict__ wu, const u16* __restrict__ wd,
                                                    const u16* __restrict__ gw, u16* __restrict__ out, char* __restrict__ ws) {
  extern __shared__ __attribute__((aligned(16))) char smem[];
  LAS char* L = (LAS char*)smem;
  const unsigned lbase = (unsigned)(uintptr_t)L;
  const int c = blockIdx.x, lane = threadIdx.x & 63, wv = __builtin_amdgcn_readfirstlane(threadIdx.x >> 6);
  const int kvh = c & 7, j = c >> 3, b = j & 1, sp = j >> 1;
  const int cp = 32 * kvh + j;
  const int nf = kvh < 4 ? 32 : 24, f = (kvh < 4 ? 32 * kvh : 128 + 24 * (kvh - 4)) + j;
  const bool has_f = j < nf;
  const int g = lane >> 4, n = lane & 15;
  ST(0);

  if (wv == 0) {
    __builtin_amdgcn_s_setprio(3);
    if (lane < 8) lds_set(L, L_FS + 4 * lane, 0);
    if (lane < 4) lds_set(L, L_A + 96 + 4 * lane, 0);
    DMA_S("", lane * 16, x, lbase + L_X);
    DMA_S("", lane * 16 + 1024, x, lbase + L_X + 1024);
    unsigned G; v4u gwv;
    asm volatile("global_load_dword %0, %1, %2 sc1" : "=v"(G) : "v"(0), "s"(ws) : "memory");
    asm volatile("global_load_dwordx4 %0, %1, %2" : "=v"(gwv) : "v"(lane * 16), "s"(gw) : "memory");
    {
      const unsigned voff = (lane >> 1) * 8192 + (lane & 1) * 16;
      const char* base = (const char*)wq + kvh * 1024 + j * 32;
#pragma unroll
      for (int i = 0; i < 16; ++i) DMA_S(WQS, voff, base + (size_t)i * 32 * 8192, lbase + L_W + i * 1024);
    }
    LGKM0();
    __builtin_amdgcn_s_barrier();
    ST(1);
    asm volatile("s_waitcnt vmcnt(16)" : "+v"(G), "+v"(gwv)::"memory");
    const unsigned Gs = __builtin_amdgcn_readfirstlane(G);
    const unsigned t12 = (Gs + 1) & 0xFFFu, t16 = (Gs + 1) & 0xFFFFu;
    lds_set(L, L_FC + 4, t12);
    v4u xf[16];
#pragma unroll
    for (int s = 0; s < 16; ++s) xf[s] = *(LAS v4u*)(L + L_X + ((n >> 2) & 1) * 1024 + s * 64 + g * 16);
    {
      float gs[2];
#pragma unroll
      for (int bb = 0; bb < 2; ++bb) {
        v4u xv = *(LAS v4u*)(L + L_X + bb * 1024 + lane * 16);
        float a = 0.f;
#pragma unroll
        for (int k = 0; k < 4; ++k) {
          unsigned pr = pk_bf16(asf(xv[k] << 16) * asf(gwv[k] << 16), asf(xv[k] & 0xFFFF0000u) * asf(gwv[k] & 0xFFFF0000u));
          a += asf(pr << 16) + asf(pr & 0xFFFF0000u);
        }
        a += dppf<XOR1>(a); a += dppf<XOR2>(a); a += dppf<ROR4>(a); a += dppf<ROR8>(a); a = x16sum(a); a = x32sum(a);
        a = rbf(a);
        gs[bb] = rbf(1.f / (1.f + __expf(-a)));
      }
      if (lane == 0) { f32x4 gv = {gs[0], rbf(1.f - gs[0]), gs[1], rbf(1.f - gs[1])}; *(LAS f32x4*)(L + L_GATE) = gv; }
    }
    LAS char* wr = L + L_W + (8 * g + (n >> 2)) * 32 + (n & 3) * 8;
    VMCNT(0);
    ST(2);
    {
      f32x4 d0 = {0, 0, 0, 0}, d1 = {0, 0, 0, 0};
#pragma unroll
      for (int s = 0; s < 16; s += 2) {
        v2u a0 = trread(wr + s * 1024), a1 = trread(wr + s * 1024 + 128), a2 = trread(wr + s * 1024 + 1024), a3 = trread(wr + s * 1024 + 1152);
        d0 = mfma(xf[s], v4u{a0.x, a0.y, a1.x, a1.y}, d0);
        d1 = mfma(xf[s + 1], v4u{a2.x, a2.y, a3.x, a3.y}, d1);
      }
      const float q = d0[0] + d1[0];
      const unsigned wd_ = (pk_bf16(q, q) & 0xFFFF0000u) | t16;
      v4u gr = {dppu<0x00>(wd_), dppu<0x55>(wd_), dppu<0xAA>(wd_), dppu<0xFF>(wd_)};
      if (lane < 32 && (lane & 3) == 0) st_sc1(WS_Q + (kvh * 2 + g) * 2048 + (j * 4 + (n >> 2)) * 16, gr, ws);
    }
    ST(3);
    {
      const unsigned qo = WS_Q + (kvh * 2 + b) * 2048 + lane * 16;
      v4u q0, q1;
      for (int it = 0; it < SPIN_CAP; ++it) {
        ld_sc1(q0, qo, ws); ld_sc1(q1, qo + 1024, ws);
        asm volatile("s_waitcnt vmcnt(0)" : "+v"(q0), "+v"(q1)::"memory");
        unsigned bad = ((q0.x ^ t16) | (q0.y ^ t16) | (q0.z ^ t16) | (q0.w ^ t16) | (q1.x ^ t16) | (q1.y ^ t16) | (q1.z ^ t16) | (q1.w ^ t16)) & 0xFFFFu;
        if (__builtin_amdgcn_ballot_w64(bad != 0) == 0) break;
      }
      v2u p0 = {__builtin_amdgcn_perm(q0.y, q0.x, 0x07060302), __builtin_amdgcn_perm(q0.w, q0.z, 0x07060302)};
      v2u p1 = {__builtin_amdgcn_perm(q1.y, q1.x, 0x07060302), __builtin_amdgcn_perm(q1.w, q1.z, 0x07060302)};
      *(LAS v2u*)(L + L_Q + (lane >> 5) * QSTR + (lane & 31) * 8) = p0;
      *(LAS v2u*)(L + L_Q + (2 + (lane >> 5)) * QSTR + (lane & 31) * 8) = p1;
      LGKM0();
      lds_set(L, L_FC, 1);
    }
    ST(4);
    if (has_f) {
      const char* a = ((lane & 1) ? (const char*)wu : (const char*)wg) + (size_t)(lane >> 1) * 3584 + f * 16;
#pragma unroll
      for (int i = 0; i < 16; ++i) DMA_V(GUS, a + (size_t)i * 32 * 3584, lbase + L_W + i * 1024);
      ST(5);
      VMCNT(0);
      ST(6);
      f32x4 d0 = {0, 0, 0, 0}, d1 = {0, 0, 0, 0};
#pragma unroll
      for (int s = 0; s < 16; s += 2) {
        v2u a0 = trread(wr + s * 1024), a1 = trread(wr + s * 1024 + 128), a2 = trread(wr + s * 1024 + 1024), a3 = trread(wr + s * 1024 + 1152);
        d0 = mfma(xf[s], v4u{a0.x, a0.y, a1.x, a1.y}, d0);
        d1 = mfma(xf[s + 1], v4u{a2.x, a2.y, a3.x, a3.y}, d1);
      }
      const float gg = rbf(d0[0] + d1[0]);
      const float uu = dppf<SHL8>(gg);
      const float h = rbf(gg / (1.f + __expf(-gg))) * uu;
      if (lane < 32 && n < 8) *(LAS u16*)(L + L_A + 64 + g * 16 + n * 2) = (u16)(pk_bf16(h, h) & 0xFFFFu);
    } else {
      if (lane < 8) lds_set(L, L_A + 64 + 4 * lane, 0);
    }
    ST(7);
    {
      const int s = (lane & 7) + 8 * (lane >> 5), bb = (lane >> 3) & 1, g1 = (lane >> 4) & 1;
      const unsigned so = WS_ST + (((cp >> 3) * 2 + bb) * 16 + s) * 16;
      const unsigned oo = WS_O + ((cp * 2 + bb) * 16 + s) * 64 + g1 * 32;
      v4u st, o0, o1;
      for (int it = 0; it < SPIN_CAP; ++it) {
        ld_sc1(st, so, ws); ld_sc1(o0, oo, ws); ld_sc1(o1, oo + 16, ws);
        asm volatile("s_waitcnt vmcnt(0)" : "+v"(st), "+v"(o0), "+v"(o1)::"memory");
        unsigned bad = bad12(st, t12) | bad12(o0, t12) | bad12(o1, t12);
        if (__builtin_amdgcn_ballot_w64(bad != 0) == 0) break;
      }
      ST(8);
      const float m = untag(st.x), l = untag(st.y);
      float M = m; M = fmaxf(M, dppf<XOR1>(M)); M = fmaxf(M, dppf<XOR2>(M)); M = fmaxf(M, dppf<HMIR>(M)); M = x32max(M);
      const float e = __builtin_amdgcn_exp2f((m - M) * 1.4426950408889634f);
      float Ls = e * l; Ls += dppf<XOR1>(Ls); Ls += dppf<XOR2>(Ls); Ls += dppf<HMIR>(Ls); Ls = x32sum(Ls);
      const float wgt = e / Ls;
      float v[4];
#pragma unroll
      for (int k = 0; k < 4; ++k) {
        auto sw = __builtin_amdgcn_permlane32_swap(asu(wgt * untag(o0[k])), asu(wgt * untag(o1[k])), false, false);
        float t = asf(sw[0]) + asf(sw[1]);
        t += dppf<XOR1>(t); t += dppf<XOR2>(t); t += dppf<HMIR>(t);
        v[k] = t;
      }
      v2u pk = {pk_bf16(v[0], v[1]), pk_bf16(v[2], v[3])};
      if ((lane & 7) == 0) *(LAS v2u*)(L + L_A + bb * 32 + g1 * 16 + (lane >> 5) * 8) = pk;
      LGKM0();
      lds_set(L, L_FC, 2);
    }
    ST(9);
    {
      v4u y[4];
      unsigned yo[4];
#pragma unroll
      for (int k = 0; k < 4; ++k) yo[k] = YLAYOUT == 0 ? WS_Y + (lane + 64 * k) * 4096 + cp * 16 : WS_Y + cp * 4096 + (lane + 64 * k) * 16;
      for (int it = 0; it < SPIN_CAP; ++it) {
#pragma unroll
        for (int k = 0; k < 4; ++k) ld_sc1(y[k], yo[k], ws);
        asm volatile("s_waitcnt vmcnt(0)" : "+v"(y[0]), "+v"(y[1]), "+v"(y[2]), "+v"(y[3])::"memory");
        unsigned bad = bad12(y[0], t12) | bad12(y[1], t12) | bad12(y[2], t12) | bad12(y[3], t12);
        if (__builtin_amdgcn_ballot_w64(bad != 0) == 0) break;
      }
      ST(10);
      float v[4];
#pragma unroll
      for (int k = 0; k < 4; ++k) v[k] = (untag(y[0][k]) + untag(y[1][k])) + (untag(y[2][k]) + untag(y[3][k]));
      auto s0 = __builtin_amdgcn_permlane32_swap(asu(v[0]), asu(v[1]), false, false);
      auto s1 = __builtin_amdgcn_permlane32_swap(asu(v[2]), asu(v[3]), false, false);
      float u0 = asf(s0[0]) + asf(s0[1]), u1 = asf(s1[0]) + asf(s1[1]);
      auto s2 = __builtin_amdgcn_permlane16_swap(asu(u0), asu(u1), false, false);
      float t = asf(s2[0]) + asf(s2[1]);
      t += dppf<XOR1>(t); t += dppf<XOR2>(t); t += dppf<ROR4>(t); t += dppf<ROR8>(t);
      if (n == 0) out[cp * 4 + ((g & 1) * 2 + (g >> 1))] = (u16)(pk_bf16(t, t) & 0xFFFFu);
      if (c == 0 && lane == 0) __hip_atomic_store((GAS unsigned*)ws, Gs + 1, __ATOMIC_RELAXED, __HIP_MEMORY_SCOPE_AGENT);
    }
    ST(11);
  } else {
    const int w = wv - 1;
    const size_t kvoff = ((size_t)(b * 8 + kvh) * 16384 + 12288 + sp * 256) * 256 + w * 1024;
    const char* kb = (const char*)kc + kvoff; const char* vb = (const char*)vc + kvoff;
    unsigned koff[4];
#pragma unroll
    for (int a = 0; a < 4; ++a) koff[a] = g * 256 + ((n ^ g ^ (4 * a)) * 16);
#pragma unroll
    for (int i = 0; i < 4; ++i) DMA_S(KVS, koff[i & 3], kb + i * 4096, lbase + L_K + w * 16384 + i * 1024);
    __builtin_amdgcn_s_barrier();
#pragma unroll
    for (int i = 4; i < 16; ++i) DMA_S(KVS, koff[i & 3], kb + i * 4096, lbase + L_K + w * 16384 + i * 1024);
    ST(1);
    const unsigned voff = g * 256 + ((n ^ (2 * (g | ((w >> 1) << 2)))) * 16);
#pragma unroll
    for (int i = 0; i < 16; ++i) DMA_S(KVS, voff, vb + i * 4096, lbase + L_V + i * 4096 + w * 1024);
    ST(2);
    wait_c(L, 1, true);
    ST(3);
    const unsigned t12 = __builtin_amdgcn_readfirstlane(*(volatile LAS unsigned*)(L + L_FC + 4));
    v4u qf[4];
#pragma unroll
    for (int s = 0; s < 4; ++s) qf[s] = *(LAS v4u*)(L + L_Q + (n & 3) * QSTR + s * 64 + g * 16);
    VMCNT(16);
    f32x4 d[4];
#pragma unroll
    for (int t = 0; t < 4; ++t) d[t] = f32x4{0, 0, 0, 0};
#pragma unroll
    for (int s = 0; s < 4; ++s)
#pragma unroll
      for (int t = 0; t < 4; ++t) d[t] = mfma(*(LAS v4u*)(L + L_K + w * 16384 + (16 * t + n) * 256 + (((4 * s + g) ^ n) * 16)), qf[s], d[t]);
    float sc[4];
#pragma unroll
    for (int r = 0; r < 4; ++r) { const float lo = (n & 4) ? d[1][r] : d[0][r], hi = (n & 4) ? d[3][r] : d[2][r]; sc[r] = (n & 8) ? hi : lo; }
    {
      const unsigned a = pk_bf16(sc[0], sc[1]), bq = pk_bf16(sc[2], sc[3]);
      constexpr float SC = 0.08838834764831845f;
      const unsigned a2 = pk_bf16(asf(a << 16) * SC, asf(a & 0xFFFF0000u) * SC), b2 = pk_bf16(asf(bq << 16) * SC, asf(bq & 0xFFFF0000u) * SC);
      sc[0] = asf(a2 << 16); sc[1] = asf(a2 & 0xFFFF0000u); sc[2] = asf(b2 << 16); sc[3] = asf(b2 & 0xFFFF0000u);
    }
    float m = fmaxf(fmaxf(sc[0], sc[1]), fmaxf(sc[2], sc[3]));
    m = fmaxf(m, dppf<ROR4>(m)); m = fmaxf(m, dppf<ROR8>(m)); m = x16max(m); m = x32max(m);
    if (lane < 4) *(LAS float*)(L + L_MX + lane * 16 + w * 4) = m;
    LGKM0();
    lds_set(L, L_FS + 4 * w, 1);
    wait_s(L, 1);
    { f32x4 mm = *(LAS f32x4*)(L + L_MX + (n & 3) * 16); m = fmaxf(fmaxf(mm[0], mm[1]), fmaxf(mm[2], mm[3])); }
    float p[4];
#pragma unroll
    for (int r = 0; r < 4; ++r) p[r] = __builtin_amdgcn_exp2f((sc[r] - m) * 1.4426950408889634f);
    *(LAS v2u*)(L + L_P + (n & 3) * PSTR + (64 * (n >> 2) + 16 * g + 4 * w) * 2) = v2u{pk_bf16(p[0], p[1]), pk_bf16(p[2], p[3])};
    float l = (p[0] + p[1]) + (p[2] + p[3]);
    l += dppf<ROR4>(l); l += dppf<ROR8>(l); l = x16sum(l); l = x32sum(l);
    if (lane < 4) *(LAS float*)(L + L_SUM + lane * 16 + w * 4) = l;
    LGKM0();
    lds_set(L, L_FS + 4 * w, 2);
    ST(4);
    v4u wr[8];
    const bool act = g < 2 || (g == 2 && has_f);
    {
      const char* a = (g == 2 && has_f) ? (const char*)wd + (size_t)(8 * f) * 1024 : (const char*)wo + (size_t)(16 * cp + 8 * (g & 1)) * 1024;
      a += w * 256 + n * 16;
      const char* a4 = a + 4096;
#pragma unroll
      for (int jj = 0; jj < 4; ++jj) asm volatile("global_load_dwordx4 %0, %1, off offset:%2" WOS : "=v"(wr[jj]) : "v"(a), "i"(jj * 1024) : "memory");
#pragma unroll
      for (int jj = 0; jj < 4; ++jj) asm volatile("global_load_dwordx4 %0, %1, off offset:%2" WOS : "=v"(wr[4 + jj]) : "v"(a4), "i"(jj * 1024) : "memory");
    }
    ST(5);
    wait_s(L, 2);
    v4u pf[8];
#pragma unroll
    for (int u = 0; u < 8; ++u) pf[u] = *(LAS v4u*)(L + L_P + (n & 3) * PSTR + u * 64 + g * 16);
    {
      f32x4 mm = *(LAS f32x4*)(L + L_MX + w * 16), ls = *(LAS f32x4*)(L + L_SUM + w * 16);
      const float mh = fmaxf(fmaxf(mm[0], mm[1]), fmaxf(mm[2], mm[3])), lh = (ls[0] + ls[1]) + (ls[2] + ls[3]);
      v4u gr = {(asu(mh) & 0xFFFFF000u) | t12, tagf(lh, t12), t12, t12};
      if (lane == 0) st_sc1(WS_ST + (((kvh * 4 + w) * 2 + b) * 16 + sp) * 16, gr, ws);
      else asm volatile("" ::: "memory");
    }
    f32x4 o0 = {0, 0, 0, 0}, o1 = {0, 0, 0, 0};
    const int q4 = n >> 2, p4 = n & 3;
    const int fr = q4 | ((g & 1) << 2);
    LAS char* va0 = L + L_V + (8 * g + q4) * 256 + ((((2 * w) ^ fr) * 2 + (p4 >> 1)) * 16) + (p4 & 1) * 8;
    LAS char* va1 = L + L_V + (8 * g + q4) * 256 + ((((2 * w + 1) ^ fr) * 2 + (p4 >> 1)) * 16) + (p4 & 1) * 8;
#define PV(u) do { v2u a0 = trread(va0 + (u) * 8192), a1 = trread(va0 + (u) * 8192 + 1024), a2 = trread(va1 + (u) * 8192), a3 = trread(va1 + (u) * 8192 + 1024); \
      o0 = mfma(v4u{a0.x, a0.y, a1.x, a1.y}, pf[u], o0); o1 = mfma(v4u{a2.x, a2.y, a3.x, a3.y}, pf[u], o1); } while (0)
    VMCNT(17); lds_set(L, L_FS + 4 * w, 3); wait_s(L, 3);
    PV(0); PV(1); PV(2); PV(3);
    ST(6);
    VMCNT(11); lds_set(L, L_FS + 4 * w, 4); wait_s(L, 4);
    PV(4); PV(5); PV(6);
    VMCNT(9); lds_set(L, L_FS + 4 * w, 5);
    ST(7);
    wait_s(L, 5);
    PV(7);
    {
      v4u gr;
#pragma unroll
      for (int r = 0; r < 4; ++r) gr[r] = tagf((n & 4) ? o1[r] : o0[r], t12);
      const int cq = 8 * (4 * kvh + (n & 3)) + 2 * w + ((n >> 2) & 1);
      if (n < 8) st_sc1(WS_O + ((cq * 2 + b) * 16 + sp) * 64 + g * 16, gr, ws);
      else asm volatile("" ::: "memory");
    }
    ST(8);
    asm volatile("s_waitcnt vmcnt(2)" : "+v"(wr[0]), "+v"(wr[1]), "+v"(wr[2]), "+v"(wr[3]), "+v"(wr[4]), "+v"(wr[5]), "+v"(wr[6]), "+v"(wr[7])::"memory");
    ST(9);
    v4u bf[8];
    {
      const unsigned slo = act ? 0x05040100u : 0x0C0C0C0Cu, shi = act ? 0x07060302u : 0x0C0C0C0Cu;
#pragma unroll
      for (int k = 0; k < 4; ++k)
#pragma unroll
        for (int pr = 0; pr < 4; ++pr) {
          bf[2 * k][pr] = __builtin_amdgcn_perm(wr[2 * pr + 1][k], wr[2 * pr][k], slo);
          bf[2 * k + 1][pr] = __builtin_amdgcn_perm(wr[2 * pr + 1][k], wr[2 * pr][k], shi);
        }
    }
    const int bm = (n >> 2) & 1, ty = n & 3;
    const unsigned aoff = (ty == 0 && g < 2) ? bm * 32 + g * 16 : (ty == 1 && g == 2) ? 64 + bm * 16 : 96;
    const f32x2 gt = *(LAS f32x2*)(L + L_GATE + (g & 1) * 8);
    const unsigned yoff = YLAYOUT == 0 ? WS_Y + c * 4096 + ((g & 1) * 128 + 32 * w + 2 * n + (g >> 1)) * 16 : WS_Y + ((g & 1) * 128 + 32 * w + 2 * n + (g >> 1)) * 4096 + c * 16;
    wait_c(L, 2, false);
    ST(10);
    const v4u af = *(LAS v4u*)(L + L_A + aoff);
    f32x4 y[8];
#pragma unroll
    for (int k = 0; k < 8; ++k) y[k] = mfma(af, bf[k], f32x4{0, 0, 0, 0});
    v4u gr;
#pragma unroll
    for (int k = 0; k < 4; ++k) {
      const float a = lane < 32 ? y[k][0] : y[4 + k][0], bb = lane < 32 ? y[k][1] : y[4 + k][1];
      gr[k] = tagf(gt[0] * a + gt[1] * bb, t12);
    }
    st_sc1(yoff, gr, ws);
    ST(11);
  }
}

extern "C" void kernel_launch(void* const* d_in, const int* in_sizes, int n_in, void* d_out, int out_size, void* d_ws, size_t ws_size, hipStream_t stream) {
  (void)hipFuncSetAttribute((const void*)fused_decode, hipFuncAttributeMaxDynamicSharedMemorySize, L_END);
  fused_decode<<<256, 320, L_END, stream>>>((const u16*)d_in[0], (const u16*)d_in[1], (const u16*)d_in[2], (const u16*)d_in[3], (const u16*)d_in[4],
                                            (const u16*)d_in[6], (const u16*)d_in[7], (const u16*)d_in[8], (const u16*)d_in[9], (u16*)d_out, (char*)d_ws);
}
```

```cpp
#define PROBE -1
#define PWG 0
#define PWAVE 0
#include <hip/hip_runtime.h>
#include <hip/hip_bf16.h>
#include <stdint.h>

#define STAMP 0
#define YLAYOUT 0
#define NT_KV 1
#define NT_WQ 1
#define NT_WO 1
#define NT_GU 1

typedef unsigned short u16;
typedef unsigned long long u64;
typedef unsigned v4u __attribute__((ext_vector_type(4)));
typedef unsigned v2u __attribute__((ext_vector_type(2)));
typedef short v4s __attribute__((ext_vector_type(4)));
typedef float f32x4 __attribute__((ext_vector_type(4)));
typedef float f32x2 __attribute__((ext_vector_type(2)));
typedef __bf16 v8bf __attribute__((ext_vector_type(8)));
typedef __bf16 v2bf __attribute__((ext_vector_type(2)));
#define GAS __attribute__((address_space(1)))
#define LAS __attribute__((address_space(3)))
#define SB() __builtin_amdgcn_sched_barrier(0)

constexpr unsigned WS_G = 0, WS_Q = 4096, WS_ST = WS_Q + 32768, WS_O = 65536, WS_Y = WS_O + 524288, WS_STAMP = 4u << 20;
constexpr unsigned L_K = 0, L_V = 65536, L_W = 131072, L_X = L_W + 16384, L_Q = L_X + 2048, QSTR = 320, L_P = L_Q + 4 * QSTR, PSTR = 576,
                   L_A = L_P + 4 * PSTR, L_MX = L_A + 128, L_SUM = L_MX + 64, L_GATE = L_SUM + 64, L_FS = L_GATE + 16, L_FC = L_FS + 16, L_END = L_FC + 16;
constexpr int SPIN_CAP = 1 << 18;

__device__ __forceinline__ float asf(unsigned u) { return __builtin_bit_cast(float, u); }
__device__ __forceinline__ unsigned asu(float f) { return __builtin_bit_cast(unsigned, f); }
__device__ __forceinline__ unsigned pk_bf16(float lo, float hi) { f32x2 x = {lo, hi}; return __builtin_bit_cast(unsigned, __builtin_convertvector(x, v2bf)); }
__device__ __forceinline__ float rbf(float f) { return asf(pk_bf16(f, f) << 16); }
template <int CTRL> __device__ __forceinline__ float dppf(float x) { return asf(__builtin_amdgcn_mov_dpp(asu(x), CTRL, 0xf, 0xf, true)); }
template <int CTRL> __device__ __forceinline__ unsigned dppu(unsigned x) { return __builtin_amdgcn_mov_dpp(x, CTRL, 0xf, 0xf, true); }
constexpr int XOR1 = 0xB1, XOR2 = 0x4E, HMIR = 0x141, ROR4 = 0x124, ROR8 = 0x128, SHL8 = 0x108;
__device__ __forceinline__ float x16sum(float x) { auto s = __builtin_amdgcn_permlane16_swap(asu(x), asu(x), false, false); return asf(s[0]) + asf(s[1]); }
__device__ __forceinline__ float x32sum(float x) { auto s = __builtin_amdgcn_permlane32_swap(asu(x), asu(x), false, false); return asf(s[0]) + asf(s[1]); }
__device__ __forceinline__ float x16max(float x) { auto s = __builtin_amdgcn_permlane16_swap(asu(x), asu(x), false, false); return fmaxf(asf(s[0]), asf(s[1])); }
__device__ __forceinline__ float x32max(float x) { auto s = __builtin_amdgcn_permlane32_swap(asu(x), asu(x), false, false); return fmaxf(asf(s[0]), asf(s[1])); }
__device__ __forceinline__ f32x4 mfma(v4u a, v4u b, f32x4 c) { return __builtin_amdgcn_mfma_f32_16x16x32_bf16(__builtin_bit_cast(v8bf, a), __builtin_bit_cast(v8bf, b), c, 0, 0, 0); }
__device__ __forceinline__ v2u trread(LAS char* p) { return __builtin_bit_cast(v2u, __builtin_amdgcn_ds_read_tr16_b64_v4i16((LAS v4s*)p)); }
__device__ __forceinline__ unsigned tagf(float v, unsigned t) { return ((asu(v) + 0x800u) & 0xFFFFF000u) | t; }
__device__ __forceinline__ float untag(unsigned w) { return asf(w & 0xFFFFF000u); }
__device__ __forceinline__ unsigned bad12(v4u g, unsigned t) { return ((g.x ^ t) | (g.y ^ t) | (g.z ^ t) | (g.w ^ t)) & 0xFFFu; }

#define DMA_S(NTSTR, voff, sbase, ldst) do { unsigned keep_; asm volatile("s_mov_b32 %0, m0\n\ts_mov_b32 m0, %3\n\ts_nop 0\n\tglobal_load_lds_dwordx4 %1, %2" NTSTR "\n\ts_mov_b32 m0, %0" \
    : "=&s"(keep_) : "v"(voff), "s"(sbase), "s"(ldst) : "memory"); } while (0)
#define DMA_V(NTSTR, vaddr, ldst) do { unsigned keep_; asm volatile("s_mov_b32 %0, m0\n\ts_mov_b32 m0, %2\n\ts_nop 0\n\tglobal_load_lds_dwordx4 %1, off" NTSTR "\n\ts_mov_b32 m0, %0" \
    : "=&s"(keep_) : "v"(vaddr), "s"(ldst) : "memory"); } while (0)
#define KVS " nt"
#define WQS " nt"
#define WOS " nt"
#define GUS " nt"
#define VMCNT(n) asm volatile("s_waitcnt vmcnt(" #n ")" ::: "memory")
#define LGKM0() asm volatile("s_waitcnt lgkmcnt(0)" ::: "memory")
__device__ __forceinline__ void ld_sc1(v4u& d, unsigned voff, const char* sbase) { asm volatile("global_load_dwordx4 %0, %1, %2 sc1" : "=v"(d) : "v"(voff), "s"(sbase) : "memory"); }
__device__ __forceinline__ void st_sc1(unsigned voff, v4u d, char* sbase) { asm volatile("global_store_dwordx4 %0, %1, %2 sc1\n\ts_nop 1" ::"v"(voff), "v"(d), "s"(sbase) : "memory"); }

#define ST(i) do { SB(); stamp[i] = __builtin_amdgcn_s_memrealtime(); SB(); } while (0)

__device__ __forceinline__ void lds_set(LAS char* L, unsigned off, unsigned v) { *(volatile LAS unsigned*)(L + off) = v; }
__device__ __forceinline__ void wait_s(LAS char* L, unsigned s) {
  for (int it = 0; it < SPIN_CAP; ++it) {
    v4u f = *(volatile LAS v4u*)(L + L_FS);
    unsigned m = min(min(f.x, f.y), min(f.z, f.w));
    if (__builtin_amdgcn_readfirstlane(m) >= s) break;
  }
  asm volatile("" ::: "memory");
}
__device__ __forceinline__ void wait_c(LAS char* L, unsigned s, bool sleep) {
  for (int it = 0; it < SPIN_CAP; ++it) {
    unsigned f = *(volatile LAS unsigned*)(L + L_FC);
    if (__builtin_amdgcn_readfirstlane(f) >= s) break;
    if (sleep) __builtin_amdgcn_s_sleep(1);
  }
  asm volatile("" ::: "memory");
}

__global__ void __launch_bounds__(320) fused_decode(const u16* __restrict__ x, const u16* __restrict__ kc, const u16* __restrict__ vc, const u16* __restrict__ wq,
                                                    const u16* __restrict__ wo, const u16* __restrict__ wg, const u16* __restrict__ wu, const u16* __restrict__ wd,
                                                    const u16* __restrict__ gw, u16* __restrict__ out, char* __restrict__ ws) {
  extern __shared__ __attribute__((aligned(16))) char smem[];
  LAS char* L = (LAS char*)smem;
  const unsigned lbase = (unsigned)(uintptr_t)L;
  const int c = blockIdx.x, lane = threadIdx.x & 63, wv = __builtin_amdgcn_readfirstlane(threadIdx.x >> 6);
  const int kvh = c & 7, j = c >> 3, b = j & 1, sp = j >> 1;
  const int cp = 32 * kvh + j;
  const int nf = kvh < 4 ? 32 : 24, f = (kvh < 4 ? 32 * kvh : 128 + 24 * (kvh - 4)) + j;
  const bool has_f = j < nf;
  const int g = lane >> 4, n = lane & 15;
  u64 stamp[16];
  for (int i = 0; i < 16; ++i) stamp[i] = 0;
  ST(0);

  if (wv == 0) {
    __builtin_amdgcn_s_setprio(3);
    if (lane < 8) lds_set(L, L_FS + 4 * lane, 0);
    if (lane < 4) lds_set(L, L_A + 96 + 4 * lane, 0);
    DMA_S("", lane * 16, x, lbase + L_X);
    DMA_S("", lane * 16 + 1024, x, lbase + L_X + 1024);
    unsigned G; v4u gwv;
    asm volatile("global_load_dword %0, %1, %2 sc1" : "=v"(G) : "v"(0), "s"(ws) : "memory");
    asm volatile("global_load_dwordx4 %0, %1, %2" : "=v"(gwv) : "v"(lane * 16), "s"(gw) : "memory");
    {
      const unsigned voff = (lane >> 1) * 8192 + (lane & 1) * 16;
      const char* base = (const char*)wq + kvh * 1024 + j * 32;
#pragma unroll
      for (int i = 0; i < 16; ++i) DMA_S(WQS, voff, base + (size_t)i * 32 * 8192, lbase + L_W + i * 1024);
    }
    LGKM0();
    __builtin_amdgcn_s_barrier();
    ST(1);
    asm volatile("s_waitcnt vmcnt(16)" : "+v"(G), "+v"(gwv)::"memory");
    const unsigned Gs = __builtin_amdgcn_readfirstlane(G);
    const unsigned t12 = (Gs + 1) & 0xFFFu, t16 = (Gs + 1) & 0xFFFFu;
    lds_set(L, L_FC + 4, t12);
    v4u xf[16];
#pragma unroll
    for (int s = 0; s < 16; ++s) xf[s] = *(LAS v4u*)(L + L_X + ((n >> 2) & 1) * 1024 + s * 64 + g * 16);
    {
      float gs[2];
#pragma unroll
      for (int bb = 0; bb < 2; ++bb) {
        v4u xv = *(LAS v4u*)(L + L_X + bb * 1024 + lane * 16);
        float a = 0.f;
#pragma unroll
        for (int k = 0; k < 4; ++k) {
          unsigned pr = pk_bf16(asf(xv[k] << 16) * asf(gwv[k] << 16), asf(xv[k] & 0xFFFF0000u) * asf(gwv[k] & 0xFFFF0000u));
          a += asf(pr << 16) + asf(pr & 0xFFFF0000u);
        }
        a += dppf<XOR1>(a); a += dppf<XOR2>(a); a += dppf<ROR4>(a); a += dppf<ROR8>(a); a = x16sum(a); a = x32sum(a);
        a = rbf(a);
        gs[bb] = rbf(1.f / (1.f + __expf(-a)));
      }
      if (lane == 0) { f32x4 gv = {gs[0], rbf(1.f - gs[0]), gs[1], rbf(1.f - gs[1])}; *(LAS f32x4*)(L + L_GATE) = gv; }
    }
    LAS char* wr = L + L_W + (8 * g + (n >> 2)) * 32 + (n & 3) * 8;
    VMCNT(0);
    ST(2);
    {
      f32x4 d0 = {0, 0, 0, 0}, d1 = {0, 0, 0, 0};
#pragma unroll
      for (int s = 0; s < 16; s += 2) {
        v2u a0 = trread(wr + s * 1024), a1 = trread(wr + s * 1024 + 128), a2 = trread(wr + s * 1024 + 1024), a3 = trread(wr + s * 1024 + 1152);
        d0 = mfma(xf[s], v4u{a0.x, a0.y, a1.x, a1.y}, d0);
        d1 = mfma(xf[s + 1], v4u{a2.x, a2.y, a3.x, a3.y}, d1);
      }
      const float q = d0[0] + d1[0];
      const unsigned wd_ = (pk_bf16(q, q) & 0xFFFF0000u) | t16;
      v4u gr = {dppu<0x00>(wd_), dppu<0x55>(wd_), dppu<0xAA>(wd_), dppu<0xFF>(wd_)};
      if (lane < 32 && (lane & 3) == 0) st_sc1(WS_Q + (kvh * 2 + g) * 2048 + (j * 4 + (n >> 2)) * 16, gr, ws);
    }
    ST(3);
    {
      const unsigned qo = WS_Q + (kvh * 2 + b) * 2048 + lane * 16;
      v4u q0, q1;
      for (int it = 0; it < SPIN_CAP; ++it) {
        ld_sc1(q0, qo, ws); ld_sc1(q1, qo + 1024, ws);
        asm volatile("s_waitcnt vmcnt(0)" : "+v"(q0), "+v"(q1)::"memory");
        unsigned bad = ((q0.x ^ t16) | (q0.y ^ t16) | (q0.z ^ t16) | (q0.w ^ t16) | (q1.x ^ t16) | (q1.y ^ t16) | (q1.z ^ t16) | (q1.w ^ t16)) & 0xFFFFu;
        if (__builtin_amdgcn_ballot_w64(bad != 0) == 0) break;
      }
      v2u p0 = {__builtin_amdgcn_perm(q0.y, q0.x, 0x07060302), __builtin_amdgcn_perm(q0.w, q0.z, 0x07060302)};
      v2u p1 = {__builtin_amdgcn_perm(q1.y, q1.x, 0x07060302), __builtin_amdgcn_perm(q1.w, q1.z, 0x07060302)};
      *(LAS v2u*)(L + L_Q + (lane >> 5) * QSTR + (lane & 31) * 8) = p0;
      *(LAS v2u*)(L + L_Q + (2 + (lane >> 5)) * QSTR + (lane & 31) * 8) = p1;
      LGKM0();
      lds_set(L, L_FC, 1);
    }
    ST(4);
    if (has_f) {
      const char* a = ((lane & 1) ? (const char*)wu : (const char*)wg) + (size_t)(lane >> 1) * 3584 + f * 16;
#pragma unroll
      for (int i = 0; i < 16; ++i) DMA_V(GUS, a + (size_t)i * 32 * 3584, lbase + L_W + i * 1024);
      ST(5);
      VMCNT(0);
      ST(6);
      f32x4 d0 = {0, 0, 0, 0}, d1 = {0, 0, 0, 0};
#pragma unroll
      for (int s = 0; s < 16; s += 2) {
        v2u a0 = trread(wr + s * 1024), a1 = trread(wr + s * 1024 + 128), a2 = trread(wr + s * 1024 + 1024), a3 = trread(wr + s * 1024 + 1152);
        d0 = mfma(xf[s], v4u{a0.x, a0.y, a1.x, a1.y}, d0);
        d1 = mfma(xf[s + 1], v4u{a2.x, a2.y, a3.x, a3.y}, d1);
      }
      const float gg = rbf(d0[0] + d1[0]);
      const float uu = dppf<SHL8>(gg);
      const float h = rbf(gg / (1.f + __expf(-gg))) * uu;
      if (lane < 32 && n < 8) *(LAS u16*)(L + L_A + 64 + g * 16 + n * 2) = (u16)(pk_bf16(h, h) & 0xFFFFu);
    } else {
      if (lane < 8) lds_set(L, L_A + 64 + 4 * lane, 0);
    }
    ST(7);
    {
      const int s = (lane & 7) + 8 * (lane >> 5), bb = (lane >> 3) & 1, g1 = (lane >> 4) & 1;
      const unsigned so = WS_ST + (((cp >> 3) * 2 + bb) * 16 + s) * 16;
      const unsigned oo = WS_O + ((cp * 2 + bb) * 16 + s) * 64 + g1 * 32;
      v4u st, o0, o1;
      for (int it = 0; it < SPIN_CAP; ++it) {
        ld_sc1(st, so, ws); ld_sc1(o0, oo, ws); ld_sc1(o1, oo + 16, ws);
        asm volatile("s_waitcnt vmcnt(0)" : "+v"(st), "+v"(o0), "+v"(o1)::"memory");
        unsigned bad = bad12(st, t12) | bad12(o0, t12) | bad12(o1, t12);
        if (__builtin_amdgcn_ballot_w64(bad != 0) == 0) break;
      }
      ST(8);
      const float m = untag(st.x), l = untag(st.y);
      float M = m; M = fmaxf(M, dppf<XOR1>(M)); M = fmaxf(M, dppf<XOR2>(M)); M = fmaxf(M, dppf<HMIR>(M)); M = x32max(M);
      const float e = __builtin_amdgcn_exp2f((m - M) * 1.4426950408889634f);
      float Ls = e * l; Ls += dppf<XOR1>(Ls); Ls += dppf<XOR2>(Ls); Ls += dppf<HMIR>(Ls); Ls = x32sum(Ls);
      const float wgt = e / Ls;
      float v[4];
#pragma unroll
      for (int k = 0; k < 4; ++k) {
        auto sw = __builtin_amdgcn_permlane32_swap(asu(wgt * untag(o0[k])), asu(wgt * untag(o1[k])), false, false);
        float t = asf(sw[0]) + asf(sw[1]);
        t += dppf<XOR1>(t); t += dppf<XOR2>(t); t += dppf<HMIR>(t);
        v[k] = t;
      }
      v2u pk = {pk_bf16(v[0], v[1]), pk_bf16(v[2], v[3])};
      if ((lane & 7) == 0) *(LAS v2u*)(L + L_A + bb * 32 + g1 * 16 + (lane >> 5) * 8) = pk;
      LGKM0();
      lds_set(L, L_FC, 2);
    }
    ST(9);
    {
      v4u y[4];
      unsigned yo[4];
#pragma unroll
      for (int k = 0; k < 4; ++k) yo[k] = YLAYOUT == 0 ? WS_Y + (lane + 64 * k) * 4096 + cp * 16 : WS_Y + cp * 4096 + (lane + 64 * k) * 16;
      for (int it = 0; it < SPIN_CAP; ++it) {
#pragma unroll
        for (int k = 0; k < 4; ++k) ld_sc1(y[k], yo[k], ws);
        asm volatile("s_waitcnt vmcnt(0)" : "+v"(y[0]), "+v"(y[1]), "+v"(y[2]), "+v"(y[3])::"memory");
        unsigned bad = bad12(y[0], t12) | bad12(y[1], t12) | bad12(y[2], t12) | bad12(y[3], t12);
        if (__builtin_amdgcn_ballot_w64(bad != 0) == 0) break;
      }
      ST(10);
      float v[4];
#pragma unroll
      for (int k = 0; k < 4; ++k) v[k] = (untag(y[0][k]) + untag(y[1][k])) + (untag(y[2][k]) + untag(y[3][k]));
      auto s0 = __builtin_amdgcn_permlane32_swap(asu(v[0]), asu(v[1]), false, false);
      auto s1 = __builtin_amdgcn_permlane32_swap(asu(v[2]), asu(v[3]), false, false);
      float u0 = asf(s0[0]) + asf(s0[1]), u1 = asf(s1[0]) + asf(s1[1]);
      auto s2 = __builtin_amdgcn_permlane16_swap(asu(u0), asu(u1), false, false);
      float t = asf(s2[0]) + asf(s2[1]);
      t += dppf<XOR1>(t); t += dppf<XOR2>(t); t += dppf<ROR4>(t); t += dppf<ROR8>(t);
      if (n == 0) out[cp * 4 + ((g & 1) * 2 + (g >> 1))] = (u16)(pk_bf16(t, t) & 0xFFFFu);
      if (c == 0 && lane == 0) __hip_atomic_store((GAS unsigned*)ws, Gs + 1, __ATOMIC_RELAXED, __HIP_MEMORY_SCOPE_AGENT);
    }
    ST(11);
  } else {
    const int w = wv - 1;
    const size_t kvoff = ((size_t)(b * 8 + kvh) * 16384 + 12288 + sp * 256) * 256 + w * 1024;
    const char* kb = (const char*)kc + kvoff; const char* vb = (const char*)vc + kvoff;
    unsigned koff[4];
#pragma unroll
    for (int a = 0; a < 4; ++a) koff[a] = g * 256 + ((n ^ g ^ (4 * a)) * 16);
#pragma unroll
    for (int i = 0; i < 4; ++i) DMA_S(KVS, koff[i & 3], kb + i * 4096, lbase + L_K + w * 16384 + i * 1024);
    __builtin_amdgcn_s_barrier();
#pragma unroll
    for (int i = 4; i < 16; ++i) DMA_S(KVS, koff[i & 3], kb + i * 4096, lbase + L_K + w * 16384 + i * 1024);
    ST(1);
    const unsigned voff = g * 256 + ((n ^ (2 * (g | ((w >> 1) << 2)))) * 16);
#pragma unroll
    for (int i = 0; i < 16; ++i) DMA_S(KVS, voff, vb + i * 4096, lbase + L_V + i * 4096 + w * 1024);
    ST(2);
    wait_c(L, 1, true);
    ST(3);
    const unsigned t12 = __builtin_amdgcn_readfirstlane(*(volatile LAS unsigned*)(L + L_FC + 4));
    v4u qf[4];
#pragma unroll
    for (int s = 0; s < 4; ++s) qf[s] = *(LAS v4u*)(L + L_Q + (n & 3) * QSTR + s * 64 + g * 16);
    VMCNT(16);
    f32x4 d[4];
#pragma unroll
    for (int t = 0; t < 4; ++t) d[t] = f32x4{0, 0, 0, 0};
#pragma unroll
    for (int s = 0; s < 4; ++s)
#pragma unroll
      for (int t = 0; t < 4; ++t) d[t] = mfma(*(LAS v4u*)(L + L_K + w * 16384 + (16 * t + n) * 256 + (((4 * s + g) ^ n) * 16)), qf[s], d[t]);
    float sc[4];
#pragma unroll
    for (int r = 0; r < 4; ++r) { const float lo = (n & 4) ? d[1][r] : d[0][r], hi = (n & 4) ? d[3][r] : d[2][r]; sc[r] = (n & 8) ? hi : lo; }
    {
      const unsigned a = pk_bf16(sc[0], sc[1]), bq = pk_bf16(sc[2], sc[3]);
      constexpr float SC = 0.08838834764831845f;
      const unsigned a2 = pk_bf16(asf(a << 16) * SC, asf(a & 0xFFFF0000u) * SC), b2 = pk_bf16(asf(bq << 16) * SC, asf(bq & 0xFFFF0000u) * SC);
      sc[0] = asf(a2 << 16); sc[1] = asf(a2 & 0xFFFF0000u); sc[2] = asf(b2 << 16); sc[3] = asf(b2 & 0xFFFF0000u);
    }
    float m = fmaxf(fmaxf(sc[0], sc[1]), fmaxf(sc[2], sc[3]));
    m = fmaxf(m, dppf<ROR4>(m)); m = fmaxf(m, dppf<ROR8>(m)); m = x16max(m); m = x32max(m);
    if (lane < 4) *(LAS float*)(L + L_MX + lane * 16 + w * 4) = m;
    LGKM0();
    lds_set(L, L_FS + 4 * w, 1);
    wait_s(L, 1);
    { f32x4 mm = *(LAS f32x4*)(L + L_MX + (n & 3) * 16); m = fmaxf(fmaxf(mm[0], mm[1]), fmaxf(mm[2], mm[3])); }
    float p[4];
#pragma unroll
    for (int r = 0; r < 4; ++r) p[r] = __builtin_amdgcn_exp2f((sc[r] - m) * 1.4426950408889634f);
    *(LAS v2u*)(L + L_P + (n & 3) * PSTR + (64 * (n >> 2) + 16 * g + 4 * w) * 2) = v2u{pk_bf16(p[0], p[1]), pk_bf16(p[2], p[3])};
    float l = (p[0] + p[1]) + (p[2] + p[3]);
    l += dppf<ROR4>(l); l += dppf<ROR8>(l); l = x16sum(l); l = x32sum(l);
    if (lane < 4) *(LAS float*)(L + L_SUM + lane * 16 + w * 4) = l;
    LGKM0();
    lds_set(L, L_FS + 4 * w, 2);
    ST(4);
    v4u wr[8];
    const bool act = g < 2 || (g == 2 && has_f);
    {
      const char* a = (g == 2 && has_f) ? (const char*)wd + (size_t)(8 * f) * 1024 : (const char*)wo + (size_t)(16 * cp + 8 * (g & 1)) * 1024;
      a += w * 256 + n * 16;
      const char* a4 = a + 4096;
#pragma unroll
      for (int jj = 0; jj < 4; ++jj) asm volatile("global_load_dwordx4 %0, %1, off offset:%2" WOS : "=v"(wr[jj]) : "v"(a), "i"(jj * 1024) : "memory");
#pragma unroll
      for (int jj = 0; jj < 4; ++jj) asm volatile("global_load_dwordx4 %0, %1, off offset:%2" WOS : "=v"(wr[4 + jj]) : "v"(a4), "i"(jj * 1024) : "memory");
    }
    ST(5);
    wait_s(L, 2);
    v4u pf[8];
#pragma unroll
    for (int u = 0; u < 8; ++u) pf[u] = *(LAS v4u*)(L + L_P + (n & 3) * PSTR + u * 64 + g * 16);
    {
      f32x4 mm = *(LAS f32x4*)(L + L_MX + w * 16), ls = *(LAS f32x4*)(L + L_SUM + w * 16);
      const float mh = fmaxf(fmaxf(mm[0], mm[1]), fmaxf(mm[2], mm[3])), lh = (ls[0] + ls[1]) + (ls[2] + ls[3]);
      v4u gr = {(asu(mh) & 0xFFFFF000u) | t12, tagf(lh, t12), t12, t12};
      if (lane == 0) st_sc1(WS_ST + (((kvh * 4 + w) * 2 + b) * 16 + sp) * 16, gr, ws);
      else asm volatile("" ::: "memory");
    }
    f32x4 o0 = {0, 0, 0, 0}, o1 = {0, 0, 0, 0};
    const int q4 = n >> 2, p4 = n & 3;
    const int fr = q4 | ((g & 1) << 2);
    LAS char* va0 = L + L_V + (8 * g + q4) * 256 + ((((2 * w) ^ fr) * 2 + (p4 >> 1)) * 16) + (p4 & 1) * 8;
    LAS char* va1 = L + L_V + (8 * g + q4) * 256 + ((((2 * w + 1) ^ fr) * 2 + (p4 >> 1)) * 16) + (p4 & 1) * 8;
#define PV(u) do { v2u a0 = trread(va0 + (u) * 8192), a1 = trread(va0 + (u) * 8192 + 1024), a2 = trread(va1 + (u) * 8192), a3 = trread(va1 + (u) * 8192 + 1024); \
      o0 = mfma(v4u{a0.x, a0.y, a1.x, a1.y}, pf[u], o0); o1 = mfma(v4u{a2.x, a2.y, a3.x, a3.y}, pf[u], o1); } while (0)
    VMCNT(17); lds_set(L, L_FS + 4 * w, 3); wait_s(L, 3);
    PV(0); PV(1); PV(2); PV(3);
    ST(6);
    VMCNT(11); lds_set(L, L_FS + 4 * w, 4); wait_s(L, 4);
    PV(4); PV(5); PV(6);
    VMCNT(9); lds_set(L, L_FS + 4 * w, 5);
    ST(7);
    wait_s(L, 5);
    PV(7);
    {
      v4u gr;
#pragma unroll
      for (int r = 0; r < 4; ++r) gr[r] = tagf((n & 4) ? o1[r] : o0[r], t12);
      const int cq = 8 * (4 * kvh + (n & 3)) + 2 * w + ((n >> 2) & 1);
      if (n < 8) st_sc1(WS_O + ((cq * 2 + b) * 16 + sp) * 64 + g * 16, gr, ws);
      else asm volatile("" ::: "memory");
    }
    ST(8);
    asm volatile("s_waitcnt vmcnt(2)" : "+v"(wr[0]), "+v"(wr[1]), "+v"(wr[2]), "+v"(wr[3]), "+v"(wr[4]), "+v"(wr[5]), "+v"(wr[6]), "+v"(wr[7])::"memory");
    ST(9);
    v4u bf[8];
    {
      const unsigned slo = act ? 0x05040100u : 0x0C0C0C0Cu, shi = act ? 0x07060302u : 0x0C0C0C0Cu;
#pragma unroll
      for (int k = 0; k < 4; ++k)
#pragma unroll
        for (int pr = 0; pr < 4; ++pr) {
          bf[2 * k][pr] = __builtin_amdgcn_perm(wr[2 * pr + 1][k], wr[2 * pr][k], slo);
          bf[2 * k + 1][pr] = __builtin_amdgcn_perm(wr[2 * pr + 1][k], wr[2 * pr][k], shi);
        }
    }
    const int bm = (n >> 2) & 1, ty = n & 3;
    const unsigned aoff = (ty == 0 && g < 2) ? bm * 32 + g * 16 : (ty == 1 && g == 2) ? 64 + bm * 16 : 96;
    const f32x2 gt = *(LAS f32x2*)(L + L_GATE + (g & 1) * 8);
    const unsigned yoff = YLAYOUT == 0 ? WS_Y + c * 4096 + ((g & 1) * 128 + 32 * w + 2 * n + (g >> 1)) * 16 : WS_Y + ((g & 1) * 128 + 32 * w + 2 * n + (g >> 1)) * 4096 + c * 16;
    wait_c(L, 2, false);
    ST(10);
    const v4u af = *(LAS v4u*)(L + L_A + aoff);
    f32x4 y[8];
#pragma unroll
    for (int k = 0; k < 8; ++k) y[k] = mfma(af, bf[k], f32x4{0, 0, 0, 0});
    v4u gr;
#pragma unroll
    for (int k = 0; k < 4; ++k) {
      const float a = lane < 32 ? y[k][0] : y[4 + k][0], bb = lane < 32 ? y[k][1] : y[4 + k][1];
      gr[k] = tagf(gt[0] * a + gt[1] * bb, t12);
    }
    st_sc1(yoff, gr, ws);
    ST(11);
  }
  if (c == PWG && wv == PWAVE) {
    const unsigned tgt = 4000u + (PROBE >= 0 ? (unsigned)(stamp[PROBE >= 0 ? PROBE : 0] - stamp[0]) : 0u);
    for (int it = 0; it < 1000000; ++it) { if ((unsigned)(__builtin_amdgcn_s_memrealtime() - stamp[0]) >= tgt) break; }
  }
}

extern "C" void kernel_launch(void* const* d_in, const int* in_sizes, int n_in, void* d_out, int out_size, void* d_ws, size_t ws_size, hipStream_t stream) {
  (void)hipFuncSetAttribute((const void*)fused_decode, hipFuncAttributeMaxDynamicSharedMemorySize, L_END);
  fused_decode<<<256, 320, L_END, stream>>>((const u16*)d_in[0], (const u16*)d_in[1], (const u16*)d_in[2], (const u16*)d_in[3], (const u16*)d_in[4],
                                            (const u16*)d_in[6], (const u16*)d_in[7], (const u16*)d_in[8], (const u16*)d_in[9], (u16*)d_out, (char*)d_ws);
}
```

```cpp
#include <hip/hip_runtime.h>
#include <hip/hip_bf16.h>
#include <stdint.h>

#define STAMP 0
#define WIN 12
#define NTS " nt"
#define NTW " nt"
#define Q_DELAY 16
#define CODE_PF 22
#define ABL 0
#define TAU 240
#define TK0 1600
#define TQ 3900
#define O_SLEEP 1
#define Y_DELAY 20

typedef unsigned short u16;
typedef unsigned long long u64;
typedef unsigned v4u __attribute__((ext_vector_type(4)));
typedef int v4i __attribute__((ext_vector_type(4)));
typedef unsigned v2u __attribute__((ext_vector_type(2)));
typedef short v4s __attribute__((ext_vector_type(4)));
typedef float f32x4 __attribute__((ext_vector_type(4)));
typedef float f32x2 __attribute__((ext_vector_type(2)));
typedef __bf16 v8bf __attribute__((ext_vector_type(8)));
typedef __bf16 v2bf __attribute__((ext_vector_type(2)));
#define GAS __attribute__((address_space(1)))
#define LAS __attribute__((address_space(3)))
#define SB() __builtin_amdgcn_sched_barrier(0)

constexpr unsigned WS_Q = 4096;
constexpr unsigned WS_H = WS_Q + 131072;
constexpr unsigned WS_ST = WS_H + 229376;
constexpr unsigned WS_O = WS_ST + 16384;
constexpr unsigned WS_Y = WS_O + 524288;
constexpr unsigned WS_STAMP = 4u << 20;
constexpr unsigned L_K = 0, L_V = 65536, L_WQ = L_V + 8 * 4096, L_GU = L_V + 12 * 4096, L_WO = 131072, L_WD = L_WO + 16384, L_X = L_WD + 8192,
                   L_Q = L_X + 2048, QSTR = 320, L_P = L_Q + 4 * QSTR, PSTR = 576, L_OB = L_P + 4 * PSTR, L_HB = L_OB + 256, L_MX = L_HB + 128,
                   L_SUM = L_MX + 64, L_GATE = L_SUM + 64, L_FS = L_GATE + 16, L_FH = L_FS + 16, L_TAG = L_FH + 16, L_END = L_TAG + 16;
static_assert(L_END <= 163840, "LDS");
constexpr int SPIN_CAP = 1 << 17;

__device__ __forceinline__ float asf(unsigned u) { return __builtin_bit_cast(float, u); }
__device__ __forceinline__ unsigned asu(float f) { return __builtin_bit_cast(unsigned, f); }
__device__ __forceinline__ unsigned pk_bf16(float lo, float hi) { f32x2 x = {lo, hi}; return __builtin_bit_cast(unsigned, __builtin_convertvector(x, v2bf)); }
__device__ __forceinline__ float rbf(float f) { return asf(pk_bf16(f, f) << 16); }
template <int CTRL> __device__ __forceinline__ float dppf(float x) { return asf(__builtin_amdgcn_mov_dpp(asu(x), CTRL, 0xf, 0xf, true)); }
constexpr int XOR1 = 0xB1, XOR2 = 0x4E, ROR4 = 0x124, ROR8 = 0x128;
__device__ __forceinline__ float sw16(float a, float b) { auto s = __builtin_amdgcn_permlane16_swap(asu(a), asu(b), false, false); return asf(s[0]) + asf(s[1]); }
__device__ __forceinline__ float sw32(float a, float b) { auto s = __builtin_amdgcn_permlane32_swap(asu(a), asu(b), false, false); return asf(s[0]) + asf(s[1]); }
__device__ __forceinline__ float vmax(float a, float b) { float r; asm("v_max_f32 %0, %1, %2" : "=v"(r) : "v"(a), "v"(b)); return r; }
__device__ __forceinline__ float x16max(float x) { auto s = __builtin_amdgcn_permlane16_swap(asu(x), asu(x), false, false); return vmax(asf(s[0]), asf(s[1])); }
__device__ __forceinline__ float x32max(float x) { auto s = __builtin_amdgcn_permlane32_swap(asu(x), asu(x), false, false); return vmax(asf(s[0]), asf(s[1])); }
__device__ __forceinline__ f32x4 mfma(v4u a, v4u b, f32x4 c) { return __builtin_amdgcn_mfma_f32_16x16x32_bf16(__builtin_bit_cast(v8bf, a), __builtin_bit_cast(v8bf, b), c, 0, 0, 0); }
__device__ __forceinline__ v2u trread(LAS char* p) { return __builtin_bit_cast(v2u, __builtin_amdgcn_ds_read_tr16_b64_v4i16((LAS v4s*)p)); }
__device__ __forceinline__ v4u tr2(LAS char* p, int d) { v2u a = trread(p), b = trread(p + d); return v4u{a.x, a.y, b.x, b.y}; }
template <int B> __device__ __forceinline__ unsigned tagf(float v, unsigned t) { return ((asu(v) + (1u << (B - 1))) & ~((1u << B) - 1)) | t; }
template <int B> __device__ __forceinline__ float untag(unsigned w) { return asf(w & ~((1u << B) - 1)); }
template <int B> __device__ __forceinline__ unsigned badtag(v4u g, unsigned t) { return ((g.x ^ t) | (g.y ^ t) | (g.z ^ t) | (g.w ^ t)) & ((1u << B) - 1); }
#define PIN(x) asm volatile("" : "+v"(x))
#define CNT(i)
#define CNT0(i)
#define ANY(x) (__builtin_amdgcn_ballot_w64((x) != 0) != 0)

#define DMA_S(NTSTR, voff, sbase, ldst) asm volatile("s_mov_b32 m0, %2\n\ts_nop 0\n\tglobal_load_lds_dwordx4 %0, %1" NTSTR :: "v"(voff), "s"(sbase), "s"(ldst) : "memory")
template <int N> __device__ __forceinline__ void vmcnt() { asm volatile("s_waitcnt vmcnt(%0)" ::"i"(N) : "memory"); }
#define LGKM0() asm volatile("s_waitcnt lgkmcnt(0)" ::: "memory")
__device__ __forceinline__ void ld_sc1(v4u& d, unsigned voff, const char* sbase) { asm volatile("global_load_dwordx4 %0, %1, %2 sc1" : "=v"(d) : "v"(voff), "s"(sbase) : "memory"); }
__device__ __forceinline__ void ld1w(v4u& a, unsigned o0, const char* sb) {
  asm volatile("global_load_dwordx4 %0, %1, %2 sc1\n\ts_waitcnt vmcnt(0)" : "=&v"(a) : "v"(o0), "s"(sb) : "memory"); }
__device__ __forceinline__ void ld2w(v4u& a, v4u& b, unsigned o0, unsigned o1, const char* sb) {
  asm volatile("global_load_dwordx4 %0, %2, %4 sc1\n\tglobal_load_dwordx4 %1, %3, %4 sc1\n\ts_waitcnt vmcnt(0)" : "=&v"(a), "=&v"(b) : "v"(o0), "v"(o1), "s"(sb) : "memory"); }
__device__ __forceinline__ void ld4w(v4u& a, v4u& b, v4u& c, v4u& d, unsigned o0, const char* sb) {
  asm volatile("global_load_dwordx4 %0, %4, %5 sc1\n\tglobal_load_dwordx4 %1, %4, %5 offset:1024 sc1\n\tglobal_load_dwordx4 %2, %4, %5 offset:2048 sc1\n\t"
               "global_load_dwordx4 %3, %4, %5 offset:3072 sc1\n\ts_waitcnt vmcnt(0)" : "=&v"(a), "=&v"(b), "=&v"(c), "=&v"(d) : "v"(o0), "s"(sb) : "memory"); }
__device__ __forceinline__ void st_sc1(unsigned voff, v4u d, char* sbase) { asm volatile("global_store_dwordx4 %0, %1, %2 sc1\n\ts_nop 1" ::"v"(voff), "v"(d), "s"(sbase) : "memory"); }

#define ST(i)

__device__ __forceinline__ unsigned now16(unsigned t0) { return ((unsigned)__builtin_amdgcn_s_memrealtime() - t0) * 16u; }
__device__ __forceinline__ int vm_inflight() { unsigned r; asm volatile("s_getreg_b32 %0, hwreg(HW_REG_IB_STS, 0, 4)" : "=s"(r)::"memory"); return (int)r; }
__device__ __forceinline__ float frcp(float x) { return __builtin_amdgcn_rcpf(x); }
__device__ __forceinline__ float fexp(float x) { return __builtin_amdgcn_exp2f(x * 1.4426950408889634f); }
__device__ __forceinline__ void lds_set(LAS char* L, unsigned off, unsigned v) {
  asm volatile("s_mov_b64 exec, 1\n\tds_write_b32 %0, %1\n\ts_mov_b64 exec, -1" ::"v"((unsigned)(uintptr_t)(L + off)), "v"(v) : "memory");
}
template <int MASK, int SLEEP> __device__ __forceinline__ void wait4(LAS char* L, unsigned off, int n) {
#pragma unroll 1
  for (int it = 0; it < SPIN_CAP; ++it) {
    const v4i f = *(volatile LAS v4i*)(L + off);
    int m = 0x7fffffff;
    if (MASK & 1) m = min(m, f.x);
    if (MASK & 2) m = min(m, f.y);
    if (MASK & 4) m = min(m, f.z);
    if (MASK & 8) m = min(m, f.w);
    if (__builtin_amdgcn_readfirstlane(m) >= n) break;
    if (SLEEP) __builtin_amdgcn_s_sleep(SLEEP);
  }
  asm volatile("" ::: "memory");
}

#define TR_ADDR(w_) const int fr = (n >> 2) | ((g & 1) << 2); \
    unsigned ta0 = (8 * g + (n >> 2)) * 256 + ((((2 * (w_)) ^ fr) * 2 + ((n & 3) >> 1)) * 16) + (n & 1) * 8; \
    unsigned ta1 = (8 * g + (n >> 2)) * 256 + ((((2 * (w_) + 1) ^ fr) * 2 + ((n & 3) >> 1)) * 16) + (n & 1) * 8; PIN(ta0); PIN(ta1)

__global__ void __launch_bounds__(512) __attribute__((amdgpu_waves_per_eu(2, 2)))
fused_decode(const u16* __restrict__ x, const u16* __restrict__ kc, const u16* __restrict__ vc, const u16* __restrict__ wq,
             const u16* __restrict__ wo, const u16* __restrict__ wg, const u16* __restrict__ wu, const u16* __restrict__ wd,
             const u16* __restrict__ gw, u16* __restrict__ out, char* __restrict__ ws) {
  extern __shared__ __attribute__((aligned(16))) char smem[];
  LAS char* L = (LAS char*)smem;
  const unsigned lbase = (unsigned)(uintptr_t)L;
  const int c = blockIdx.x, lane = threadIdx.x & 63, wv = __builtin_amdgcn_readfirstlane(threadIdx.x >> 6);
  const int xc = c & 7, j = c >> 3;
  const int kvh = xc, b = j & 1, sp = j >> 1;
  const int rg = 8 * xc + (j >> 2), cg = j & 3;
  const int qp = xc >> 1, qc = 32 * (xc & 1) + j;
  const bool has_f = j < 28;
  const int g = lane >> 4, n = lane & 15;
  asm volatile("" ::"s"(x), "s"(kc), "s"(vc), "s"(wq), "s"(wo), "s"(wg), "s"(wu), "s"(wd), "s"(gw), "s"(out), "s"(ws));
  const unsigned t0 = (unsigned)__builtin_amdgcn_s_memrealtime();
  ST(0);
  if (wv == 4) {
    const char* pc;
    asm volatile("s_getpc_b64 %0" : "=s"(pc));
    asm volatile("s_mov_b32 m0, %2\n\ts_nop 0\n\tglobal_load_lds_dword %0, %1" :: "v"(min(j, CODE_PF) * 512 + (lane & 7) * 64), "s"((const char*)((uintptr_t)pc & ~(uintptr_t)255)), "s"(lbase + L_OB) : "memory");
  }

  if (wv >= 4) {
    const int hw = wv - 4;
    const unsigned fh = L_FH + 4 * hw;
    __builtin_amdgcn_s_setprio(2);
    __builtin_amdgcn_s_barrier();
    wait4<15, 0>(L, L_FS, 1);
    const unsigned t32 = __builtin_amdgcn_readfirstlane(*(LAS unsigned*)(L + L_P + 1024)) + 1, t12 = t32 & 0xFFFu, t8 = t32 & 0xFFu;
    ST(1);
    if (hw < 2) {
      v4u xq[4];
#pragma unroll
      for (int s = 0; s < 4; ++s) xq[s] = *(LAS v4u*)(L + L_X + (n & 1) * 1024 + qp * 256 + s * 64 + g * 16);
      const int col = 64 * qc + 32 * hw + 16 * ((n >> 1) & 1) + 4 * g;
      const unsigned qoff = WS_Q + (((((col >> 9) * 2 + (n & 1)) * 4 + ((col >> 7) & 3)) * 4 + qp) * 128 + (col & 127)) * 4;
      LAS char* a = L + L_WQ + (8 * g + (n >> 2)) * 128 + hw * 64 + (n & 3) * 8;
      wait4<15, 0>(L, L_FS, 5);
      ST(2);
      v4u af[8];
#pragma unroll
      for (int s = 0; s < 4; ++s) { af[2 * s] = tr2(a + s * 4096, 512); af[2 * s + 1] = tr2(a + s * 4096 + 32, 512); }
      SB();
      f32x4 d0 = {0, 0, 0, 0}, d1 = {0, 0, 0, 0};
#pragma unroll
      for (int s = 0; s < 4; ++s) { d0 = mfma(af[2 * s], xq[s], d0); d1 = mfma(af[2 * s + 1], xq[s], d1); }
      v4u gr;
#pragma unroll
      for (int r = 0; r < 4; ++r) gr[r] = tagf<8>((n & 2) ? d1[r] : d0[r], t8);
      if (n < 4) st_sc1(qoff, gr, ws);
      else asm volatile("" ::: "memory");
      lds_set(L, fh, 1);
    } else {
      v4u xg[2];
#pragma unroll
      for (int s = 0; s < 2; ++s) xg[s] = *(LAS v4u*)(L + L_X + (n & 1) * 1024 + xc * 128 + s * 64 + g * 16);
      const unsigned hoff = WS_H + ((((n & 1) * 2 + (hw & 1)) * 8 + xc) * 448 + 16 * j + 4 * ((n >> 1) & 3) + g) * 16;
      LAS char* a = L + L_GU + (hw & 1) * 8192 + (8 * g + (n >> 2)) * 128 + (n & 3) * 8;
      {
        const v4u gwv = *(LAS v4u*)(L + L_P + lane * 16), xv = *(LAS v4u*)(L + L_X + (hw & 1) * 1024 + lane * 16);
        float s = 0.f;
#pragma unroll
        for (int k = 0; k < 4; ++k) {
          const unsigned pr = pk_bf16(asf(xv[k] << 16) * asf(gwv[k] << 16), asf(xv[k] & 0xFFFF0000u) * asf(gwv[k] & 0xFFFF0000u));
          s += asf(pr << 16) + asf(pr & 0xFFFF0000u);
        }
        s += dppf<XOR1>(s); s += dppf<XOR2>(s); s += dppf<ROR4>(s); s += dppf<ROR8>(s); s = sw16(s, s); s = sw32(s, s);
        const float gs = rbf(frcp(1.f + fexp(-rbf(s))));
        if (lane == 0) *(LAS f32x2*)(L + L_GATE + (hw & 1) * 8) = f32x2{gs, rbf(1.f - gs)};
        lds_set(L, L_TAG, t32);
      }
      wait4<15, 1>(L, L_FS, 9);
      ST(2);
      v4u af[8];
#pragma unroll
      for (int s = 0; s < 2; ++s)
#pragma unroll
        for (int t = 0; t < 4; ++t) af[4 * s + t] = tr2(a + s * 4096 + t * 32, 512);
      SB();
      f32x4 d[4];
#pragma unroll
      for (int t = 0; t < 4; ++t) d[t] = mfma(af[4 + t], xg[1], mfma(af[t], xg[0], f32x4{0, 0, 0, 0}));
      v4u gr;
#pragma unroll
      for (int r = 0; r < 4; ++r) { const float lo = (n & 2) ? d[1][r] : d[0][r], hi = (n & 2) ? d[3][r] : d[2][r]; gr[r] = tagf<12>((n & 4) ? hi : lo, t12); }
      if (n < 8 && has_f) st_sc1(hoff, gr, ws);
      else asm volatile("" ::: "memory");
      lds_set(L, fh, 1);
    }
    ST(3);
    {
      const int e = lane & 7;
      const unsigned qo = WS_Q + ((kvh * 2 + b) * 4 + hw) * 2048 + lane * 16;
      const unsigned ho = WS_H + (((hw & 1) * 16 + (lane >> 3)) * 448 + 7 * rg + (e < 7 ? e : 6)) * 16;
#pragma unroll 1
      for (int it = 0; it < SPIN_CAP && now16(t0) < TQ; ++it) __builtin_amdgcn_s_sleep(1);
      v4u q0, q1;
      CNT0(14); CNT0(15);
#pragma unroll 1
      for (int it = 0; it < SPIN_CAP; ++it) { ld2w(q0, q1, qo, qo + 1024, ws); if (!ANY(badtag<8>(q0, t8) | badtag<8>(q1, t8))) break; CNT(14); }
      float v[4];
#pragma unroll
      for (int r = 0; r < 4; ++r) { v[r] = untag<8>(q0[r]) + untag<8>(q1[r]); v[r] = sw32(v[r], v[r]); }
      if (lane < 32) *(LAS v2u*)(L + L_Q + hw * QSTR + lane * 8) = v2u{pk_bf16(v[0], v[1]), pk_bf16(v[2], v[3])};
      LGKM0(); lds_set(L, fh, 2);
      ST(4);
      if (hw >= 2) {
        v4u h0, h1;
#pragma unroll 1
        for (int it = 0; it < SPIN_CAP; ++it) { ld2w(h0, h1, ho, ho + 8 * 448 * 16, ws); if (!ANY(badtag<12>(h0, t12) | badtag<12>(h1, t12))) break; CNT(15); }
        float hv[4];
#pragma unroll
        for (int r = 0; r < 4; ++r) {
          float gg = untag<12>(h0[r]), uu = untag<12>(h1[r]);
          gg += dppf<ROR8>(gg); uu += dppf<ROR8>(uu); gg = sw16(gg, gg); uu = sw16(uu, uu); gg = rbf(sw32(gg, gg)); uu = rbf(sw32(uu, uu));
          hv[r] = rbf(gg * frcp(1.f + fexp(-gg))) * uu;
        }
        if (lane < 8) *(LAS v2u*)(L + L_HB + (hw & 1) * 64 + e * 8) = v2u{e < 7 ? pk_bf16(hv[0], hv[1]) : 0u, e < 7 ? pk_bf16(hv[2], hv[3]) : 0u};
      }
    }
    ST(5);
    wait4<15, 1>(L, L_FH, 2);
    v4u qf[4], kf[16];
#pragma unroll
    for (int s = 0; s < 4; ++s) qf[s] = *(LAS v4u*)(L + L_Q + (n & 3) * QSTR + s * 64 + g * 16);
    wait4<15, 0>(L, L_FS, 13 + 4 * hw);
    ST(6);
#pragma unroll
    for (int s = 0; s < 4; ++s)
#pragma unroll
      for (int t = 0; t < 4; ++t) kf[s * 4 + t] = *(LAS v4u*)(L + L_K + (4 * hw + t) * 4096 + n * 256 + (((4 * s + g) ^ n) * 16));
    SB();
    float sc[4];
    {
      f32x4 d[4];
#pragma unroll
      for (int t = 0; t < 4; ++t) d[t] = f32x4{0, 0, 0, 0};
#pragma unroll
      for (int s = 0; s < 4; ++s)
#pragma unroll
        for (int t = 0; t < 4; ++t) d[t] = mfma(kf[s * 4 + t], qf[s], d[t]);
      constexpr float SC = 0.08838834764831845f;
      float v[4];
#pragma unroll
      for (int r = 0; r < 4; ++r) { const float lo = (n & 4) ? d[1][r] : d[0][r], hi = (n & 4) ? d[3][r] : d[2][r]; v[r] = (n & 8) ? hi : lo; }
      const unsigned a = pk_bf16(v[0], v[1]), bq = pk_bf16(v[2], v[3]);
      const unsigned a2 = pk_bf16(asf(a << 16) * SC, asf(a & 0xFFFF0000u) * SC), b2 = pk_bf16(asf(bq << 16) * SC, asf(bq & 0xFFFF0000u) * SC);
      sc[0] = asf(a2 << 16); sc[1] = asf(a2 & 0xFFFF0000u); sc[2] = asf(b2 << 16); sc[3] = asf(b2 & 0xFFFF0000u);
    }
    float m = vmax(vmax(sc[0], sc[1]), vmax(sc[2], sc[3]));
    m = vmax(m, dppf<ROR4>(m)); m = vmax(m, dppf<ROR8>(m)); m = x16max(m); m = x32max(m);
    if (lane < 4) *(LAS float*)(L + L_MX + lane * 16 + hw * 4) = m;
    LGKM0(); lds_set(L, fh, 3);
    wait4<15, 0>(L, L_FH, 3);
    { const f32x4 mm = *(LAS f32x4*)(L + L_MX + (n & 3) * 16); m = vmax(vmax(mm[0], mm[1]), vmax(mm[2], mm[3])); }
    {
      float p[4];
#pragma unroll
      for (int r = 0; r < 4; ++r) p[r] = __builtin_amdgcn_exp2f((sc[r] - m) * 1.4426950408889634f);
      *(LAS v2u*)(L + L_P + (n & 3) * PSTR + (64 * hw + 16 * (n >> 2) + 4 * g) * 2) = v2u{pk_bf16(p[0], p[1]), pk_bf16(p[2], p[3])};
      float l = (p[0] + p[1]) + (p[2] + p[3]);
      l += dppf<ROR4>(l); l += dppf<ROR8>(l); l = sw16(l, l); l = sw32(l, l);
      if (lane < 4) *(LAS float*)(L + L_SUM + lane * 16 + hw * 4) = l;
    }
    LGKM0(); lds_set(L, fh, 4);
    ST(7);
    TR_ADDR(hw);
    unsigned ooff = WS_O + (((2 * (4 * kvh + (n & 3)) + (hw >> 1)) * 2 + b) * 16 + sp) * 256 + (hw & 1) * 128 + ((n >> 2) & 1) * 64 + g * 16;
    PIN(ooff);
    wait4<15, 0>(L, L_FH, 4);
    v4u pf[8];
#pragma unroll
    for (int u = 0; u < 8; ++u) pf[u] = *(LAS v4u*)(L + L_P + (n & 3) * PSTR + u * 64 + g * 16);
    if (hw == 2) {
      const f32x4 ls = *(LAS f32x4*)(L + L_SUM + (n & 3) * 16);
      const v4u gr = {(asu(m) & 0xFFFFF000u) | t12, tagf<12>((ls[0] + ls[1]) + (ls[2] + ls[3]), t12), t12, t12};
      if (lane < 4) st_sc1(WS_ST + (((kvh * 4 + lane) * 2 + b) * 16 + sp) * 16, gr, ws);
    }
    f32x4 o0 = {0, 0, 0, 0}, o1 = {0, 0, 0, 0};
#define PVN(u0, nu) do { v4u va[nu], vb[nu]; _Pragma("unroll") for (int u = 0; u < nu; ++u) { va[u] = tr2(L + L_V + ta0 + (u0 + u) * 8192, 1024); vb[u] = tr2(L + L_V + ta1 + (u0 + u) * 8192, 1024); } SB(); \
      _Pragma("unroll") for (int u = 0; u < nu; ++u) { o0 = mfma(va[u], pf[u0 + u], o0); o1 = mfma(vb[u], pf[u0 + u], o1); } } while (0)
    wait4<15, 1>(L, L_FS, 33);
    PVN(0, 4); PIN(o0); PIN(o1);
    ST(8);
    wait4<15, 0>(L, L_FS, 39);
    PVN(4, 3); PIN(o0); PIN(o1);
    wait4<15, 0>(L, L_FS, 41);
    ST(9);
    PVN(7, 1);
    {
      v4u gr;
#pragma unroll
      for (int r = 0; r < 4; ++r) gr[r] = tagf<12>((n & 4) ? o1[r] : o0[r], t12);
      if (n < 8) st_sc1(ooff, gr, ws);
      else asm volatile("" ::: "memory");
    }
    ST(10);
    if (hw < 2) {
      const unsigned so = WS_ST + (((rg >> 1) * 2 + hw) * 16 + 4 * (n & 3) + g) * 16;
      const unsigned oo = WS_O + (rg * 2 + hw) * 4096 + lane * 16;
      v4u st;
#pragma unroll 1
      for (int it = 0; it < SPIN_CAP; ++it) { ld1w(st, so, ws); if (!ANY(badtag<12>(st, t12))) break; CNT(14); }
      const float ms = untag<12>(st.x), ls = untag<12>(st.y);
      float M = ms; M = vmax(M, dppf<XOR1>(M)); M = vmax(M, dppf<XOR2>(M)); M = x16max(M); M = x32max(M);
      const float ex = __builtin_amdgcn_exp2f((ms - M) * 1.4426950408889634f);
      float Ls = ex * ls; Ls += dppf<XOR1>(Ls); Ls += dppf<XOR2>(Ls); Ls = sw16(Ls, Ls); Ls = sw32(Ls, Ls);
      const float wv_ = ex * frcp(Ls);
      float wt0 = dppf<0x00>(wv_), wt1 = dppf<0x55>(wv_), wt2 = dppf<0xAA>(wv_), wt3 = dppf<0xFF>(wv_);
      PIN(wt0); PIN(wt1); PIN(wt2); PIN(wt3);
      ST(11);
      v4u a0, a1, a2, a3;
#pragma unroll 1
      for (int it = 0; it < SPIN_CAP; ++it) {
        ld4w(a0, a1, a2, a3, oo, ws);
        if (!ANY(badtag<12>(a0, t12) | badtag<12>(a1, t12) | badtag<12>(a2, t12) | badtag<12>(a3, t12))) break;
        CNT(15);
        if (O_SLEEP) __builtin_amdgcn_s_sleep(O_SLEEP);
      }
      ST(12);
      float v[4];
#pragma unroll
      for (int r = 0; r < 4; ++r) v[r] = fmaf(wt0, untag<12>(a0[r]), wt1 * untag<12>(a1[r])) + fmaf(wt2, untag<12>(a2[r]), wt3 * untag<12>(a3[r]));
      const float t = sw16(sw32(v[0], v[1]), sw32(v[2], v[3]));
      *(LAS u16*)(L + L_OB + hw * 128 + (4 * n + (g & 1) * 2 + (g >> 1)) * 2) = (u16)(pk_bf16(t, t) & 0xFFFFu);
      LGKM0();
      ST(13);
      __builtin_amdgcn_s_barrier();
    } else if (hw == 2) {
      __builtin_amdgcn_s_barrier();
    } else {
      unsigned dummy;
      asm volatile("s_nop 4\n\tglobal_load_dword %0, %1, %2\n\ts_waitcnt vmcnt(0)" : "=&v"(dummy) : "v"(0), "s"(out) : "memory");
      unsigned yo = WS_Y + (((xc >> 1) * 64 + lane) * 2 + (xc & 1)) * 512 + j * 16;
      u16* op = out + (xc & 1) * 512 + (xc >> 1) * 128 + 4 * j + (g & 1) * 2 + (g >> 1);
      v4u y;
      PIN(yo); PIN(op);
      __builtin_amdgcn_s_barrier();
      for (int z = 0; z < Y_DELAY; ++z) __builtin_amdgcn_s_sleep(1);
#pragma unroll 1
      for (int it = 0; it < SPIN_CAP; ++it) {
        ld1w(y, yo, ws);
        if (!ANY(badtag<12>(y, t12))) break;
        CNT(15);
      }
      ST(12);
      float t = sw16(sw32(untag<12>(y.x), untag<12>(y.y)), sw32(untag<12>(y.z), untag<12>(y.w)));
      t += dppf<XOR1>(t); t += dppf<XOR2>(t); t += dppf<ROR4>(t); t += dppf<ROR8>(t);
      if (n == 0) *op = (u16)(pk_bf16(t, t) & 0xFFFFu);
      if (c == 0 && lane == 0) __hip_atomic_store((GAS unsigned*)ws, t32, __ATOMIC_RELAXED, __HIP_MEMORY_SCOPE_AGENT);
      ST(13);
    }
  } else {
    const int w = wv;
    const unsigned fs = L_FS + 4 * w;
    int issued = 9, landed = 0;
    unsigned due = TK0 + w * (TAU / 4);
#define PACE() do { _Pragma("unroll 1") for (int it_ = 0; it_ < SPIN_CAP; ++it_) { const int vm_ = vm_inflight(); if (issued - vm_ != landed) { landed = issued - vm_; lds_set(L, fs, landed); } \
        if (now16(t0) >= due && vm_ < WIN) break; } due += TAU; ++issued; } while (0)
    {
      const char* sb = w == 0 ? (const char*)x : w == 1 ? (const char*)x + 1024 : w == 2 ? (const char*)gw : (const char*)ws;
      DMA_S("", w == 3 ? 0 : lane * 16, sb, lbase + (w < 2 ? L_X : L_P - 2048) + w * 1024);
    }
    {
      const char* base = (const char*)wq + (size_t)(128 * qp + 8 * w) * 8192 + qc * 128;
      const unsigned voff = (lane >> 3) * 8192 + (lane & 7) * 16;
#pragma unroll
      for (int i = 0; i < 4; ++i) DMA_S(NTW, voff, base + (size_t)i * 32 * 8192, lbase + L_WQ + (4 * i + w) * 1024);
    }
    if (w == 0 && lane < 8) *(volatile LAS int*)(L + L_FS + 4 * lane) = -64;
    {
      const unsigned voff = has_f ? (lane >> 3) * 3584 + (lane & 7) * 16 : 0;
      const size_t off = has_f ? (size_t)(64 * xc + 8 * w) * 3584 + j * 128 : 0;
      const unsigned st = has_f ? 32 * 3584 : 0;
#pragma unroll
      for (int i = 0; i < 4; ++i) { DMA_S(NTW, voff, (const char*)(i < 2 ? wg : wu) + off + (i & 1) * st, lbase + L_GU + (4 * i + w) * 1024); }
    }
    LGKM0();
    __builtin_amdgcn_s_barrier();
    const size_t kvoff = ((size_t)(b * 8 + kvh) * 16384 + 12288 + sp * 256) * 256 + w * 1024;
    {
      const char* kb = (const char*)kc + kvoff;
      const unsigned voff = g * 256 + ((n ^ (4 * w + g)) * 16);
#pragma unroll 1
      for (int i = 0; i < 16; ++i) { PACE(); DMA_S(NTS, voff, kb + (((i + j) & 15) << 12), lbase + L_K + i * 4096 + w * 1024); }
    }
    ST(1);
    {
      const unsigned voff = g * 256 + ((n ^ (2 * (g | ((w >> 1) << 2)))) * 16);
      const char* vb = (const char*)vc + kvoff;
#pragma unroll 1
      for (int i = 0; i < 8; ++i) { PACE(); DMA_S(NTS, voff, vb + (((i + j) & 15) << 12), lbase + L_V + i * 4096 + w * 1024); }
      wait4<15, 0>(L, L_FH, 1);
#pragma unroll 1
      for (int i = 8; i < 16; ++i) { PACE(); DMA_S(NTS, voff, vb + (((i + j) & 15) << 12), lbase + L_V + i * 4096 + w * 1024); }
    }
    ST(2);
    {
      const unsigned vo = g * 1024 + ((n ^ (2 * (g | ((w >> 1) << 2)))) * 16);
      const char* ob = (const char*)wo + (size_t)(64 * rg + 4 * w) * 1024 + cg * 256;
      const char* db = (const char*)wd + (size_t)(28 * rg) * 1024 + cg * 256;
#pragma unroll 1
      for (int i = 0; i < 6; ++i) {
        const char* src = i < 4 ? ob + i * 16384 : db + (i == 4 ? 4 * w : w < 3 ? 16 + 4 * w : 0) * 1024;
        PACE();
        DMA_S(NTW, vo, src, lbase + L_WO + i * 4096 + w * 1024);
      }
    }
    ST(3);
#pragma unroll 1
    for (int it = 0; it < SPIN_CAP; ++it) { const int vm = vm_inflight(); if (issued - vm != landed) { landed = issued - vm; lds_set(L, fs, landed); } if (landed >= 41) break; }
    ST(4);
    const unsigned t12 = __builtin_amdgcn_readfirstlane(*(volatile LAS unsigned*)(L + L_TAG)) & 0xFFFu;
    TR_ADDR(w);
    unsigned yoff = WS_Y + ((cg * 64 + rg) * 2 + (n & 1)) * 512 + w * 128 + ((n >> 1) & 1) * 64 + g * 16;
    PIN(yoff);
    vmcnt<0>(); lds_set(L, fs, 47);
    ST(5);
    wait4<12, 0>(L, L_FH, 3);
    wait4<15, 0>(L, L_FS, 47);
    v4u wf[4];
    wf[0] = tr2(L + L_WO + ta0, 1024); wf[1] = tr2(L + L_WO + ta1, 1024); wf[2] = tr2(L + L_WO + ta0 + 8192, 1024); wf[3] = tr2(L + L_WO + ta1 + 8192, 1024);
    const f32x2 gt = *(LAS f32x2*)(L + L_GATE + (n & 1) * 8);
    f32x4 fy;
    {
      const v4u df0 = tr2(L + L_WD + ta0, 1024), df1 = tr2(L + L_WD + ta1, 1024);
      const v4u hb = *(LAS v4u*)(L + L_HB + (n & 1) * 64 + g * 16);
      const f32x4 f0 = mfma(df0, hb, f32x4{0, 0, 0, 0}), f1 = mfma(df1, hb, f32x4{0, 0, 0, 0});
#pragma unroll
      for (int r = 0; r < 4; ++r) fy[r] = gt[1] * ((n & 2) ? f1[r] : f0[r]);
    }
    PIN(fy); PIN(wf[0]); PIN(wf[1]); PIN(wf[2]); PIN(wf[3]);
    ST(6);
    __builtin_amdgcn_s_barrier();
    asm volatile("" ::: "memory");
    ST(10);
    {
      const v4u b0 = *(LAS v4u*)(L + L_OB + (n & 1) * 128 + g * 16), b1 = *(LAS v4u*)(L + L_OB + (n & 1) * 128 + 64 + g * 16);
      f32x4 y0 = mfma(wf[0], b0, f32x4{0, 0, 0, 0}), y1 = mfma(wf[1], b0, f32x4{0, 0, 0, 0});
      y0 = mfma(wf[2], b1, y0); y1 = mfma(wf[3], b1, y1);
      v4u gr;
#pragma unroll
      for (int r = 0; r < 4; ++r) gr[r] = tagf<12>(fmaf(gt[0], (n & 2) ? y1[r] : y0[r], fy[r]), t12);
      if (n < 4) st_sc1(yoff, gr, ws);
      else asm volatile("" ::: "memory");
    }
    ST(11);
  }
}

extern "C" void kernel_launch(void* const* d_in, const int* in_sizes, int n_in, void* d_out, int out_size, void* d_ws, size_t ws_size, hipStream_t stream) {
  (void)hipFuncSetAttribute((const void*)fused_decode, hipFuncAttributeMaxDynamicSharedMemorySize, L_END);
  fused_decode<<<256, 512, L_END, stream>>>((const u16*)d_in[0], (const u16*)d_in[1], (const u16*)d_in[2], (const u16*)d_in[3], (const u16*)d_in[4],
                                            (const u16*)d_in[6], (const u16*)d_in[7], (const u16*)d_in[8], (const u16*)d_in[9], (u16*)d_out, (char*)d_ws);
}
```

```cpp
#define PROBE -1
#define PWG 0
#define PWAVE 0
#include <hip/hip_runtime.h>
#include <hip/hip_bf16.h>
#include <stdint.h>

#define STAMP 0
#define WIN 12
#define NTS " nt"
#define NTW " nt"
#define Q_DELAY 16
#define CODE_PF 22
#define ABL 0
#define TAU 240
#define TK0 1600
#define TQ 3900
#define O_SLEEP 1
#define Y_DELAY 20

typedef unsigned short u16;
typedef unsigned long long u64;
typedef unsigned v4u __attribute__((ext_vector_type(4)));
typedef int v4i __attribute__((ext_vector_type(4)));
typedef unsigned v2u __attribute__((ext_vector_type(2)));
typedef short v4s __attribute__((ext_vector_type(4)));
typedef float f32x4 __attribute__((ext_vector_type(4)));
typedef float f32x2 __attribute__((ext_vector_type(2)));
typedef __bf16 v8bf __attribute__((ext_vector_type(8)));
typedef __bf16 v2bf __attribute__((ext_vector_type(2)));
#define GAS __attribute__((address_space(1)))
#define LAS __attribute__((address_space(3)))
#define SB() __builtin_amdgcn_sched_barrier(0)

constexpr unsigned WS_Q = 4096;
constexpr unsigned WS_H = WS_Q + 131072;
constexpr unsigned WS_ST = WS_H + 229376;
constexpr unsigned WS_O = WS_ST + 16384;
constexpr unsigned WS_Y = WS_O + 524288;
constexpr unsigned WS_STAMP = 4u << 20;
constexpr unsigned L_K = 0, L_V = 65536, L_WQ = L_V + 8 * 4096, L_GU = L_V + 12 * 4096, L_WO = 131072, L_WD = L_WO + 16384, L_X = L_WD + 8192,
                   L_Q = L_X + 2048, QSTR = 320, L_P = L_Q + 4 * QSTR, PSTR = 576, L_OB = L_P + 4 * PSTR, L_HB = L_OB + 256, L_MX = L_HB + 128,
                   L_SUM = L_MX + 64, L_GATE = L_SUM + 64, L_FS = L_GATE + 16, L_FH = L_FS + 16, L_TAG = L_FH + 16, L_END = L_TAG + 16;
static_assert(L_END <= 163840, "LDS");
constexpr int SPIN_CAP = 1 << 17;

__device__ __forceinline__ float asf(unsigned u) { return __builtin_bit_cast(float, u); }
__device__ __forceinline__ unsigned asu(float f) { return __builtin_bit_cast(unsigned, f); }
__device__ __forceinline__ unsigned pk_bf16(float lo, float hi) { f32x2 x = {lo, hi}; return __builtin_bit_cast(unsigned, __builtin_convertvector(x, v2bf)); }
__device__ __forceinline__ float rbf(float f) { return asf(pk_bf16(f, f) << 16); }
template <int CTRL> __device__ __forceinline__ float dppf(float x) { return asf(__builtin_amdgcn_mov_dpp(asu(x), CTRL, 0xf, 0xf, true)); }
constexpr int XOR1 = 0xB1, XOR2 = 0x4E, ROR4 = 0x124, ROR8 = 0x128;
__device__ __forceinline__ float sw16(float a, float b) { auto s = __builtin_amdgcn_permlane16_swap(asu(a), asu(b), false, false); return asf(s[0]) + asf(s[1]); }
__device__ __forceinline__ float sw32(float a, float b) { auto s = __builtin_amdgcn_permlane32_swap(asu(a), asu(b), false, false); return asf(s[0]) + asf(s[1]); }
__device__ __forceinline__ float vmax(float a, float b) { float r; asm("v_max_f32 %0, %1, %2" : "=v"(r) : "v"(a), "v"(b)); return r; }
__device__ __forceinline__ float x16max(float x) { auto s = __builtin_amdgcn_permlane16_swap(asu(x), asu(x), false, false); return vmax(asf(s[0]), asf(s[1])); }
__device__ __forceinline__ float x32max(float x) { auto s = __builtin_amdgcn_permlane32_swap(asu(x), asu(x), false, false); return vmax(asf(s[0]), asf(s[1])); }
__device__ __forceinline__ f32x4 mfma(v4u a, v4u b, f32x4 c) { return __builtin_amdgcn_mfma_f32_16x16x32_bf16(__builtin_bit_cast(v8bf, a), __builtin_bit_cast(v8bf, b), c, 0, 0, 0); }
__device__ __forceinline__ v2u trread(LAS char* p) { return __builtin_bit_cast(v2u, __builtin_amdgcn_ds_read_tr16_b64_v4i16((LAS v4s*)p)); }
__device__ __forceinline__ v4u tr2(LAS char* p, int d) { v2u a = trread(p), b = trread(p + d); return v4u{a.x, a.y, b.x, b.y}; }
template <int B> __device__ __forceinline__ unsigned tagf(float v, unsigned t) { return ((asu(v) + (1u << (B - 1))) & ~((1u << B) - 1)) | t; }
template <int B> __device__ __forceinline__ float untag(unsigned w) { return asf(w & ~((1u << B) - 1)); }
template <int B> __device__ __forceinline__ unsigned badtag(v4u g, unsigned t) { return ((g.x ^ t) | (g.y ^ t) | (g.z ^ t) | (g.w ^ t)) & ((1u << B) - 1); }
#define PIN(x) asm volatile("" : "+v"(x))
#define CNT(i)
#define CNT0(i)
#define ANY(x) (__builtin_amdgcn_ballot_w64((x) != 0) != 0)

#define DMA_S(NTSTR, voff, sbase, ldst) asm volatile("s_mov_b32 m0, %2\n\ts_nop 0\n\tglobal_load_lds_dwordx4 %0, %1" NTSTR :: "v"(voff), "s"(sbase), "s"(ldst) : "memory")
template <int N> __device__ __forceinline__ void vmcnt() { asm volatile("s_waitcnt vmcnt(%0)" ::"i"(N) : "memory"); }
#define LGKM0() asm volatile("s_waitcnt lgkmcnt(0)" ::: "memory")
__device__ __forceinline__ void ld_sc1(v4u& d, unsigned voff, const char* sbase) { asm volatile("global_load_dwordx4 %0, %1, %2 sc1" : "=v"(d) : "v"(voff), "s"(sbase) : "memory"); }
__device__ __forceinline__ void ld1w(v4u& a, unsigned o0, const char* sb) {
  asm volatile("global_load_dwordx4 %0, %1, %2 sc1\n\ts_waitcnt vmcnt(0)" : "=&v"(a) : "v"(o0), "s"(sb) : "memory"); }
__device__ __forceinline__ void ld2w(v4u& a, v4u& b, unsigned o0, unsigned o1, const char* sb) {
  asm volatile("global_load_dwordx4 %0, %2, %4 sc1\n\tglobal_load_dwordx4 %1, %3, %4 sc1\n\ts_waitcnt vmcnt(0)" : "=&v"(a), "=&v"(b) : "v"(o0), "v"(o1), "s"(sb) : "memory"); }
__device__ __forceinline__ void ld4w(v4u& a, v4u& b, v4u& c, v4u& d, unsigned o0, const char* sb) {
  asm volatile("global_load_dwordx4 %0, %4, %5 sc1\n\tglobal_load_dwordx4 %1, %4, %5 offset:1024 sc1\n\tglobal_load_dwordx4 %2, %4, %5 offset:2048 sc1\n\t"
               "global_load_dwordx4 %3, %4, %5 offset:3072 sc1\n\ts_waitcnt vmcnt(0)" : "=&v"(a), "=&v"(b), "=&v"(c), "=&v"(d) : "v"(o0), "s"(sb) : "memory"); }
__device__ __forceinline__ void st_sc1(unsigned voff, v4u d, char* sbase) { asm volatile("global_store_dwordx4 %0, %1, %2 sc1\n\ts_nop 1" ::"v"(voff), "v"(d), "s"(sbase) : "memory"); }

#define ST(i) do { SB(); stamp[i] = (unsigned)__builtin_amdgcn_s_memrealtime(); SB(); } while (0)

__device__ __forceinline__ unsigned now16(unsigned t0) { return ((unsigned)__builtin_amdgcn_s_memrealtime() - t0) * 16u; }
__device__ __forceinline__ int vm_inflight() { unsigned r; asm volatile("s_getreg_b32 %0, hwreg(HW_REG_IB_STS, 0, 4)" : "=s"(r)::"memory"); return (int)r; }
__device__ __forceinline__ float frcp(float x) { return __builtin_amdgcn_rcpf(x); }
__device__ __forceinline__ float fexp(float x) { return __builtin_amdgcn_exp2f(x * 1.4426950408889634f); }
__device__ __forceinline__ void lds_set(LAS char* L, unsigned off, unsigned v) {
  asm volatile("s_mov_b64 exec, 1\n\tds_write_b32 %0, %1\n\ts_mov_b64 exec, -1" ::"v"((unsigned)(uintptr_t)(L + off)), "v"(v) : "memory");
}
template <int MASK, int SLEEP> __device__ __forceinline__ void wait4(LAS char* L, unsigned off, int n) {
#pragma unroll 1
  for (int it = 0; it < SPIN_CAP; ++it) {
    const v4i f = *(volatile LAS v4i*)(L + off);
    int m = 0x7fffffff;
    if (MASK & 1) m = min(m, f.x);
    if (MASK & 2) m = min(m, f.y);
    if (MASK & 4) m = min(m, f.z);
    if (MASK & 8) m = min(m, f.w);
    if (__builtin_amdgcn_readfirstlane(m) >= n) break;
    if (SLEEP) __builtin_amdgcn_s_sleep(SLEEP);
  }
  asm volatile("" ::: "memory");
}

#define TR_ADDR(w_) const int fr = (n >> 2) | ((g & 1) << 2); \
    unsigned ta0 = (8 * g + (n >> 2)) * 256 + ((((2 * (w_)) ^ fr) * 2 + ((n & 3) >> 1)) * 16) + (n & 1) * 8; \
    unsigned ta1 = (8 * g + (n >> 2)) * 256 + ((((2 * (w_) + 1) ^ fr) * 2 + ((n & 3) >> 1)) * 16) + (n & 1) * 8; PIN(ta0); PIN(ta1)

__global__ void __launch_bounds__(512) __attribute__((amdgpu_waves_per_eu(2, 2)))
fused_decode(const u16* __restrict__ x, const u16* __restrict__ kc, const u16* __restrict__ vc, const u16* __restrict__ wq,
             const u16* __restrict__ wo, const u16* __restrict__ wg, const u16* __restrict__ wu, const u16* __restrict__ wd,
             const u16* __restrict__ gw, u16* __restrict__ out, char* __restrict__ ws) {
  extern __shared__ __attribute__((aligned(16))) char smem[];
  LAS char* L = (LAS char*)smem;
  const unsigned lbase = (unsigned)(uintptr_t)L;
  const int c = blockIdx.x, lane = threadIdx.x & 63, wv = __builtin_amdgcn_readfirstlane(threadIdx.x >> 6);
  const int xc = c & 7, j = c >> 3;
  const int kvh = xc, b = j & 1, sp = j >> 1;
  const int rg = 8 * xc + (j >> 2), cg = j & 3;
  const int qp = xc >> 1, qc = 32 * (xc & 1) + j;
  const bool has_f = j < 28;
  const int g = lane >> 4, n = lane & 15;
  unsigned stamp[16];
  for (int i = 0; i < 16; ++i) stamp[i] = 0;
  { u64 ta_; asm volatile("s_memrealtime %0" : "=s"(ta_)); SB();
  asm volatile("" ::"s"(x), "s"(kc), "s"(vc), "s"(wq), "s"(wo), "s"(wg), "s"(wu), "s"(wd), "s"(gw), "s"(out), "s"(ws));
  asm volatile("s_waitcnt lgkmcnt(0)" : "+s"(ta_)); stamp[0] = (unsigned)ta_; }
  const unsigned t0 = (unsigned)__builtin_amdgcn_s_memrealtime();
  stamp[15] = t0;
  if (wv == 4) {
    const char* pc;
    asm volatile("s_getpc_b64 %0" : "=s"(pc));
    asm volatile("s_mov_b32 m0, %2\n\ts_nop 0\n\tglobal_load_lds_dword %0, %1" :: "v"(min(j, CODE_PF) * 512 + (lane & 7) * 64), "s"((const char*)((uintptr_t)pc & ~(uintptr_t)255)), "s"(lbase + L_OB) : "memory");
  }

  if (wv >= 4) {
    const int hw = wv - 4;
    const unsigned fh = L_FH + 4 * hw;
    __builtin_amdgcn_s_setprio(2);
    __builtin_amdgcn_s_barrier();
    wait4<15, 0>(L, L_FS, 1);
    const unsigned t32 = __builtin_amdgcn_readfirstlane(*(LAS unsigned*)(L + L_P + 1024)) + 1, t12 = t32 & 0xFFFu, t8 = t32 & 0xFFu;
    ST(1);
    if (hw < 2) {
      v4u xq[4];
#pragma unroll
      for (int s = 0; s < 4; ++s) xq[s] = *(LAS v4u*)(L + L_X + (n & 1) * 1024 + qp * 256 + s * 64 + g * 16);
      const int col = 64 * qc + 32 * hw + 16 * ((n >> 1) & 1) + 4 * g;
      const unsigned qoff = WS_Q + (((((col >> 9) * 2 + (n & 1)) * 4 + ((col >> 7) & 3)) * 4 + qp) * 128 + (col & 127)) * 4;
      LAS char* a = L + L_WQ + (8 * g + (n >> 2)) * 128 + hw * 64 + (n & 3) * 8;
      wait4<15, 0>(L, L_FS, 5);
      ST(2);
      v4u af[8];
#pragma unroll
      for (int s = 0; s < 4; ++s) { af[2 * s] = tr2(a + s * 4096, 512); af[2 * s + 1] = tr2(a + s * 4096 + 32, 512); }
      SB();
      f32x4 d0 = {0, 0, 0, 0}, d1 = {0, 0, 0, 0};
#pragma unroll
      for (int s = 0; s < 4; ++s) { d0 = mfma(af[2 * s], xq[s], d0); d1 = mfma(af[2 * s + 1], xq[s], d1); }
      v4u gr;
#pragma unroll
      for (int r = 0; r < 4; ++r) gr[r] = tagf<8>((n & 2) ? d1[r] : d0[r], t8);
      if (n < 4) st_sc1(qoff, gr, ws);
      else asm volatile("" ::: "memory");
      lds_set(L, fh, 1);
    } else {
      v4u xg[2];
#pragma unroll
      for (int s = 0; s < 2; ++s) xg[s] = *(LAS v4u*)(L + L_X + (n & 1) * 1024 + xc * 128 + s * 64 + g * 16);
      const unsigned hoff = WS_H + ((((n & 1) * 2 + (hw & 1)) * 8 + xc) * 448 + 16 * j + 4 * ((n >> 1) & 3) + g) * 16;
      LAS char* a = L + L_GU + (hw & 1) * 8192 + (8 * g + (n >> 2)) * 128 + (n & 3) * 8;
      {
        const v4u gwv = *(LAS v4u*)(L + L_P + lane * 16), xv = *(LAS v4u*)(L + L_X + (hw & 1) * 1024 + lane * 16);
        float s = 0.f;
#pragma unroll
        for (int k = 0; k < 4; ++k) {
          const unsigned pr = pk_bf16(asf(xv[k] << 16) * asf(gwv[k] << 16), asf(xv[k] & 0xFFFF0000u) * asf(gwv[k] & 0xFFFF0000u));
          s += asf(pr << 16) + asf(pr & 0xFFFF0000u);
        }
        s += dppf<XOR1>(s); s += dppf<XOR2>(s); s += dppf<ROR4>(s); s += dppf<ROR8>(s); s = sw16(s, s); s = sw32(s, s);
        const float gs = rbf(frcp(1.f + fexp(-rbf(s))));
        if (lane == 0) *(LAS f32x2*)(L + L_GATE + (hw & 1) * 8) = f32x2{gs, rbf(1.f - gs)};
        lds_set(L, L_TAG, t32);
      }
      wait4<15, 1>(L, L_FS, 9);
      ST(2);
      v4u af[8];
#pragma unroll
      for (int s = 0; s < 2; ++s)
#pragma unroll
        for (int t = 0; t < 4; ++t) af[4 * s + t] = tr2(a + s * 4096 + t * 32, 512);
      SB();
      f32x4 d[4];
#pragma unroll
      for (int t = 0; t < 4; ++t) d[t] = mfma(af[4 + t], xg[1], mfma(af[t], xg[0], f32x4{0, 0, 0, 0}));
      v4u gr;
#pragma unroll
      for (int r = 0; r < 4; ++r) { const float lo = (n & 2) ? d[1][r] : d[0][r], hi = (n & 2) ? d[3][r] : d[2][r]; gr[r] = tagf<12>((n & 4) ? hi : lo, t12); }
      if (n < 8 && has_f) st_sc1(hoff, gr, ws);
      else asm volatile("" ::: "memory");
      lds_set(L, fh, 1);
    }
    ST(3);
    {
      const int e = lane & 7;
      const unsigned qo = WS_Q + ((kvh * 2 + b) * 4 + hw) * 2048 + lane * 16;
      const unsigned ho = WS_H + (((hw & 1) * 16 + (lane >> 3)) * 448 + 7 * rg + (e < 7 ? e : 6)) * 16;
#pragma unroll 1
      for (int it = 0; it < SPIN_CAP && now16(t0) < TQ; ++it) __builtin_amdgcn_s_sleep(1);
      v4u q0, q1;
      CNT0(14); CNT0(15);
#pragma unroll 1
      for (int it = 0; it < SPIN_CAP; ++it) { ld2w(q0, q1, qo, qo + 1024, ws); if (!ANY(badtag<8>(q0, t8) | badtag<8>(q1, t8))) break; CNT(14); }
      float v[4];
#pragma unroll
      for (int r = 0; r < 4; ++r) { v[r] = untag<8>(q0[r]) + untag<8>(q1[r]); v[r] = sw32(v[r], v[r]); }
      if (lane < 32) *(LAS v2u*)(L + L_Q + hw * QSTR + lane * 8) = v2u{pk_bf16(v[0], v[1]), pk_bf16(v[2], v[3])};
      LGKM0(); lds_set(L, fh, 2);
      ST(4);
      if (hw >= 2) {
        v4u h0, h1;
#pragma unroll 1
        for (int it = 0; it < SPIN_CAP; ++it) { ld2w(h0, h1, ho, ho + 8 * 448 * 16, ws); if (!ANY(badtag<12>(h0, t12) | badtag<12>(h1, t12))) break; CNT(15); }
        float hv[4];
#pragma unroll
        for (int r = 0; r < 4; ++r) {
          float gg = untag<12>(h0[r]), uu = untag<12>(h1[r]);
          gg += dppf<ROR8>(gg); uu += dppf<ROR8>(uu); gg = sw16(gg, gg); uu = sw16(uu, uu); gg = rbf(sw32(gg, gg)); uu = rbf(sw32(uu, uu));
          hv[r] = rbf(gg * frcp(1.f + fexp(-gg))) * uu;
        }
        if (lane < 8) *(LAS v2u*)(L + L_HB + (hw & 1) * 64 + e * 8) = v2u{e < 7 ? pk_bf16(hv[0], hv[1]) : 0u, e < 7 ? pk_bf16(hv[2], hv[3]) : 0u};
      }
    }
    ST(5);
    wait4<15, 1>(L, L_FH, 2);
    v4u qf[4], kf[16];
#pragma unroll
    for (int s = 0; s < 4; ++s) qf[s] = *(LAS v4u*)(L + L_Q + (n & 3) * QSTR + s * 64 + g * 16);
    wait4<15, 0>(L, L_FS, 13 + 4 * hw);
    ST(6);
#pragma unroll
    for (int s = 0; s < 4; ++s)
#pragma unroll
      for (int t = 0; t < 4; ++t) kf[s * 4 + t] = *(LAS v4u*)(L + L_K + (4 * hw + t) * 4096 + n * 256 + (((4 * s + g) ^ n) * 16));
    SB();
    float sc[4];
    {
      f32x4 d[4];
#pragma unroll
      for (int t = 0; t < 4; ++t) d[t] = f32x4{0, 0, 0, 0};
#pragma unroll
      for (int s = 0; s < 4; ++s)
#pragma unroll
        for (int t = 0; t < 4; ++t) d[t] = mfma(kf[s * 4 + t], qf[s], d[t]);
      constexpr float SC = 0.08838834764831845f;
      float v[4];
#pragma unroll
      for (int r = 0; r < 4; ++r) { const float lo = (n & 4) ? d[1][r] : d[0][r], hi = (n & 4) ? d[3][r] : d[2][r]; v[r] = (n & 8) ? hi : lo; }
      const unsigned a = pk_bf16(v[0], v[1]), bq = pk_bf16(v[2], v[3]);
      const unsigned a2 = pk_bf16(asf(a << 16) * SC, asf(a & 0xFFFF0000u) * SC), b2 = pk_bf16(asf(bq << 16) * SC, asf(bq & 0xFFFF0000u) * SC);
      sc[0] = asf(a2 << 16); sc[1] = asf(a2 & 0xFFFF0000u); sc[2] = asf(b2 << 16); sc[3] = asf(b2 & 0xFFFF0000u);
    }
    float m = vmax(vmax(sc[0], sc[1]), vmax(sc[2], sc[3]));
    m = vmax(m, dppf<ROR4>(m)); m = vmax(m, dppf<ROR8>(m)); m = x16max(m); m = x32max(m);
    if (lane < 4) *(LAS float*)(L + L_MX + lane * 16 + hw * 4) = m;
    LGKM0(); lds_set(L, fh, 3);
    wait4<15, 0>(L, L_FH, 3);
    { const f32x4 mm = *(LAS f32x4*)(L + L_MX + (n & 3) * 16); m = vmax(vmax(mm[0], mm[1]), vmax(mm[2], mm[3])); }
    {
      float p[4];
#pragma unroll
      for (int r = 0; r < 4; ++r) p[r] = __builtin_amdgcn_exp2f((sc[r] - m) * 1.4426950408889634f);
      *(LAS v2u*)(L + L_P + (n & 3) * PSTR + (64 * hw + 16 * (n >> 2) + 4 * g) * 2) = v2u{pk_bf16(p[0], p[1]), pk_bf16(p[2], p[3])};
      float l = (p[0] + p[1]) + (p[2] + p[3]);
      l += dppf<ROR4>(l); l += dppf<ROR8>(l); l = sw16(l, l); l = sw32(l, l);
      if (lane < 4) *(LAS float*)(L + L_SUM + lane * 16 + hw * 4) = l;
    }
    LGKM0(); lds_set(L, fh, 4);
    ST(7);
    TR_ADDR(hw);
    unsigned ooff = WS_O + (((2 * (4 * kvh + (n & 3)) + (hw >> 1)) * 2 + b) * 16 + sp) * 256 + (hw & 1) * 128 + ((n >> 2) & 1) * 64 + g * 16;
    PIN(ooff);
    wait4<15, 0>(L, L_FH, 4);
    v4u pf[8];
#pragma unroll
    for (int u = 0; u < 8; ++u) pf[u] = *(LAS v4u*)(L + L_P + (n & 3) * PSTR + u * 64 + g * 16);
    if (hw == 2) {
      const f32x4 ls = *(LAS f32x4*)(L + L_SUM + (n & 3) * 16);
      const v4u gr = {(asu(m) & 0xFFFFF000u) | t12, tagf<12>((ls[0] + ls[1]) + (ls[2] + ls[3]), t12), t12, t12};
      if (lane < 4) st_sc1(WS_ST + (((kvh * 4 + lane) * 2 + b) * 16 + sp) * 16, gr, ws);
    }
    f32x4 o0 = {0, 0, 0, 0}, o1 = {0, 0, 0, 0};
#define PVN(u0, nu) do { v4u va[nu], vb[nu]; _Pragma("unroll") for (int u = 0; u < nu; ++u) { va[u] = tr2(L + L_V + ta0 + (u0 + u) * 8192, 1024); vb[u] = tr2(L + L_V + ta1 + (u0 + u) * 8192, 1024); } SB(); \
      _Pragma("unroll") for (int u = 0; u < nu; ++u) { o0 = mfma(va[u], pf[u0 + u], o0); o1 = mfma(vb[u], pf[u0 + u], o1); } } while (0)
    wait4<15, 1>(L, L_FS, 33);
    PVN(0, 4); PIN(o0); PIN(o1);
    ST(8);
    wait4<15, 0>(L, L_FS, 39);
    PVN(4, 3); PIN(o0); PIN(o1);
    wait4<15, 0>(L, L_FS, 41);
    ST(9);
    PVN(7, 1);
    {
      v4u gr;
#pragma unroll
      for (int r = 0; r < 4; ++r) gr[r] = tagf<12>((n & 4) ? o1[r] : o0[r], t12);
      if (n < 8) st_sc1(ooff, gr, ws);
      else asm volatile("" ::: "memory");
    }
    ST(10);
    if (hw < 2) {
      const unsigned so = WS_ST + (((rg >> 1) * 2 + hw) * 16 + 4 * (n & 3) + g) * 16;
      const unsigned oo = WS_O + (rg * 2 + hw) * 4096 + lane * 16;
      v4u st;
#pragma unroll 1
      for (int it = 0; it < SPIN_CAP; ++it) { ld1w(st, so, ws); if (!ANY(badtag<12>(st, t12))) break; CNT(14); }
      const float ms = untag<12>(st.x), ls = untag<12>(st.y);
      float M = ms; M = vmax(M, dppf<XOR1>(M)); M = vmax(M, dppf<XOR2>(M)); M = x16max(M); M = x32max(M);
      const float ex = __builtin_amdgcn_exp2f((ms - M) * 1.4426950408889634f);
      float Ls = ex * ls; Ls += dppf<XOR1>(Ls); Ls += dppf<XOR2>(Ls); Ls = sw16(Ls, Ls); Ls = sw32(Ls, Ls);
      const float wv_ = ex * frcp(Ls);
      float wt0 = dppf<0x00>(wv_), wt1 = dppf<0x55>(wv_), wt2 = dppf<0xAA>(wv_), wt3 = dppf<0xFF>(wv_);
      PIN(wt0); PIN(wt1); PIN(wt2); PIN(wt3);
      ST(11);
      v4u a0, a1, a2, a3;
#pragma unroll 1
      for (int it = 0; it < SPIN_CAP; ++it) {
        ld4w(a0, a1, a2, a3, oo, ws);
        if (!ANY(badtag<12>(a0, t12) | badtag<12>(a1, t12) | badtag<12>(a2, t12) | badtag<12>(a3, t12))) break;
        CNT(15);
        if (O_SLEEP) __builtin_amdgcn_s_sleep(O_SLEEP);
      }
      ST(12);
      float v[4];
#pragma unroll
      for (int r = 0; r < 4; ++r) v[r] = fmaf(wt0, untag<12>(a0[r]), wt1 * untag<12>(a1[r])) + fmaf(wt2, untag<12>(a2[r]), wt3 * untag<12>(a3[r]));
      const float t = sw16(sw32(v[0], v[1]), sw32(v[2], v[3]));
      *(LAS u16*)(L + L_OB + hw * 128 + (4 * n + (g & 1) * 2 + (g >> 1)) * 2) = (u16)(pk_bf16(t, t) & 0xFFFFu);
      LGKM0();
      ST(13);
      __builtin_amdgcn_s_barrier();
    } else if (hw == 2) {
      __builtin_amdgcn_s_barrier();
    } else {
      unsigned dummy;
      asm volatile("s_nop 4\n\tglobal_load_dword %0, %1, %2\n\ts_waitcnt vmcnt(0)" : "=&v"(dummy) : "v"(0), "s"(out) : "memory");
      unsigned yo = WS_Y + (((xc >> 1) * 64 + lane) * 2 + (xc & 1)) * 512 + j * 16;
      u16* op = out + (xc & 1) * 512 + (xc >> 1) * 128 + 4 * j + (g & 1) * 2 + (g >> 1);
      v4u y;
      PIN(yo); PIN(op);
      __builtin_amdgcn_s_barrier();
      for (int z = 0; z < Y_DELAY; ++z) __builtin_amdgcn_s_sleep(1);
#pragma unroll 1
      for (int it = 0; it < SPIN_CAP; ++it) {
        ld1w(y, yo, ws);
        if (!ANY(badtag<12>(y, t12))) break;
        CNT(15);
      }
      ST(12);
      float t = sw16(sw32(untag<12>(y.x), untag<12>(y.y)), sw32(untag<12>(y.z), untag<12>(y.w)));
      t += dppf<XOR1>(t); t += dppf<XOR2>(t); t += dppf<ROR4>(t); t += dppf<ROR8>(t);
      if (n == 0) *op = (u16)(pk_bf16(t, t) & 0xFFFFu);
      if (c == 0 && lane == 0) __hip_atomic_store((GAS unsigned*)ws, t32, __ATOMIC_RELAXED, __HIP_MEMORY_SCOPE_AGENT);
      ST(13);
    }
  } else {
    const int w = wv;
    const unsigned fs = L_FS + 4 * w;
    int issued = 9, landed = 0;
    unsigned due = TK0 + w * (TAU / 4);
#define PACE() do { _Pragma("unroll 1") for (int it_ = 0; it_ < SPIN_CAP; ++it_) { const int vm_ = vm_inflight(); if (issued - vm_ != landed) { landed = issued - vm_; lds_set(L, fs, landed); } \
        if (now16(t0) >= due && vm_ < WIN) break; } due += TAU; ++issued; } while (0)
    {
      const char* sb = w == 0 ? (const char*)x : w == 1 ? (const char*)x + 1024 : w == 2 ? (const char*)gw : (const char*)ws;
      DMA_S("", w == 3 ? 0 : lane * 16, sb, lbase + (w < 2 ? L_X : L_P - 2048) + w * 1024);
    }
    {
      const char* base = (const char*)wq + (size_t)(128 * qp + 8 * w) * 8192 + qc * 128;
      const unsigned voff = (lane >> 3) * 8192 + (lane & 7) * 16;
#pragma unroll
      for (int i = 0; i < 4; ++i) DMA_S(NTW, voff, base + (size_t)i * 32 * 8192, lbase + L_WQ + (4 * i + w) * 1024);
    }
    if (w == 0 && lane < 8) *(volatile LAS int*)(L + L_FS + 4 * lane) = -64;
    {
      const unsigned voff = has_f ? (lane >> 3) * 3584 + (lane & 7) * 16 : 0;
      const size_t off = has_f ? (size_t)(64 * xc + 8 * w) * 3584 + j * 128 : 0;
      const unsigned st = has_f ? 32 * 3584 : 0;
#pragma unroll
      for (int i = 0; i < 4; ++i) { DMA_S(NTW, voff, (const char*)(i < 2 ? wg : wu) + off + (i & 1) * st, lbase + L_GU + (4 * i + w) * 1024); }
    }
    LGKM0();
    __builtin_amdgcn_s_barrier();
    const size_t kvoff = ((size_t)(b * 8 + kvh) * 16384 + 12288 + sp * 256) * 256 + w * 1024;
    {
      const char* kb = (const char*)kc + kvoff;
      const unsigned voff = g * 256 + ((n ^ (4 * w + g)) * 16);
#pragma unroll 1
      for (int i = 0; i < 16; ++i) { PACE(); DMA_S(NTS, voff, kb + (((i + j) & 15) << 12), lbase + L_K + i * 4096 + w * 1024); }
    }
    ST(1);
    {
      const unsigned voff = g * 256 + ((n ^ (2 * (g | ((w >> 1) << 2)))) * 16);
      const char* vb = (const char*)vc + kvoff;
#pragma unroll 1
      for (int i = 0; i < 8; ++i) { PACE(); DMA_S(NTS, voff, vb + (((i + j) & 15) << 12), lbase + L_V + i * 4096 + w * 1024); }
      wait4<15, 0>(L, L_FH, 1);
#pragma unroll 1
      for (int i = 8; i < 16; ++i) { PACE(); DMA_S(NTS, voff, vb + (((i + j) & 15) << 12), lbase + L_V + i * 4096 + w * 1024); }
    }
    ST(2);
    {
      const unsigned vo = g * 1024 + ((n ^ (2 * (g | ((w >> 1) << 2)))) * 16);
      const char* ob = (const char*)wo + (size_t)(64 * rg + 4 * w) * 1024 + cg * 256;
      const char* db = (const char*)wd + (size_t)(28 * rg) * 1024 + cg * 256;
#pragma unroll 1
      for (int i = 0; i < 6; ++i) {
        const char* src = i < 4 ? ob + i * 16384 : db + (i == 4 ? 4 * w : w < 3 ? 16 + 4 * w : 0) * 1024;
        PACE();
        DMA_S(NTW, vo, src, lbase + L_WO + i * 4096 + w * 1024);
      }
    }
    ST(3);
#pragma unroll 1
    for (int it = 0; it < SPIN_CAP; ++it) { const int vm = vm_inflight(); if (issued - vm != landed) { landed = issued - vm; lds_set(L, fs, landed); } if (landed >= 41) break; }
    ST(4);
    const unsigned t12 = __builtin_amdgcn_readfirstlane(*(volatile LAS unsigned*)(L + L_TAG)) & 0xFFFu;
    TR_ADDR(w);
    unsigned yoff = WS_Y + ((cg * 64 + rg) * 2 + (n & 1)) * 512 + w * 128 + ((n >> 1) & 1) * 64 + g * 16;
    PIN(yoff);
    vmcnt<0>(); lds_set(L, fs, 47);
    ST(5);
    wait4<12, 0>(L, L_FH, 3);
    wait4<15, 0>(L, L_FS, 47);
    v4u wf[4];
    wf[0] = tr2(L + L_WO + ta0, 1024); wf[1] = tr2(L + L_WO + ta1, 1024); wf[2] = tr2(L + L_WO + ta0 + 8192, 1024); wf[3] = tr2(L + L_WO + ta1 + 8192, 1024);
    const f32x2 gt = *(LAS f32x2*)(L + L_GATE + (n & 1) * 8);
    f32x4 fy;
    {
      const v4u df0 = tr2(L + L_WD + ta0, 1024), df1 = tr2(L + L_WD + ta1, 1024);
      const v4u hb = *(LAS v4u*)(L + L_HB + (n & 1) * 64 + g * 16);
      const f32x4 f0 = mfma(df0, hb, f32x4{0, 0, 0, 0}), f1 = mfma(df1, hb, f32x4{0, 0, 0, 0});
#pragma unroll
      for (int r = 0; r < 4; ++r) fy[r] = gt[1] * ((n & 2) ? f1[r] : f0[r]);
    }
    PIN(fy); PIN(wf[0]); PIN(wf[1]); PIN(wf[2]); PIN(wf[3]);
    ST(6);
    __builtin_amdgcn_s_barrier();
    asm volatile("" ::: "memory");
    ST(10);
    {
      const v4u b0 = *(LAS v4u*)(L + L_OB + (n & 1) * 128 + g * 16), b1 = *(LAS v4u*)(L + L_OB + (n & 1) * 128 + 64 + g * 16);
      f32x4 y0 = mfma(wf[0], b0, f32x4{0, 0, 0, 0}), y1 = mfma(wf[1], b0, f32x4{0, 0, 0, 0});
      y0 = mfma(wf[2], b1, y0); y1 = mfma(wf[3], b1, y1);
      v4u gr;
#pragma unroll
      for (int r = 0; r < 4; ++r) gr[r] = tagf<12>(fmaf(gt[0], (n & 2) ? y1[r] : y0[r], fy[r]), t12);
      if (n < 4) st_sc1(yoff, gr, ws);
      else asm volatile("" ::: "memory");
    }
    ST(11);
  }
  if (c == PWG && wv == PWAVE) {
    const unsigned tgt = 4000u + (PROBE >= 0 ? (unsigned)(stamp[PROBE >= 0 ? PROBE : 0] - stamp[0]) : 0u);
    for (int it = 0; it < 1000000; ++it) { if ((unsigned)__builtin_amdgcn_s_memrealtime() - stamp[0] >= tgt) break; }
  }
}

extern "C" void kernel_launch(void* const* d_in, const int* in_sizes, int n_in, void* d_out, int out_size, void* d_ws, size_t ws_size, hipStream_t stream) {
  (void)hipFuncSetAttribute((const void*)fused_decode, hipFuncAttributeMaxDynamicSharedMemorySize, L_END);
  fused_decode<<<256, 512, L_END, stream>>>((const u16*)d_in[0], (const u16*)d_in[1], (const u16*)d_in[2], (const u16*)d_in[3], (const u16*)d_in[4],
                                            (const u16*)d_in[6], (const u16*)d_in[7], (const u16*)d_in[8], (const u16*)d_in[9], (u16*)d_out, (char*)d_ws);
}
```

```cpp
#define TAU 240
#define TAUL 340
#define NL 5
#define TAU2 256
#define TO 2500
#define TY 1800
#include <hip/hip_runtime.h>
#include <hip/hip_bf16.h>
#include <stdint.h>

#define STAMP 0
#define WIN 12
#define NTS " nt"
#define NTW " nt"
#define Q_DELAY 16
#define CODE_PF 25
#define ABL 0
#define TK0 440
#define TT 2900
#define TQ 4300
#define TH 5600
#define GAP 0
#define HOLD 0
#define PF 0
#define DITH 0
#define TVE (TK0 + (35 - NL) * TAU + NL * TAUL)
#define TBIAS 0
#define O_SLEEP 1
#define Y_DELAY 4

typedef unsigned short u16;
typedef unsigned long long u64;
typedef unsigned v4u __attribute__((ext_vector_type(4)));
typedef int v4i __attribute__((ext_vector_type(4)));
typedef unsigned v2u __attribute__((ext_vector_type(2)));
typedef short v4s __attribute__((ext_vector_type(4)));
typedef float f32x4 __attribute__((ext_vector_type(4)));
typedef float f32x2 __attribute__((ext_vector_type(2)));
typedef __bf16 v8bf __attribute__((ext_vector_type(8)));
typedef __bf16 v2bf __attribute__((ext_vector_type(2)));
#define GAS __attribute__((address_space(1)))
#define LAS __attribute__((address_space(3)))
#define SB() __builtin_amdgcn_sched_barrier(0)

constexpr unsigned WS_T = 1024;
constexpr unsigned WS_Q = 4096;
constexpr unsigned WS_H = WS_Q + 131072;
constexpr unsigned WS_ST = WS_H + 229376;
constexpr unsigned WS_O = WS_ST + 16384;
constexpr unsigned WS_Y = WS_O + 524288;
constexpr unsigned WS_STAMP = 4u << 20;
constexpr unsigned L_K = 0, L_V = 65536, L_WQ = L_V + 8 * 4096, L_WO = 131072, L_GU = L_WO, L_WD = L_WO + 16384, L_X = L_WD + 8192,
                   L_Q = L_X + 2048, QSTR = 320, L_P = L_Q + 4 * QSTR, PSTR = 576, L_OB = L_P + 4 * PSTR, L_HB = L_OB + 256, L_MX = L_HB + 128,
                   L_SUM = L_MX + 64, L_GATE = L_SUM + 64, L_FS = L_GATE + 16, L_FH = L_FS + 16, L_DT = L_FH + 16, L_TAG = L_DT + 4, L_END = L_DT + 16;
static_assert(L_END <= 163840, "LDS");
constexpr int SPIN_CAP = 1 << 17;

__device__ __forceinline__ float asf(unsigned u) { return __builtin_bit_cast(float, u); }
__device__ __forceinline__ unsigned asu(float f) { return __builtin_bit_cast(unsigned, f); }
__device__ __forceinline__ unsigned pk_bf16(float lo, float hi) { f32x2 x = {lo, hi}; return __builtin_bit_cast(unsigned, __builtin_convertvector(x, v2bf)); }
__device__ __forceinline__ float rbf(float f) { return asf(pk_bf16(f, f) << 16); }
template <int CTRL> __device__ __forceinline__ float dppf(float x) { return asf(__builtin_amdgcn_mov_dpp(asu(x), CTRL, 0xf, 0xf, true)); }
constexpr int XOR1 = 0xB1, XOR2 = 0x4E, ROR4 = 0x124, ROR8 = 0x128;
__device__ __forceinline__ float sw16(float a, float b) { auto s = __builtin_amdgcn_permlane16_swap(asu(a), asu(b), false, false); return asf(s[0]) + asf(s[1]); }
__device__ __forceinline__ float sw32(float a, float b) { auto s = __builtin_amdgcn_permlane32_swap(asu(a), asu(b), false, false); return asf(s[0]) + asf(s[1]); }
__device__ __forceinline__ float vmax(float a, float b) { float r; asm("v_max_f32 %0, %1, %2" : "=v"(r) : "v"(a), "v"(b)); return r; }
__device__ __forceinline__ float x16max(float x) { auto s = __builtin_amdgcn_permlane16_swap(asu(x), asu(x), false, false); return vmax(asf(s[0]), asf(s[1])); }
__device__ __forceinline__ float x32max(float x) { auto s = __builtin_amdgcn_permlane32_swap(asu(x), asu(x), false, false); return vmax(asf(s[0]), asf(s[1])); }
__device__ __forceinline__ f32x4 mfma(v4u a, v4u b, f32x4 c) { return __builtin_amdgcn_mfma_f32_16x16x32_bf16(__builtin_bit_cast(v8bf, a), __builtin_bit_cast(v8bf, b), c, 0, 0, 0); }
__device__ __forceinline__ v2u trread(LAS char* p) { return __builtin_bit_cast(v2u, __builtin_amdgcn_ds_read_tr16_b64_v4i16((LAS v4s*)p)); }
__device__ __forceinline__ v4u tr2(LAS char* p, int d) { v2u a = trread(p), b = trread(p + d); return v4u{a.x, a.y, b.x, b.y}; }
template <int B> __device__ __forceinline__ unsigned tagf(float v, unsigned t) { return ((asu(v) + (1u << (B - 1))) & ~((1u << B) - 1)) | t; }
template <int B> __device__ __forceinline__ float untag(unsigned w) { return asf(w & ~((1u << B) - 1)); }
template <int B> __device__ __forceinline__ unsigned badtag(v4u g, unsigned t) {
  return (__builtin_amdgcn_bitop3_b32(g.x, g.y, t, 0x7E) | __builtin_amdgcn_bitop3_b32(g.z, g.w, t, 0x7E)) & ((1u << B) - 1);
}
#define PIN(x) asm volatile("" : "+v"(x))
#define CNT(i)
#define CNT0(i)
#define ANY(x) (__builtin_amdgcn_ballot_w64((x) != 0) != 0)

#define DMA_S(NTSTR, voff, sbase, ldst) asm volatile("s_mov_b32 m0, %2\n\ts_nop 0\n\tglobal_load_lds_dwordx4 %0, %1" NTSTR :: "v"(voff), "s"(sbase), "s"(ldst) : "memory")
template <int N> __device__ __forceinline__ void vmcnt() { asm volatile("s_waitcnt vmcnt(%0)" ::"i"(N) : "memory"); }
#define LGKM0() asm volatile("s_waitcnt lgkmcnt(0)" ::: "memory")
__device__ __forceinline__ void ld_sc1(v4u& d, unsigned voff, const char* sbase) { asm volatile("global_load_dwordx4 %0, %1, %2 sc1" : "=v"(d) : "v"(voff), "s"(sbase) : "memory"); }
__device__ __forceinline__ void ld1w(v4u& a, unsigned o0, const char* sb) {
  asm volatile("global_load_dwordx4 %0, %1, %2 sc1\n\ts_waitcnt vmcnt(0)" : "=&v"(a) : "v"(o0), "s"(sb) : "memory"); }
__device__ __forceinline__ void ld2w(v4u& a, v4u& b, unsigned o0, unsigned o1, const char* sb) {
  asm volatile("global_load_dwordx4 %0, %2, %4 sc1\n\tglobal_load_dwordx4 %1, %3, %4 sc1\n\ts_waitcnt vmcnt(0)" : "=&v"(a), "=&v"(b) : "v"(o0), "v"(o1), "s"(sb) : "memory"); }
__device__ __forceinline__ void ld4w(v4u& a, v4u& b, v4u& c, v4u& d, unsigned o0, const char* sb) {
  asm volatile("global_load_dwordx4 %0, %4, %5 sc1\n\tglobal_load_dwordx4 %1, %4, %5 offset:1024 sc1\n\tglobal_load_dwordx4 %2, %4, %5 offset:2048 sc1\n\t"
               "global_load_dwordx4 %3, %4, %5 offset:3072 sc1\n\ts_waitcnt vmcnt(0)" : "=&v"(a), "=&v"(b), "=&v"(c), "=&v"(d) : "v"(o0), "s"(sb) : "memory"); }
__device__ __forceinline__ void st_sc1(unsigned voff, v4u d, char* sbase) { asm volatile("global_store_dwordx4 %0, %1, %2 sc1\n\ts_nop 1" ::"v"(voff), "v"(d), "s"(sbase) : "memory"); }

#define ST(i)

__device__ __forceinline__ unsigned now16(unsigned t0) { return ((unsigned)__builtin_amdgcn_s_memrealtime() - t0) * 16u; }
__device__ __forceinline__ int vm_inflight() { unsigned r; asm volatile("s_getreg_b32 %0, hwreg(HW_REG_IB_STS, 0, 4)" : "=s"(r)::"memory"); return (int)r; }
__device__ __forceinline__ float frcp(float x) { return __builtin_amdgcn_rcpf(x); }
__device__ __forceinline__ float fexp(float x) { return __builtin_amdgcn_exp2f(x * 1.4426950408889634f); }
__device__ __forceinline__ int cnow16(LAS char* L, unsigned t0) {
  const int d = *(volatile LAS int*)(L + L_DT);
  return (int)now16(t0) - __builtin_amdgcn_readfirstlane(d);
}
template <int SLEEP> __device__ __forceinline__ void wait_until(LAS char* L, unsigned t0, int t) {
#pragma unroll 1
  for (int it = 0; it < SPIN_CAP && cnow16(L, t0) < t; ++it) { if (SLEEP) __builtin_amdgcn_s_sleep(SLEEP); }
}
__device__ __forceinline__ void lds_set(LAS char* L, unsigned off, unsigned v) {
  asm volatile("s_mov_b64 exec, 1\n\tds_write_b32 %0, %1\n\ts_mov_b64 exec, -1" ::"v"((unsigned)(uintptr_t)(L + off)), "v"(v) : "memory");
}
template <int MASK, int SLEEP> __device__ __forceinline__ void wait4(LAS char* L, unsigned off, int n) {
#pragma unroll 1
  for (int it = 0; it < SPIN_CAP; ++it) {
    const v4i f = *(volatile LAS v4i*)(L + off);
    int m = 0x7fffffff;
    if (MASK & 1) m = min(m, f.x);
    if (MASK & 2) m = min(m, f.y);
    if (MASK & 4) m = min(m, f.z);
    if (MASK & 8) m = min(m, f.w);
    if (__builtin_amdgcn_readfirstlane(m) >= n) break;
    if (SLEEP) __builtin_amdgcn_s_sleep(SLEEP);
  }
  asm volatile("" ::: "memory");
}

#define TR_ADDR(w_) const int fr = (n >> 2) | ((g & 1) << 2); \
    unsigned ta0 = (8 * g + (n >> 2)) * 256 + ((((2 * (w_)) ^ fr) * 2 + ((n & 3) >> 1)) * 16) + (n & 1) * 8; \
    unsigned ta1 = (8 * g + (n >> 2)) * 256 + ((((2 * (w_) + 1) ^ fr) * 2 + ((n & 3) >> 1)) * 16) + (n & 1) * 8; PIN(ta0); PIN(ta1)

__global__ void __launch_bounds__(512) __attribute__((amdgpu_waves_per_eu(2, 2)))
fused_decode(const u16* __restrict__ x, const u16* __restrict__ gw, char* __restrict__ ws, const u16* __restrict__ wq, const u16* __restrict__ wg,
             const u16* __restrict__ wu, const u16* __restrict__ kc, const u16* __restrict__ vc, const u16* __restrict__ wo, const u16* __restrict__ wd,
             u16* __restrict__ out) {
  extern __shared__ __attribute__((aligned(16))) char smem[];
  LAS char* L = (LAS char*)smem;
  const unsigned lbase = (unsigned)(uintptr_t)L;
  const int c = blockIdx.x, lane = threadIdx.x & 63, wv = __builtin_amdgcn_readfirstlane(threadIdx.x >> 6);
  const int xc = c & 7, j = c >> 3;
  const int kvh = xc, b = j & 1, sp = j >> 1;
  const int rg = 8 * xc + (j >> 2), cg = j & 3;
  const int qp = xc >> 1, qc = 32 * (xc & 1) + j;
  const bool has_f = j < 28;
  const int g = lane >> 4, n = lane & 15;
  asm volatile("" ::"s"(x), "s"(kc), "s"(vc), "s"(wq), "s"(wo), "s"(wg), "s"(wu), "s"(wd), "s"(gw), "s"(out), "s"(ws));
  const unsigned t0 = (unsigned)__builtin_amdgcn_s_memrealtime();
  ST(0);
  if (wv == 4) {
    const char* pc;
    asm volatile("s_getpc_b64 %0" : "=s"(pc));
    asm volatile("s_mov_b32 m0, %2\n\ts_nop 0\n\tglobal_load_lds_dword %0, %1" :: "v"(min(j, CODE_PF) * 512 + (lane & 7) * 64), "s"((const char*)((uintptr_t)pc & ~(uintptr_t)255)), "s"(lbase + L_OB) : "memory");
  }

  if (wv >= 4) {
    const int hw = wv - 4;
    const unsigned fh = L_FH + 4 * hw;
    __builtin_amdgcn_s_setprio(2);
    __builtin_amdgcn_s_barrier();
    wait4<15, 0>(L, L_FS, 1);
    const unsigned t32 = __builtin_amdgcn_readfirstlane(*(LAS unsigned*)(L + L_P + 1024)) + 1, t12 = t32 & 0xFFFu, t8 = t32 & 0xFFu;
    ST(1);
    if (hw == 2 && c < 8) { if (lane == 0) st_sc1(WS_T + c * 128, v4u{t0, t32, t0, t32}, ws); }
    if (hw < 2) {
      v4u xq[4];
#pragma unroll
      for (int s = 0; s < 4; ++s) xq[s] = *(LAS v4u*)(L + L_X + (n & 1) * 1024 + qp * 256 + s * 64 + g * 16);
      const int col = 64 * qc + 32 * hw + 16 * ((n >> 1) & 1) + 4 * g;
      const unsigned qoff = WS_Q + (((((col >> 9) * 2 + (n & 1)) * 4 + ((col >> 7) & 3)) * 4 + qp) * 128 + (col & 127)) * 4;
      LAS char* a = L + L_WQ + (8 * g + (n >> 2)) * 128 + hw * 64 + (n & 3) * 8;
      wait4<15, 0>(L, L_FS, 5);
      ST(2);
      v4u af[8];
#pragma unroll
      for (int s = 0; s < 4; ++s) { af[2 * s] = tr2(a + s * 4096, 512); af[2 * s + 1] = tr2(a + s * 4096 + 32, 512); }
      SB();
      f32x4 d0 = {0, 0, 0, 0}, d1 = {0, 0, 0, 0};
#pragma unroll
      for (int s = 0; s < 4; ++s) { d0 = mfma(af[2 * s], xq[s], d0); d1 = mfma(af[2 * s + 1], xq[s], d1); }
      v4u gr;
#pragma unroll
      for (int r = 0; r < 4; ++r) gr[r] = tagf<8>((n & 2) ? d1[r] : d0[r], t8);
      if (n < 4) st_sc1(qoff, gr, ws);
      else asm volatile("" ::: "memory");
      lds_set(L, fh, 1);
    } else {
      {
        const v4u gwv = *(LAS v4u*)(L + L_P + lane * 16), xv = *(LAS v4u*)(L + L_X + (hw & 1) * 1024 + lane * 16);
        float s = 0.f;
#pragma unroll
        for (int k = 0; k < 4; ++k) {
          const unsigned pr = pk_bf16(asf(xv[k] << 16) * asf(gwv[k] << 16), asf(xv[k] & 0xFFFF0000u) * asf(gwv[k] & 0xFFFF0000u));
          s += asf(pr << 16) + asf(pr & 0xFFFF0000u);
        }
        s += dppf<XOR1>(s); s += dppf<XOR2>(s); s += dppf<ROR4>(s); s += dppf<ROR8>(s); s = sw16(s, s); s = sw32(s, s);
        const float gs = rbf(frcp(1.f + fexp(-rbf(s))));
        if (lane == 0) *(LAS f32x2*)(L + L_GATE + (hw & 1) * 8) = f32x2{gs, rbf(1.f - gs)};
        lds_set(L, L_TAG, t32);
      }
    }
    if (hw >= 2) {
      v4u xg[2];
#pragma unroll
      for (int s = 0; s < 2; ++s) xg[s] = *(LAS v4u*)(L + L_X + (n & 1) * 1024 + xc * 128 + s * 64 + g * 16);
      const unsigned hoff = WS_H + ((((n & 1) * 2 + (hw & 1)) * 8 + xc) * 448 + 16 * j + 4 * ((n >> 1) & 3) + g) * 16;
      LAS char* a = L + L_GU + (hw & 1) * 8192 + (8 * g + (n >> 2)) * 128 + (n & 3) * 8;
      wait4<15, 0>(L, L_FS, 9);
      ST(2);
      v4u af[8];
#pragma unroll
      for (int s = 0; s < 2; ++s)
#pragma unroll
        for (int t = 0; t < 4; ++t) af[4 * s + t] = tr2(a + s * 4096 + t * 32, 512);
      SB();
      f32x4 d[4];
#pragma unroll
      for (int t = 0; t < 4; ++t) d[t] = mfma(af[4 + t], xg[1], mfma(af[t], xg[0], f32x4{0, 0, 0, 0}));
      v4u gr;
#pragma unroll
      for (int r = 0; r < 4; ++r) { const float lo = (n & 2) ? d[1][r] : d[0][r], hi = (n & 2) ? d[3][r] : d[2][r]; gr[r] = tagf<12>((n & 4) ? hi : lo, t12); }
      if (n < 8 && has_f) st_sc1(hoff, gr, ws);
      else asm volatile("" ::: "memory");
      lds_set(L, fh, 1);
    }
    ST(3);
    {
      const unsigned qo = WS_Q + ((kvh * 2 + b) * 4 + hw) * 2048 + lane * 16;
      if (hw == 1) {
        wait_until<1>(L, t0, TT);
        v4u tv;
        ld1w(tv, WS_T + (lane & 7) * 128, ws);
        const bool ok = tv.y == t32 && tv.w == t32;
        float d = ok ? (float)(int)(tv.x - t0) : 0.f, k = ok ? 1.f : 0.f;
        d += dppf<XOR1>(d); k += dppf<XOR1>(k); d += dppf<XOR2>(d); k += dppf<XOR2>(k); d += dppf<0x141>(d); k += dppf<0x141>(k);
        const float dm = fminf(fmaxf(d * 16.f * frcp(fmaxf(k, 1.f)), -1600.f), 1600.f);
        lds_set(L, L_DT, (int)dm + (k > 0.f ? TBIAS : 0));
      }
      wait_until<1>(L, t0, TQ);
      v4u q0, q1;
      CNT0(14); CNT0(15);
#pragma unroll 1
      for (int it = 0; it < SPIN_CAP; ++it) { ld2w(q0, q1, qo, qo + 1024, ws); if (!ANY(badtag<8>(q0, t8) | badtag<8>(q1, t8))) break; CNT(14); }
      float v[4];
#pragma unroll
      for (int r = 0; r < 4; ++r) { v[r] = untag<8>(q0[r]) + untag<8>(q1[r]); v[r] = sw32(v[r], v[r]); }
      if (lane < 32) *(LAS v2u*)(L + L_Q + hw * QSTR + lane * 8) = v2u{pk_bf16(v[0], v[1]), pk_bf16(v[2], v[3])};
      LGKM0(); lds_set(L, fh, 2);
      ST(4);
    }
    const int e = lane & 7;
    const unsigned ho = WS_H + (((hw & 1) * 16 + (lane >> 3)) * 448 + 7 * rg + (e < 7 ? e : 6)) * 16;
    if (hw >= 2) {
      wait_until<1>(L, t0, TH);
      DMA_S(" sc1", ho, ws, lbase + L_WD + (hw & 1) * 2048); DMA_S(" sc1", ho + 8 * 448 * 16, ws, lbase + L_WD + (hw & 1) * 2048 + 1024);
    }
    wait4<15, 1>(L, L_FH, 2);
    v4u qf[4], kf[16];
#pragma unroll
    for (int s = 0; s < 4; ++s) qf[s] = *(LAS v4u*)(L + L_Q + (n & 3) * QSTR + s * 64 + g * 16);
    wait4<15, 0>(L, L_FS, 13 + 4 * hw);
    ST(6);
#pragma unroll
    for (int s = 0; s < 4; ++s)
#pragma unroll
      for (int t = 0; t < 4; ++t) kf[s * 4 + t] = *(LAS v4u*)(L + L_K + (4 * hw + t) * 4096 + n * 256 + (((4 * s + g) ^ n) * 16));
    SB();
    float sc[4];
    {
      f32x4 d[4];
#pragma unroll
      for (int t = 0; t < 4; ++t) d[t] = f32x4{0, 0, 0, 0};
#pragma unroll
      for (int s = 0; s < 4; ++s)
#pragma unroll
        for (int t = 0; t < 4; ++t) d[t] = mfma(kf[s * 4 + t], qf[s], d[t]);
      constexpr float SC = 0.08838834764831845f;
      float v[4];
#pragma unroll
      for (int r = 0; r < 4; ++r) { const float lo = (n & 4) ? d[1][r] : d[0][r], hi = (n & 4) ? d[3][r] : d[2][r]; v[r] = (n & 8) ? hi : lo; }
      const unsigned a = pk_bf16(v[0], v[1]), bq = pk_bf16(v[2], v[3]);
      const unsigned a2 = pk_bf16(asf(a << 16) * SC, asf(a & 0xFFFF0000u) * SC), b2 = pk_bf16(asf(bq << 16) * SC, asf(bq & 0xFFFF0000u) * SC);
      sc[0] = asf(a2 << 16); sc[1] = asf(a2 & 0xFFFF0000u); sc[2] = asf(b2 << 16); sc[3] = asf(b2 & 0xFFFF0000u);
    }
    float m = vmax(vmax(sc[0], sc[1]), vmax(sc[2], sc[3]));
    m = vmax(m, dppf<ROR4>(m)); m = vmax(m, dppf<ROR8>(m)); m = x16max(m); m = x32max(m);
    if (lane < 4) *(LAS float*)(L + L_MX + lane * 16 + hw * 4) = m;
    LGKM0(); lds_set(L, fh, 3);
    wait4<15, 0>(L, L_FH, 3);
    { const f32x4 mm = *(LAS f32x4*)(L + L_MX + (n & 3) * 16); m = vmax(vmax(mm[0], mm[1]), vmax(mm[2], mm[3])); }
    {
      float p[4];
#pragma unroll
      for (int r = 0; r < 4; ++r) p[r] = __builtin_amdgcn_exp2f((sc[r] - m) * 1.4426950408889634f);
      *(LAS v2u*)(L + L_P + (n & 3) * PSTR + (64 * hw + 16 * (n >> 2) + 4 * g) * 2) = v2u{pk_bf16(p[0], p[1]), pk_bf16(p[2], p[3])};
      float l = (p[0] + p[1]) + (p[2] + p[3]);
      l += dppf<ROR4>(l); l += dppf<ROR8>(l); l = sw16(l, l); l = sw32(l, l);
      if (lane < 4) *(LAS float*)(L + L_SUM + lane * 16 + hw * 4) = l;
    }
    LGKM0(); lds_set(L, fh, 4);
    ST(7);
    TR_ADDR(hw);
    unsigned ooff = WS_O + (((2 * (4 * kvh + (n & 3)) + (hw >> 1)) * 2 + b) * 16 + sp) * 256 + (hw & 1) * 128 + ((n >> 2) & 1) * 64 + g * 16;
    PIN(ooff);
    wait4<15, 0>(L, L_FH, 4);
    v4u pf[8];
#pragma unroll
    for (int u = 0; u < 8; ++u) pf[u] = *(LAS v4u*)(L + L_P + (n & 3) * PSTR + u * 64 + g * 16);
    if (hw == 2) {
      const f32x4 ls = *(LAS f32x4*)(L + L_SUM + (n & 3) * 16);
      const v4u gr = {(asu(m) & 0xFFFFF000u) | t12, tagf<12>((ls[0] + ls[1]) + (ls[2] + ls[3]), t12), t12, t12};
      if (lane < 4) st_sc1(WS_ST + (((kvh * 4 + lane) * 2 + b) * 16 + sp) * 16, gr, ws);
    }
    if (hw >= 2) {
      if (hw == 2) vmcnt<1>(); else vmcnt<0>();
      v4u h0 = *(LAS v4u*)(L + L_WD + (hw & 1) * 2048 + lane * 16), h1 = *(LAS v4u*)(L + L_WD + (hw & 1) * 2048 + 1024 + lane * 16);
#pragma unroll 1
      for (int it = 0; it < SPIN_CAP && ANY(badtag<12>(h0, t12) | badtag<12>(h1, t12)); ++it) { ld2w(h0, h1, ho, ho + 8 * 448 * 16, ws); CNT(14); }
      float v[4];
#pragma unroll
      for (int r = 0; r < 4; ++r) v[r] = sw32(untag<12>(h0[r]), untag<12>(h1[r]));
      const float a0 = sw16(v[0], v[1]), a1 = sw16(v[2], v[3]);
      const float b0 = a0 + dppf<ROR8>(a0), b1 = a1 + dppf<ROR8>(a1);
      const float gg = rbf((lane & 8) ? b1 : b0);
      const auto sw = __builtin_amdgcn_permlane32_swap(asu(gg), asu(gg), false, false);
      const float hv = rbf(gg * frcp(1.f + fexp(-gg))) * asf(sw[1]);
      if (lane < 32) *(LAS u16*)(L + L_HB + (hw & 1) * 64 + (4 * e + ((lane >> 2) & 2) + (lane >> 4)) * 2) = e < 7 ? (u16)(pk_bf16(hv, hv) & 0xFFFFu) : (u16)0;
      LGKM0(); lds_set(L, fh, 5);
      ST(5);
    }
    f32x4 o0 = {0, 0, 0, 0}, o1 = {0, 0, 0, 0};
#define PVN(u0, nu) do { v4u va[nu], vb[nu]; _Pragma("unroll") for (int u = 0; u < nu; ++u) { va[u] = tr2(L + L_V + ta0 + (u0 + u) * 8192, 1024); vb[u] = tr2(L + L_V + ta1 + (u0 + u) * 8192, 1024); } SB(); \
      _Pragma("unroll") for (int u = 0; u < nu; ++u) { o0 = mfma(va[u], pf[u0 + u], o0); o1 = mfma(vb[u], pf[u0 + u], o1); } } while (0)
    wait4<15, 1>(L, L_FS, 33);
    PVN(0, 4); PIN(o0); PIN(o1);
    ST(8);
    wait4<15, 0>(L, L_FS, 39);
    PVN(4, 3); PIN(o0); PIN(o1);
    wait4<15, 0>(L, L_FS, 41);
    ST(9);
    PVN(7, 1);
    {
      v4u gr;
#pragma unroll
      for (int r = 0; r < 4; ++r) gr[r] = tagf<12>((n & 4) ? o1[r] : o0[r], t12);
      if (n < 8) st_sc1(ooff, gr, ws);
      else asm volatile("" ::: "memory");
    }
    ST(10);
    if (hw < 2) {
      const unsigned so = WS_ST + (((rg >> 1) * 2 + hw) * 16 + 4 * (n & 3) + g) * 16;
      const unsigned oo = WS_O + (rg * 2 + hw) * 4096 + lane * 16;
      wait_until<0>(L, t0, TVE + TO);
      ST(11);
      v4u st, a0, a1, a2, a3;
      asm volatile("global_load_dwordx4 %0, %5, %7 sc1\n\tglobal_load_dwordx4 %1, %6, %7 sc1\n\tglobal_load_dwordx4 %2, %6, %7 offset:1024 sc1\n\t"
                   "global_load_dwordx4 %3, %6, %7 offset:2048 sc1\n\tglobal_load_dwordx4 %4, %6, %7 offset:3072 sc1\n\ts_waitcnt vmcnt(4)"
                   : "=&v"(st), "=&v"(a0), "=&v"(a1), "=&v"(a2), "=&v"(a3) : "v"(so), "v"(oo), "s"(ws) : "memory");
      if (ANY(badtag<12>(st, t12))) {
#pragma unroll 1
        for (int it = 0; it < SPIN_CAP; ++it) { CNT(14); ld1w(st, so, ws); if (!ANY(badtag<12>(st, t12))) break; }
      }
      const float ms = untag<12>(st.x), ls = untag<12>(st.y);
      float M = ms; M = vmax(M, dppf<XOR1>(M)); M = vmax(M, dppf<XOR2>(M)); M = x16max(M); M = x32max(M);
      const float ex = __builtin_amdgcn_exp2f((ms - M) * 1.4426950408889634f);
      float Ls = ex * ls; Ls += dppf<XOR1>(Ls); Ls += dppf<XOR2>(Ls); Ls = sw16(Ls, Ls); Ls = sw32(Ls, Ls);
      const float wv_ = ex * frcp(Ls);
      float wt0 = dppf<0x00>(wv_), wt1 = dppf<0x55>(wv_), wt2 = dppf<0xAA>(wv_), wt3 = dppf<0xFF>(wv_);
      PIN(wt0); PIN(wt1); PIN(wt2); PIN(wt3);
      asm volatile("s_waitcnt vmcnt(0)" : "+v"(a0), "+v"(a1), "+v"(a2), "+v"(a3)::"memory");
      ST(12);
      unsigned oba = L_OB + hw * 128 + (4 * n + (g & 1) * 2 + (g >> 1)) * 2;
#pragma unroll 1
      for (int it = 0; it < SPIN_CAP; ++it) {
        const f32x2 w0 = {wt0, wt0}, w1 = {wt1, wt1}, w2 = {wt2, wt2}, w3 = {wt3, wt3};
#define UT2(a, i) f32x2{untag<12>(a[i]), untag<12>(a[i + 1])}
        const f32x2 lo = __builtin_elementwise_fma(w0, UT2(a0, 0), w1 * UT2(a1, 0)) + __builtin_elementwise_fma(w2, UT2(a2, 0), w3 * UT2(a3, 0));
        const f32x2 hi = __builtin_elementwise_fma(w0, UT2(a0, 2), w1 * UT2(a1, 2)) + __builtin_elementwise_fma(w2, UT2(a2, 2), w3 * UT2(a3, 2));
        const float t = sw16(sw32(lo[0], lo[1]), sw32(hi[0], hi[1]));
        *(volatile LAS u16*)(L + oba) = (u16)(pk_bf16(t, t) & 0xFFFFu);
        if (!ANY(badtag<12>(a0, t12) | badtag<12>(a1, t12) | badtag<12>(a2, t12) | badtag<12>(a3, t12))) break;
        CNT(15);
        ld4w(a0, a1, a2, a3, oo, ws);
      }
      LGKM0();
      ST(13);
      __builtin_amdgcn_s_barrier();
    } else if (hw == 2) {
      __builtin_amdgcn_s_barrier();
    } else {
      unsigned dummy;
      asm volatile("s_nop 4\n\tglobal_load_dword %0, %1, %2\n\ts_waitcnt vmcnt(0)" : "=&v"(dummy) : "v"(0), "s"(out) : "memory");
      unsigned yo = WS_Y + (((xc >> 1) * 64 + lane) * 2 + (xc & 1)) * 512 + j * 16;
      u16* op = out + (xc & 1) * 512 + (xc >> 1) * 128 + 4 * j + (g & 1) * 2 + (g >> 1);
      v4u y;
      PIN(yo); PIN(op);
      __builtin_amdgcn_s_barrier();
      for (int z = 0; z < Y_DELAY; ++z) __builtin_amdgcn_s_sleep(1);
      wait_until<0>(L, t0, TVE + TO + TY);
      ST(11);
#pragma unroll 1
      for (int it = 0; it < SPIN_CAP; ++it) {
        ld1w(y, yo, ws);
        if (!ANY(badtag<12>(y, t12))) break;
        CNT(15);
      }
      ST(12);
      float t = sw16(sw32(untag<12>(y.x), untag<12>(y.y)), sw32(untag<12>(y.z), untag<12>(y.w)));
      t += dppf<XOR1>(t); t += dppf<XOR2>(t); t += dppf<ROR4>(t); t += dppf<ROR8>(t);
      if (n == 0) *op = (u16)(pk_bf16(t, t) & 0xFFFFu);
      if (c == 0 && lane == 0) __hip_atomic_store((GAS unsigned*)ws, t32, __ATOMIC_RELAXED, __HIP_MEMORY_SCOPE_AGENT);
      ST(13);
    }
  } else {
    const int w = wv;
    const unsigned fs = L_FS + 4 * w;
    int issued = 5, landed = 0;
    int due = TK0 + (DITH == 0 ? w * (TAU / 4) : DITH == 1 ? ((w + (j >> 1)) & 3) * (TAU / 4) : DITH == 2 ? (((w + (j >> 1)) & 3) * 8 + xc) * (TAU / 32) : (((w + (j >> 2) + xc) & 3) * 4 + (j & 3)) * (TAU / 16));
    int seen = 0, dt = 0;
#define PACE() do { \
      if (seen < due || vm_inflight() >= WIN) { \
        const int d_ = *(volatile LAS int*)(L + L_DT); \
        _Pragma("unroll 1") for (int it_ = 0; it_ < SPIN_CAP; ++it_) { \
          seen = (int)now16(t0) - dt; \
          const int vm_ = vm_inflight(); if (issued - vm_ != landed) { landed = issued - vm_; lds_set(L, fs, landed); } \
          if (seen >= due && vm_ < WIN) break; } \
        const int dn_ = __builtin_amdgcn_readfirstlane(d_); seen += dt - dn_; dt = dn_; } \
      due += TAU; ++issued; } while (0)
    {
      const char* sb = w == 0 ? (const char*)x : w == 1 ? (const char*)x + 1024 : w == 2 ? (const char*)gw : (const char*)ws;
      DMA_S("", w == 3 ? 0 : lane * 16, sb, lbase + (w < 2 ? L_X : L_P - 2048) + w * 1024);
    }
    {
      const char* base = (const char*)wq + (size_t)(128 * qp + 8 * w) * 8192 + qc * 128;
      const unsigned voff = (lane >> 3) * 8192 + (lane & 7) * 16;
#pragma unroll
      for (int i = 0; i < 4; ++i) DMA_S(NTW, voff, base + (size_t)i * 32 * 8192, lbase + L_WQ + (4 * i + w) * 1024);
    }
    if (w == 0 && lane < 9) *(volatile LAS int*)(L + L_FS + 4 * lane) = lane < 8 ? -64 : 0;
    ST(7);
    LGKM0();
    __builtin_amdgcn_s_barrier();
    {
      const unsigned voff = has_f ? (lane >> 3) * 3584 + (lane & 7) * 16 : 0;
      const size_t off = has_f ? (size_t)(64 * xc + 8 * w) * 3584 + j * 128 : 0;
      const unsigned st = has_f ? 32 * 3584 : 0;
#pragma unroll 1
      for (int i = 0; i < 4; ++i) { PACE(); DMA_S(NTW, voff, (const char*)(i < 2 ? wg : wu) + off + (i & 1) * st, lbase + L_GU + (4 * i + w) * 1024); }
    }
    const int rot = (j >> 2) * 2 + (j & 1);
    const size_t kvoff = ((size_t)(b * 8 + kvh) * 16384 + 12288 + sp * 256) * 256 + w * 1024;
    {
      const char* kb = (const char*)kc + kvoff;
      const unsigned voff = g * 256 + ((n ^ (4 * w + g)) * 16);
#pragma unroll 1
      for (int i = 0; i < 16; ++i) { PACE(); DMA_S(NTS, voff, kb + (((i + rot) & 15) << 12), lbase + L_K + i * 4096 + w * 1024); }
    }
    ST(1);
    {
      const unsigned voff = g * 256 + ((n ^ (2 * (g | ((w >> 1) << 2)))) * 16);
      const char* vb = (const char*)vc + kvoff;
#pragma unroll 1
      for (int i = 0; i < 8; ++i) { if (i >= 16 - NL) due += TAUL - TAU; PACE(); DMA_S(NTS, voff, vb + (((i + rot) & 15) << 12), lbase + L_V + i * 4096 + w * 1024); }
      wait4<3, 0>(L, L_FH, 1);
#pragma unroll 1
      for (int i = 8; i < 16; ++i) { if (i >= 16 - NL) due += TAUL - TAU; PACE(); DMA_S(NTS, voff, vb + (((i + rot) & 15) << 12), lbase + L_V + i * 4096 + w * 1024); }
    }
    ST(2);
    due += GAP;
    wait4<12, 0>(L, L_FH, 5);
    {
      const unsigned vo = g * 1024 + ((n ^ (2 * (g | ((w >> 1) << 2)))) * 16);
      const char* ob = (const char*)wo + (size_t)(64 * rg + 4 * w) * 1024 + cg * 256;
      const char* db = (const char*)wd + (size_t)(28 * rg) * 1024 + cg * 256;
#pragma unroll 1
      for (int i = 0; i < 6; ++i) {
        const char* src = i >= 2 ? ob + (i - 2) * 16384 : db + (i == 0 ? 4 * w : w < 3 ? 16 + 4 * w : 0) * 1024;
        due += TAU2 - TAU; if (i == 6 - HOLD) due = TVE + TO + 32; PACE(); if (i >= 6 - HOLD) due -= TAU2;
        DMA_S(NTW, vo, src, lbase + L_WO + (i >= 2 ? i - 2 : i + 4) * 4096 + w * 1024);
      }
    }
    ST(3);
#pragma unroll 1
    for (int it = 0; it < SPIN_CAP; ++it) { const int vm = vm_inflight(); if (issued - vm != landed) { landed = issued - vm; lds_set(L, fs, landed); } if (landed >= 41) break; }
    ST(4);
    const unsigned t12 = __builtin_amdgcn_readfirstlane(*(volatile LAS unsigned*)(L + L_TAG)) & 0xFFFu;
    TR_ADDR(w);
    unsigned yoff = WS_Y + ((cg * 64 + rg) * 2 + (n & 1)) * 512 + w * 128 + ((n >> 1) & 1) * 64 + g * 16;
    PIN(yoff);
    const f32x2 gt = *(LAS f32x2*)(L + L_GATE + (n & 1) * 8);
    vmcnt<4>(); lds_set(L, fs, 43 > landed ? 43 : landed);
    wait4<15, 0>(L, L_FS, 43);
    f32x4 fy;
    {
      v4u df0 = tr2(L + L_WD + ta0, 1024), df1 = tr2(L + L_WD + ta1, 1024);
      PIN(df0); PIN(df1);
      const v4u hb = *(LAS v4u*)(L + L_HB + (n & 1) * 64 + g * 16);
      const f32x4 f0 = mfma(df0, hb, f32x4{0, 0, 0, 0}), f1 = mfma(df1, hb, f32x4{0, 0, 0, 0});
#pragma unroll
      for (int r = 0; r < 4; ++r) fy[r] = gt[1] * ((n & 2) ? f1[r] : f0[r]);
    }
    PIN(fy);
    vmcnt<0>(); lds_set(L, fs, 47);
    ST(5);
    wait4<15, 0>(L, L_FS, 47);
    v4u wf[4];
    wf[0] = tr2(L + L_WO + ta0, 1024); wf[1] = tr2(L + L_WO + ta1, 1024); wf[2] = tr2(L + L_WO + ta0 + 8192, 1024); wf[3] = tr2(L + L_WO + ta1 + 8192, 1024);
    unsigned oba = L_OB + (n & 1) * 128 + g * 16;
    PIN(fy); PIN(wf[0]); PIN(wf[1]); PIN(wf[2]); PIN(wf[3]); PIN(oba);
    __builtin_amdgcn_s_setprio(3);
    ST(6);
    __builtin_amdgcn_s_barrier();
    asm volatile("" ::: "memory");
    ST(10);
    {
      const v4u b0 = *(LAS v4u*)(L + oba), b1 = *(LAS v4u*)(L + oba + 64);
      f32x4 y0 = mfma(wf[0], b0, f32x4{0, 0, 0, 0}), y1 = mfma(wf[1], b0, f32x4{0, 0, 0, 0});
      y0 = mfma(wf[2], b1, y0); y1 = mfma(wf[3], b1, y1);
      v4u gr;
#pragma unroll
      for (int r = 0; r < 4; ++r) gr[r] = tagf<12>(fmaf(gt[0], (n & 2) ? y1[r] : y0[r], fy[r]), t12);
      if (n < 4) st_sc1(yoff, gr, ws);
      else asm volatile("" ::: "memory");
    }
    ST(11);
  }
}

extern "C" void kernel_launch(void* const* d_in, const int* in_sizes, int n_in, void* d_out, int out_size, void* d_ws, size_t ws_size, hipStream_t stream) {
  (void)hipFuncSetAttribute((const void*)fused_decode, hipFuncAttributeMaxDynamicSharedMemorySize, L_END);
  fused_decode<<<256, 512, L_END, stream>>>((const u16*)d_in[0], (const u16*)d_in[9], (char*)d_ws, (const u16*)d_in[3], (const u16*)d_in[6], (const u16*)d_in[7],
                                            (const u16*)d_in[1], (const u16*)d_in[2], (const u16*)d_in[4], (const u16*)d_in[8], (u16*)d_out);
}
```

```cpp
#define TAU 240
#define TAUL 340
#define NL 5
#define TAU2 256
#define TO 2500
#define TY 1800
#include <hip/hip_runtime.h>
#include <hip/hip_bf16.h>
#include <stdint.h>

#define STAMP 0
#define WIN 12
#define NTS " nt"
#define NTW " nt"
#define Q_DELAY 16
#define CODE_PF 25
#define ABL 0
#define TK0 440
#define TT 2900
#define TQ 4300
#define TH 5600
#define GAP 0
#define HOLD 0
#define PF 0
#define DITH 0
#define TVE (TK0 + (35 - NL) * TAU + NL * TAUL)
#define TBIAS 0
#define O_SLEEP 1
#define Y_DELAY 4

typedef unsigned short u16;
typedef unsigned long long u64;
typedef unsigned v4u __attribute__((ext_vector_type(4)));
typedef int v4i __attribute__((ext_vector_type(4)));
typedef unsigned v2u __attribute__((ext_vector_type(2)));
typedef short v4s __attribute__((ext_vector_type(4)));
typedef float f32x4 __attribute__((ext_vector_type(4)));
typedef float f32x2 __attribute__((ext_vector_type(2)));
typedef __bf16 v8bf __attribute__((ext_vector_type(8)));
typedef __bf16 v2bf __attribute__((ext_vector_type(2)));
#define GAS __attribute__((address_space(1)))
#define LAS __attribute__((address_space(3)))
#define SB() __builtin_amdgcn_sched_barrier(0)

constexpr unsigned WS_T = 1024;
constexpr unsigned WS_Q = 4096;
constexpr unsigned WS_H = WS_Q + 131072;
constexpr unsigned WS_ST = WS_H + 229376;
constexpr unsigned WS_O = WS_ST + 16384;
constexpr unsigned WS_Y = WS_O + 524288;
constexpr unsigned WS_STAMP = 4u << 20;
constexpr unsigned L_K = 0, L_V = 65536, L_WQ = L_V + 8 * 4096, L_WO = 131072, L_GU = L_WO, L_WD = L_WO + 16384, L_X = L_WD + 8192,
                   L_Q = L_X + 2048, QSTR = 320, L_P = L_Q + 4 * QSTR, PSTR = 576, L_OB = L_P + 4 * PSTR, L_HB = L_OB + 256, L_MX = L_HB + 128,
                   L_SUM = L_MX + 64, L_GATE = L_SUM + 64, L_FS = L_GATE + 16, L_FH = L_FS + 16, L_DT = L_FH + 16, L_TAG = L_DT + 4, L_END = L_DT + 16;
static_assert(L_END <= 163840, "LDS");
constexpr int SPIN_CAP = 1 << 17;

__device__ __forceinline__ float asf(unsigned u) { return __builtin_bit_cast(float, u); }
__device__ __forceinline__ unsigned asu(float f) { return __builtin_bit_cast(unsigned, f); }
__device__ __forceinline__ unsigned pk_bf16(float lo, float hi) { f32x2 x = {lo, hi}; return __builtin_bit_cast(unsigned, __builtin_convertvector(x, v2bf)); }
__device__ __forceinline__ float rbf(float f) { return asf(pk_bf16(f, f) << 16); }
template <int CTRL> __device__ __forceinline__ float dppf(float x) { return asf(__builtin_amdgcn_mov_dpp(asu(x), CTRL, 0xf, 0xf, true)); }
constexpr int XOR1 = 0xB1, XOR2 = 0x4E, ROR4 = 0x124, ROR8 = 0x128;
__device__ __forceinline__ float sw16(float a, float b) { auto s = __builtin_amdgcn_permlane16_swap(asu(a), asu(b), false, false); return asf(s[0]) + asf(s[1]); }
__device__ __forceinline__ float sw32(float a, float b) { auto s = __builtin_amdgcn_permlane32_swap(asu(a), asu(b), false, false); return asf(s[0]) + asf(s[1]); }
__device__ __forceinline__ float vmax(float a, float b) { float r; asm("v_max_f32 %0, %1, %2" : "=v"(r) : "v"(a), "v"(b)); return r; }
__device__ __forceinline__ float x16max(float x) { auto s = __builtin_amdgcn_permlane16_swap(asu(x), asu(x), false, false); return vmax(asf(s[0]), asf(s[1])); }
__device__ __forceinline__ float x32max(float x) { auto s = __builtin_amdgcn_permlane32_swap(asu(x), asu(x), false, false); return vmax(asf(s[0]), asf(s[1])); }
__device__ __forceinline__ f32x4 mfma(v4u a, v4u b, f32x4 c) { return __builtin_amdgcn_mfma_f32_16x16x32_bf16(__builtin_bit_cast(v8bf, a), __builtin_bit_cast(v8bf, b), c, 0, 0, 0); }
__device__ __forceinline__ v2u trread(LAS char* p) { return __builtin_bit_cast(v2u, __builtin_amdgcn_ds_read_tr16_b64_v4i16((LAS v4s*)p)); }
__device__ __forceinline__ v4u tr2(LAS char* p, int d) { v2u a = trread(p), b = trread(p + d); return v4u{a.x, a.y, b.x, b.y}; }
template <int B> __device__ __forceinline__ unsigned tagf(float v, unsigned t) { return ((asu(v) + (1u << (B - 1))) & ~((1u << B) - 1)) | t; }
template <int B> __device__ __forceinline__ float untag(unsigned w) { return asf(w & ~((1u << B) - 1)); }
template <int B> __device__ __forceinline__ unsigned badtag(v4u g, unsigned t) {
  return (__builtin_amdgcn_bitop3_b32(g.x, g.y, t, 0x7E) | __builtin_amdgcn_bitop3_b32(g.z, g.w, t, 0x7E)) & ((1u << B) - 1);
}
#define PIN(x) asm volatile("" : "+v"(x))
#define CNT(i)
#define CNT0(i)
#define ANY(x) (__builtin_amdgcn_ballot_w64((x) != 0) != 0)

#define DMA_S(NTSTR, voff, sbase, ldst) asm volatile("s_mov_b32 m0, %2\n\ts_nop 0\n\tglobal_load_lds_dwordx4 %0, %1" NTSTR :: "v"(voff), "s"(sbase), "s"(ldst) : "memory")
template <int N> __device__ __forceinline__ void vmcnt() { asm volatile("s_waitcnt vmcnt(%0)" ::"i"(N) : "memory"); }
#define LGKM0() asm volatile("s_waitcnt lgkmcnt(0)" ::: "memory")
__device__ __forceinline__ void ld_sc1(v4u& d, unsigned voff, const char* sbase) { asm volatile("global_load_dwordx4 %0, %1, %2 sc1" : "=v"(d) : "v"(voff), "s"(sbase) : "memory"); }
__device__ __forceinline__ void ld1w(v4u& a, unsigned o0, const char* sb) {
  asm volatile("global_load_dwordx4 %0, %1, %2 sc1\n\ts_waitcnt vmcnt(0)" : "=&v"(a) : "v"(o0), "s"(sb) : "memory"); }
__device__ __forceinline__ void ld2w(v4u& a, v4u& b, unsigned o0, unsigned o1, const char* sb) {
  asm volatile("global_load_dwordx4 %0, %2, %4 sc1\n\tglobal_load_dwordx4 %1, %3, %4 sc1\n\ts_waitcnt vmcnt(0)" : "=&v"(a), "=&v"(b) : "v"(o0), "v"(o1), "s"(sb) : "memory"); }
__device__ __forceinline__ void ld4w(v4u& a, v4u& b, v4u& c, v4u& d, unsigned o0, const char* sb) {
  asm volatile("global_load_dwordx4 %0, %4, %5 sc1\n\tglobal_load_dwordx4 %1, %4, %5 offset:1024 sc1\n\tglobal_load_dwordx4 %2, %4, %5 offset:2048 sc1\n\t"
               "global_load_dwordx4 %3, %4, %5 offset:3072 sc1\n\ts_waitcnt vmcnt(0)" : "=&v"(a), "=&v"(b), "=&v"(c), "=&v"(d) : "v"(o0), "s"(sb) : "memory"); }
__device__ __forceinline__ void st_sc1(unsigned voff, v4u d, char* sbase) { asm volatile("global_store_dwordx4 %0, %1, %2 sc1\n\ts_nop 1" ::"v"(voff), "v"(d), "s"(sbase) : "memory"); }

#define ST(i)

__device__ __forceinline__ unsigned now16(unsigned t0) { return ((unsigned)__builtin_amdgcn_s_memrealtime() - t0) * 16u; }
__device__ __forceinline__ int vm_inflight() { unsigned r; asm volatile("s_getreg_b32 %0, hwreg(HW_REG_IB_STS, 0, 4)" : "=s"(r)::"memory"); return (int)r; }
__device__ __forceinline__ float frcp(float x) { return __builtin_amdgcn_rcpf(x); }
__device__ __forceinline__ float fexp(float x) { return __builtin_amdgcn_exp2f(x * 1.4426950408889634f); }
__device__ __forceinline__ int cnow16(LAS char* L, unsigned t0) {
  const int d = *(volatile LAS int*)(L + L_DT);
  return (int)now16(t0) - __builtin_amdgcn_readfirstlane(d);
}
template <int SLEEP> __device__ __forceinline__ void wait_until(LAS char* L, unsigned t0, int t) {
#pragma unroll 1
  for (int it = 0; it < SPIN_CAP && cnow16(L, t0) < t; ++it) { if (SLEEP) __builtin_amdgcn_s_sleep(SLEEP); }
}
__device__ __forceinline__ void lds_set(LAS char* L, unsigned off, unsigned v) {
  asm volatile("s_mov_b64 exec, 1\n\tds_write_b32 %0, %1\n\ts_mov_b64 exec, -1" ::"v"((unsigned)(uintptr_t)(L + off)), "v"(v) : "memory");
}
template <int MASK, int SLEEP> __device__ __forceinline__ void wait4(LAS char* L, unsigned off, int n) {
#pragma unroll 1
  for (int it = 0; it < SPIN_CAP; ++it) {
    const v4i f = *(volatile LAS v4i*)(L + off);
    int m = 0x7fffffff;
    if (MASK & 1) m = min(m, f.x);
    if (MASK & 2) m = min(m, f.y);
    if (MASK & 4) m = min(m, f.z);
    if (MASK & 8) m = min(m, f.w);
    if (__builtin_amdgcn_readfirstlane(m) >= n) break;
    if (SLEEP) __builtin_amdgcn_s_sleep(SLEEP);
  }
  asm volatile("" ::: "memory");
}

#define TR_ADDR(w_) const int fr = (n >> 2) | ((g & 1) << 2); \
    unsigned ta0 = (8 * g + (n >> 2)) * 256 + ((((2 * (w_)) ^ fr) * 2 + ((n & 3) >> 1)) * 16) + (n & 1) * 8; \
    unsigned ta1 = (8 * g + (n >> 2)) * 256 + ((((2 * (w_) + 1) ^ fr) * 2 + ((n & 3) >> 1)) * 16) + (n & 1) * 8; PIN(ta0); PIN(ta1)

__global__ void __launch_bounds__(512) __attribute__((amdgpu_waves_per_eu(2, 2)))
fused_decode(const u16* __restrict__ x, const u16* __restrict__ gw, char* __restrict__ ws, const u16* __restrict__ wq, const u16* __restrict__ wg,
             const u16* __restrict__ wu, const u16* __restrict__ kc, const u16* __restrict__ vc, const u16* __restrict__ wo, const u16* __restrict__ wd,
             u16* __restrict__ out) {
  extern __shared__ __attribute__((aligned(16))) char smem[];
  LAS char* L = (LAS char*)smem;
  const unsigned lbase = (unsigned)(uintptr_t)L;
  const int c = blockIdx.x, lane = threadIdx.x & 63, wv = __builtin_amdgcn_readfirstlane(threadIdx.x >> 6);
  const int xc = c & 7, j = c >> 3;
  const int kvh = xc, b = j & 1, sp = j >> 1;
  const int rg = 8 * xc + (j >> 2), cg = j & 3;
  const int qp = xc >> 1, qc = 32 * (xc & 1) + j;
  const bool has_f = j < 28;
  const int g = lane >> 4, n = lane & 15;
  asm volatile("" ::"s"(x), "s"(kc), "s"(vc), "s"(wq), "s"(wo), "s"(wg), "s"(wu), "s"(wd), "s"(gw), "s"(out), "s"(ws));
  const unsigned t0 = (unsigned)__builtin_amdgcn_s_memrealtime();
  ST(0);
  if (wv == 4) {
    const char* pc;
    asm volatile("s_getpc_b64 %0" : "=s"(pc));
    asm volatile("s_mov_b32 m0, %2\n\ts_nop 0\n\tglobal_load_lds_dword %0, %1" :: "v"(min(j, CODE_PF) * 512 + (lane & 7) * 64), "s"((const char*)((uintptr_t)pc & ~(uintptr_t)255)), "s"(lbase + L_OB) : "memory");
  }

  if (wv >= 4) {
    const int hw = wv - 4;
    const unsigned fh = L_FH + 4 * hw;
    __builtin_amdgcn_s_setprio(2);
    __builtin_amdgcn_s_barrier();
    wait4<15, 0>(L, L_FS, 1);
    const unsigned t32 = __builtin_amdgcn_readfirstlane(*(LAS unsigned*)(L + L_P + 1024)) + 1, t12 = t32 & 0xFFFu, t8 = t32 & 0xFFu;
    ST(1);
    if (hw == 2 && c < 8) { if (lane == 0) st_sc1(WS_T + c * 128, v4u{t0, t32, t0, t32}, ws); }
    if (hw < 2) {
      v4u xq[4];
#pragma unroll
      for (int s = 0; s < 4; ++s) xq[s] = *(LAS v4u*)(L + L_X + (n & 1) * 1024 + qp * 256 + s * 64 + g * 16);
      const int col = 64 * qc + 32 * hw + 16 * ((n >> 1) & 1) + 4 * g;
      const unsigned qoff = WS_Q + (((((col >> 9) * 2 + (n & 1)) * 4 + ((col >> 7) & 3)) * 4 + qp) * 128 + (col & 127)) * 4;
      LAS char* a = L + L_WQ + (8 * g + (n >> 2)) * 128 + hw * 64 + (n & 3) * 8;
      wait4<15, 0>(L, L_FS, 5);
      ST(2);
      v4u af[8];
#pragma unroll
      for (int s = 0; s < 4; ++s) { af[2 * s] = tr2(a + s * 4096, 512); af[2 * s + 1] = tr2(a + s * 4096 + 32, 512); }
      SB();
      f32x4 d0 = {0, 0, 0, 0}, d1 = {0, 0, 0, 0};
#pragma unroll
      for (int s = 0; s < 4; ++s) { d0 = mfma(af[2 * s], xq[s], d0); d1 = mfma(af[2 * s + 1], xq[s], d1); }
      v4u gr;
#pragma unroll
      for (int r = 0; r < 4; ++r) gr[r] = tagf<8>((n & 2) ? d1[r] : d0[r], t8);
      if (n < 4) st_sc1(qoff, gr, ws);
      else asm volatile("" ::: "memory");
      lds_set(L, fh, 1);
    } else {
      {
        const v4u gwv = *(LAS v4u*)(L + L_P + lane * 16), xv = *(LAS v4u*)(L + L_X + (hw & 1) * 1024 + lane * 16);
        float s = 0.f;
#pragma unroll
        for (int k = 0; k < 4; ++k) {
          const unsigned pr = pk_bf16(asf(xv[k] << 16) * asf(gwv[k] << 16), asf(xv[k] & 0xFFFF0000u) * asf(gwv[k] & 0xFFFF0000u));
          s += asf(pr << 16) + asf(pr & 0xFFFF0000u);
        }
        s += dppf<XOR1>(s); s += dppf<XOR2>(s); s += dppf<ROR4>(s); s += dppf<ROR8>(s); s = sw16(s, s); s = sw32(s, s);
        const float gs = rbf(frcp(1.f + fexp(-rbf(s))));
        if (lane == 0) *(LAS f32x2*)(L + L_GATE + (hw & 1) * 8) = f32x2{gs, rbf(1.f - gs)};
        lds_set(L, L_TAG, t32);
      }
    }
    if (hw >= 2) {
      v4u xg[2];
#pragma unroll
      for (int s = 0; s < 2; ++s) xg[s] = *(LAS v4u*)(L + L_X + (n & 1) * 1024 + xc * 128 + s * 64 + g * 16);
      const unsigned hoff = WS_H + ((((n & 1) * 2 + (hw & 1)) * 8 + xc) * 448 + 16 * j + 4 * ((n >> 1) & 3) + g) * 16;
      LAS char* a = L + L_GU + (hw & 1) * 8192 + (8 * g + (n >> 2)) * 128 + (n & 3) * 8;
      wait4<15, 0>(L, L_FS, 9);
      ST(2);
      v4u af[8];
#pragma unroll
      for (int s = 0; s < 2; ++s)
#pragma unroll
        for (int t = 0; t < 4; ++t) af[4 * s + t] = tr2(a + s * 4096 + t * 32, 512);
      SB();
      f32x4 d[4];
#pragma unroll
      for (int t = 0; t < 4; ++t) d[t] = mfma(af[4 + t], xg[1], mfma(af[t], xg[0], f32x4{0, 0, 0, 0}));
      v4u gr;
#pragma unroll
      for (int r = 0; r < 4; ++r) { const float lo = (n & 2) ? d[1][r] : d[0][r], hi = (n & 2) ? d[3][r] : d[2][r]; gr[r] = tagf<12>((n & 4) ? hi : lo, t12); }
      if (n < 8 && has_f) st_sc1(hoff, gr, ws);
      else asm volatile("" ::: "memory");
      lds_set(L, fh, 1);
    }
    ST(3);
    {
      const unsigned qo = WS_Q + ((kvh * 2 + b) * 4 + hw) * 2048 + lane * 16;
      if (hw == 1) {
        wait_until<1>(L, t0, TT);
        v4u tv;
        ld1w(tv, WS_T + (lane & 7) * 128, ws);
        const bool ok = tv.y == t32 && tv.w == t32;
        float d = ok ? (float)(int)(tv.x - t0) : 0.f, k = ok ? 1.f : 0.f;
        d += dppf<XOR1>(d); k += dppf<XOR1>(k); d += dppf<XOR2>(d); k += dppf<XOR2>(k); d += dppf<0x141>(d); k += dppf<0x141>(k);
        const float dm = fminf(fmaxf(d * 16.f * frcp(fmaxf(k, 1.f)), -1600.f), 1600.f);
        lds_set(L, L_DT, (int)dm + (k > 0.f ? TBIAS : 0));
      }
      wait_until<1>(L, t0, TQ);
      v4u q0, q1;
      CNT0(14); CNT0(15);
#pragma unroll 1
      for (int it = 0; it < SPIN_CAP; ++it) { ld2w(q0, q1, qo, qo + 1024, ws); if (!ANY(badtag<8>(q0, t8) | badtag<8>(q1, t8))) break; CNT(14); }
      float v[4];
#pragma unroll
      for (int r = 0; r < 4; ++r) { v[r] = untag<8>(q0[r]) + untag<8>(q1[r]); v[r] = sw32(v[r], v[r]); }
      if (lane < 32) *(LAS v2u*)(L + L_Q + hw * QSTR + lane * 8) = v2u{pk_bf16(v[0], v[1]), pk_bf16(v[2], v[3])};
      LGKM0(); lds_set(L, fh, 2);
      ST(4);
    }
    const int e = lane & 7;
    const unsigned ho = WS_H + (((hw & 1) * 16 + (lane >> 3)) * 448 + 7 * rg + (e < 7 ? e : 6)) * 16;
    if (hw >= 2) {
      wait_until<1>(L, t0, TH);
      DMA_S(" sc1", ho, ws, lbase + L_WD + (hw & 1) * 2048); DMA_S(" sc1", ho + 8 * 448 * 16, ws, lbase + L_WD + (hw & 1) * 2048 + 1024);
    }
    wait4<15, 1>(L, L_FH, 2);
    v4u qf[4], kf[16];
#pragma unroll
    for (int s = 0; s < 4; ++s) qf[s] = *(LAS v4u*)(L + L_Q + (n & 3) * QSTR + s * 64 + g * 16);
    wait4<15, 0>(L, L_FS, 13 + 4 * hw);
    ST(6);
#pragma unroll
    for (int s = 0; s < 4; ++s)
#pragma unroll
      for (int t = 0; t < 4; ++t) kf[s * 4 + t] = *(LAS v4u*)(L + L_K + (4 * hw + t) * 4096 + n * 256 + (((4 * s + g) ^ n) * 16));
    SB();
    float sc[4];
    {
      f32x4 d[4];
#pragma unroll
      for (int t = 0; t < 4; ++t) d[t] = f32x4{0, 0, 0, 0};
#pragma unroll
      for (int s = 0; s < 4; ++s)
#pragma unroll
        for (int t = 0; t < 4; ++t) d[t] = mfma(kf[s * 4 + t], qf[s], d[t]);
      constexpr float SC = 0.08838834764831845f;
      float v[4];
#pragma unroll
      for (int r = 0; r < 4; ++r) { const float lo = (n & 4) ? d[1][r] : d[0][r], hi = (n & 4) ? d[3][r] : d[2][r]; v[r] = (n & 8) ? hi : lo; }
      const unsigned a = pk_bf16(v[0], v[1]), bq = pk_bf16(v[2], v[3]);
      const unsigned a2 = pk_bf16(asf(a << 16) * SC, asf(a & 0xFFFF0000u) * SC), b2 = pk_bf16(asf(bq << 16) * SC, asf(bq & 0xFFFF0000u) * SC);
      sc[0] = asf(a2 << 16); sc[1] = asf(a2 & 0xFFFF0000u); sc[2] = asf(b2 << 16); sc[3] = asf(b2 & 0xFFFF0000u);
    }
    float m = vmax(vmax(sc[0], sc[1]), vmax(sc[2], sc[3]));
    m = vmax(m, dppf<ROR4>(m)); m = vmax(m, dppf<ROR8>(m)); m = x16max(m); m = x32max(m);
    if (lane < 4) *(LAS float*)(L + L_MX + lane * 16 + hw * 4) = m;
    LGKM0(); lds_set(L, fh, 3);
    wait4<15, 0>(L, L_FH, 3);
    { const f32x4 mm = *(LAS f32x4*)(L + L_MX + (n & 3) * 16); m = vmax(vmax(mm[0], mm[1]), vmax(mm[2], mm[3])); }
    {
      float p[4];
#pragma unroll
      for (int r = 0; r < 4; ++r) p[r] = __builtin_amdgcn_exp2f((sc[r] - m) * 1.4426950408889634f);
      *(LAS v2u*)(L + L_P + (n & 3) * PSTR + (64 * hw + 16 * (n >> 2) + 4 * g) * 2) = v2u{pk_bf16(p[0], p[1]), pk_bf16(p[2], p[3])};
      float l = (p[0] + p[1]) + (p[2] + p[3]);
      l += dppf<ROR4>(l); l += dppf<ROR8>(l); l = sw16(l, l); l = sw32(l, l);
      if (lane < 4) *(LAS float*)(L + L_SUM + lane * 16 + hw * 4) = l;
    }
    LGKM0(); lds_set(L, fh, 4);
    ST(7);
    TR_ADDR(hw);
    unsigned ooff = WS_O + (((2 * (4 * kvh + (n & 3)) + (hw >> 1)) * 2 + b) * 16 + sp) * 256 + (hw & 1) * 128 + ((n >> 2) & 1) * 64 + g * 16;
    PIN(ooff);
    wait4<15, 0>(L, L_FH, 4);
    v4u pf[8];
#pragma unroll
    for (int u = 0; u < 8; ++u) pf[u] = *(LAS v4u*)(L + L_P + (n & 3) * PSTR + u * 64 + g * 16);
    if (hw == 2) {
      const f32x4 ls = *(LAS f32x4*)(L + L_SUM + (n & 3) * 16);
      const v4u gr = {(asu(m) & 0xFFFFF000u) | t12, tagf<12>((ls[0] + ls[1]) + (ls[2] + ls[3]), t12), t12, t12};
      if (lane < 4) st_sc1(WS_ST + (((kvh * 4 + lane) * 2 + b) * 16 + sp) * 16, gr, ws);
    }
    if (hw >= 2) {
      if (hw == 2) vmcnt<1>(); else vmcnt<0>();
      v4u h0 = *(LAS v4u*)(L + L_WD + (hw & 1) * 2048 + lane * 16), h1 = *(LAS v4u*)(L + L_WD + (hw & 1) * 2048 + 1024 + lane * 16);
#pragma unroll 1
      for (int it = 0; it < SPIN_CAP && ANY(badtag<12>(h0, t12) | badtag<12>(h1, t12)); ++it) { ld2w(h0, h1, ho, ho + 8 * 448 * 16, ws); CNT(14); }
      float v[4];
#pragma unroll
      for (int r = 0; r < 4; ++r) v[r] = sw32(untag<12>(h0[r]), untag<12>(h1[r]));
      const float a0 = sw16(v[0], v[1]), a1 = sw16(v[2], v[3]);
      const float b0 = a0 + dppf<ROR8>(a0), b1 = a1 + dppf<ROR8>(a1);
      const float gg = rbf((lane & 8) ? b1 : b0);
      const auto sw = __builtin_amdgcn_permlane32_swap(asu(gg), asu(gg), false, false);
      const float hv = rbf(gg * frcp(1.f + fexp(-gg))) * asf(sw[1]);
      if (lane < 32) *(LAS u16*)(L + L_HB + (hw & 1) * 64 + (4 * e + ((lane >> 2) & 2) + (lane >> 4)) * 2) = e < 7 ? (u16)(pk_bf16(hv, hv) & 0xFFFFu) : (u16)0;
      LGKM0(); lds_set(L, fh, 5);
      ST(5);
    }
    f32x4 o0 = {0, 0, 0, 0}, o1 = {0, 0, 0, 0};
#define PVN(u0, nu) do { v4u va[nu], vb[nu]; _Pragma("unroll") for (int u = 0; u < nu; ++u) { va[u] = tr2(L + L_V + ta0 + (u0 + u) * 8192, 1024); vb[u] = tr2(L + L_V + ta1 + (u0 + u) * 8192, 1024); } SB(); \
      _Pragma("unroll") for (int u = 0; u < nu; ++u) { o0 = mfma(va[u], pf[u0 + u], o0); o1 = mfma(vb[u], pf[u0 + u], o1); } } while (0)
    wait4<15, 1>(L, L_FS, 33);
    PVN(0, 4); PIN(o0); PIN(o1);
    ST(8);
    wait4<15, 0>(L, L_FS, 39);
    PVN(4, 3); PIN(o0); PIN(o1);
    wait4<15, 0>(L, L_FS, 41);
    ST(9);
    PVN(7, 1);
    {
      v4u gr;
#pragma unroll
      for (int r = 0; r < 4; ++r) gr[r] = tagf<12>((n & 4) ? o1[r] : o0[r], t12);
      if (n < 8) st_sc1(ooff, gr, ws);
      else asm volatile("" ::: "memory");
    }
    ST(10);
    if (hw < 2) {
      const unsigned so = WS_ST + (((rg >> 1) * 2 + hw) * 16 + 4 * (n & 3) + g) * 16;
      const unsigned oo = WS_O + (rg * 2 + hw) * 4096 + lane * 16;
      wait_until<0>(L, t0, TVE + TO);
      ST(11);
      v4u st, a0, a1, a2, a3;
      asm volatile("global_load_dwordx4 %0, %5, %7 sc1\n\tglobal_load_dwordx4 %1, %6, %7 sc1\n\tglobal_load_dwordx4 %2, %6, %7 offset:1024 sc1\n\t"
                   "global_load_dwordx4 %3, %6, %7 offset:2048 sc1\n\tglobal_load_dwordx4 %4, %6, %7 offset:3072 sc1\n\ts_waitcnt vmcnt(4)"
                   : "=&v"(st), "=&v"(a0), "=&v"(a1), "=&v"(a2), "=&v"(a3) : "v"(so), "v"(oo), "s"(ws) : "memory");
      if (ANY(badtag<12>(st, t12))) {
#pragma unroll 1
        for (int it = 0; it < SPIN_CAP; ++it) { CNT(14); ld1w(st, so, ws); if (!ANY(badtag<12>(st, t12))) break; }
      }
      const float ms = untag<12>(st.x), ls = untag<12>(st.y);
      float M = ms; M = vmax(M, dppf<XOR1>(M)); M = vmax(M, dppf<XOR2>(M)); M = x16max(M); M = x32max(M);
      const float ex = __builtin_amdgcn_exp2f((ms - M) * 1.4426950408889634f);
      float Ls = ex * ls; Ls += dppf<XOR1>(Ls); Ls += dppf<XOR2>(Ls); Ls = sw16(Ls, Ls); Ls = sw32(Ls, Ls);
      const float wv_ = ex * frcp(Ls);
      float wt0 = dppf<0x00>(wv_), wt1 = dppf<0x55>(wv_), wt2 = dppf<0xAA>(wv_), wt3 = dppf<0xFF>(wv_);
      PIN(wt0); PIN(wt1); PIN(wt2); PIN(wt3);
      asm volatile("s_waitcnt vmcnt(0)" : "+v"(a0), "+v"(a1), "+v"(a2), "+v"(a3)::"memory");
      ST(12);
      unsigned oba = L_OB + hw * 128 + (4 * n + (g & 1) * 2 + (g >> 1)) * 2;
#pragma unroll 1
      for (int it = 0; it < SPIN_CAP; ++it) {
        const f32x2 w0 = {wt0, wt0}, w1 = {wt1, wt1}, w2 = {wt2, wt2}, w3 = {wt3, wt3};
#define UT2(a, i) f32x2{untag<12>(a[i]), untag<12>(a[i + 1])}
        const f32x2 lo = __builtin_elementwise_fma(w0, UT2(a0, 0), w1 * UT2(a1, 0)) + __builtin_elementwise_fma(w2, UT2(a2, 0), w3 * UT2(a3, 0));
        const f32x2 hi = __builtin_elementwise_fma(w0, UT2(a0, 2), w1 * UT2(a1, 2)) + __builtin_elementwise_fma(w2, UT2(a2, 2), w3 * UT2(a3, 2));
        const float t = sw16(sw32(lo[0], lo[1]), sw32(hi[0], hi[1]));
        *(volatile LAS u16*)(L + oba) = (u16)(pk_bf16(t, t) & 0xFFFFu);
        if (!ANY(badtag<12>(a0, t12) | badtag<12>(a1, t12) | badtag<12>(a2, t12) | badtag<12>(a3, t12))) break;
        CNT(15);
        ld4w(a0, a1, a2, a3, oo, ws);
      }
      LGKM0();
      ST(13);
      __builtin_amdgcn_s_barrier();
    } else if (hw == 2) {
      __builtin_amdgcn_s_barrier();
    } else {
      unsigned dummy;
      asm volatile("s_nop 4\n\tglobal_load_dword %0, %1, %2\n\ts_waitcnt vmcnt(0)" : "=&v"(dummy) : "v"(0), "s"(out) : "memory");
      unsigned yo = WS_Y + (((xc >> 1) * 64 + lane) * 2 + (xc & 1)) * 512 + j * 16;
      u16* op = out + (xc & 1) * 512 + (xc >> 1) * 128 + 4 * j + (g & 1) * 2 + (g >> 1);
      v4u y;
      PIN(yo); PIN(op);
      __builtin_amdgcn_s_barrier();
      for (int z = 0; z < Y_DELAY; ++z) __builtin_amdgcn_s_sleep(1);
      wait_until<0>(L, t0, TVE + TO + TY);
      ST(11);
#pragma unroll 1
      for (int it = 0; it < SPIN_CAP; ++it) {
        ld1w(y, yo, ws);
        if (!ANY(badtag<12>(y, t12))) break;
        CNT(15);
      }
      ST(12);
      float t = sw16(sw32(untag<12>(y.x), untag<12>(y.y)), sw32(untag<12>(y.z), untag<12>(y.w)));
      t += dppf<XOR1>(t); t += dppf<XOR2>(t); t += dppf<ROR4>(t); t += dppf<ROR8>(t);
      if (n == 0) *op = (u16)(pk_bf16(t, t) & 0xFFFFu);
      if (c == 0 && lane == 0) __hip_atomic_store((GAS unsigned*)ws, t32, __ATOMIC_RELAXED, __HIP_MEMORY_SCOPE_AGENT);
      ST(13);
    }
  } else {
    const int w = wv;
    const unsigned fs = L_FS + 4 * w;
    int issued = 5, landed = 0;
    int due = TK0 + (DITH == 0 ? w * (TAU / 4) : DITH == 1 ? ((w + (j >> 1)) & 3) * (TAU / 4) : DITH == 2 ? (((w + (j >> 1)) & 3) * 8 + xc) * (TAU / 32) : (((w + (j >> 2) + xc) & 3) * 4 + (j & 3)) * (TAU / 16));
    int seen = 0, dt = 0;
#define PUBLISH(vm_) do { if (issued - (vm_) != landed) { landed = issued - (vm_); lds_set(L, fs, landed); } } while (0)
#define PACE() do { \
      const int v0_ = vm_inflight(); PUBLISH(v0_); \
      if (seen < due || v0_ >= WIN) { \
        const int d_ = *(volatile LAS int*)(L + L_DT); \
        _Pragma("unroll 1") for (int it_ = 0; it_ < SPIN_CAP; ++it_) { \
          seen = (int)now16(t0) - dt; \
          const int vm_ = vm_inflight(); PUBLISH(vm_); \
          if (seen >= due && vm_ < WIN) break; } \
        const int dn_ = __builtin_amdgcn_readfirstlane(d_); seen += dt - dn_; dt = dn_; } \
      due += TAU; ++issued; } while (0)
    {
      const char* sb = w == 0 ? (const char*)x : w == 1 ? (const char*)x + 1024 : w == 2 ? (const char*)gw : (const char*)ws;
      DMA_S("", w == 3 ? 0 : lane * 16, sb, lbase + (w < 2 ? L_X : L_P - 2048) + w * 1024);
    }
    {
      const char* base = (const char*)wq + (size_t)(128 * qp + 8 * w) * 8192 + qc * 128;
      const unsigned voff = (lane >> 3) * 8192 + (lane & 7) * 16;
#pragma unroll
      for (int i = 0; i < 4; ++i) DMA_S(NTW, voff, base + (size_t)i * 32 * 8192, lbase + L_WQ + (4 * i + w) * 1024);
    }
    if (w == 0 && lane < 9) *(volatile LAS int*)(L + L_FS + 4 * lane) = lane < 8 ? -64 : 0;
    ST(7);
    LGKM0();
    __builtin_amdgcn_s_barrier();
    {
      const unsigned voff = has_f ? (lane >> 3) * 3584 + (lane & 7) * 16 : 0;
      const size_t off = has_f ? (size_t)(64 * xc + 8 * w) * 3584 + j * 128 : 0;
      const unsigned st = has_f ? 32 * 3584 : 0;
#pragma unroll 1
      for (int i = 0; i < 4; ++i) { PACE(); DMA_S(NTW, voff, (const char*)(i < 2 ? wg : wu) + off + (i & 1) * st, lbase + L_GU + (4 * i + w) * 1024); }
    }
    const int rot = (j >> 2) * 2 + (j & 1);
    const size_t kvoff = ((size_t)(b * 8 + kvh) * 16384 + 12288 + sp * 256) * 256 + w * 1024;
    {
      const char* kb = (const char*)kc + kvoff;
      const unsigned voff = g * 256 + ((n ^ (4 * w + g)) * 16);
#pragma unroll 1
      for (int i = 0; i < 16; ++i) { PACE(); DMA_S(NTS, voff, kb + (((i + rot) & 15) << 12), lbase + L_K + i * 4096 + w * 1024); }
    }
    ST(1);
    {
      const unsigned voff = g * 256 + ((n ^ (2 * (g | ((w >> 1) << 2)))) * 16);
      const char* vb = (const char*)vc + kvoff;
#pragma unroll 1
      for (int i = 0; i < 8; ++i) { if (i >= 16 - NL) due += TAUL - TAU; PACE(); DMA_S(NTS, voff, vb + (((i + rot) & 15) << 12), lbase + L_V + i * 4096 + w * 1024); }
      wait4<3, 0>(L, L_FH, 1);
#pragma unroll 1
      for (int i = 8; i < 16; ++i) { if (i >= 16 - NL) due += TAUL - TAU; PACE(); DMA_S(NTS, voff, vb + (((i + rot) & 15) << 12), lbase + L_V + i * 4096 + w * 1024); }
    }
    ST(2);
    due += GAP;
    wait4<12, 0>(L, L_FH, 5);
    {
      const unsigned vo = g * 1024 + ((n ^ (2 * (g | ((w >> 1) << 2)))) * 16);
      const char* ob = (const char*)wo + (size_t)(64 * rg + 4 * w) * 1024 + cg * 256;
      const char* db = (const char*)wd + (size_t)(28 * rg) * 1024 + cg * 256;
#pragma unroll 1
      for (int i = 0; i < 6; ++i) {
        const char* src = i >= 2 ? ob + (i - 2) * 16384 : db + (i == 0 ? 4 * w : w < 3 ? 16 + 4 * w : 0) * 1024;
        due += TAU2 - TAU; if (i == 6 - HOLD) due = TVE + TO + 32; PACE(); if (i >= 6 - HOLD) due -= TAU2;
        DMA_S(NTW, vo, src, lbase + L_WO + (i >= 2 ? i - 2 : i + 4) * 4096 + w * 1024);
      }
    }
    ST(3);
#pragma unroll 1
    for (int it = 0; it < SPIN_CAP; ++it) { const int vm = vm_inflight(); if (issued - vm != landed) { landed = issued - vm; lds_set(L, fs, landed); } if (landed >= 41) break; }
    ST(4);
    const unsigned t12 = __builtin_amdgcn_readfirstlane(*(volatile LAS unsigned*)(L + L_TAG)) & 0xFFFu;
    TR_ADDR(w);
    unsigned yoff = WS_Y + ((cg * 64 + rg) * 2 + (n & 1)) * 512 + w * 128 + ((n >> 1) & 1) * 64 + g * 16;
    PIN(yoff);
    const f32x2 gt = *(LAS f32x2*)(L + L_GATE + (n & 1) * 8);
    vmcnt<4>(); lds_set(L, fs, 43 > landed ? 43 : landed);
    wait4<15, 0>(L, L_FS, 43);
    f32x4 fy;
    {
      v4u df0 = tr2(L + L_WD + ta0, 1024), df1 = tr2(L + L_WD + ta1, 1024);
      PIN(df0); PIN(df1);
      const v4u hb = *(LAS v4u*)(L + L_HB + (n & 1) * 64 + g * 16);
      const f32x4 f0 = mfma(df0, hb, f32x4{0, 0, 0, 0}), f1 = mfma(df1, hb, f32x4{0, 0, 0, 0});
#pragma unroll
      for (int r = 0; r < 4; ++r) fy[r] = gt[1] * ((n & 2) ? f1[r] : f0[r]);
    }
    PIN(fy);
    vmcnt<0>(); lds_set(L, fs, 47);
    ST(5);
    wait4<15, 0>(L, L_FS, 47);
    v4u wf[4];
    wf[0] = tr2(L + L_WO + ta0, 1024); wf[1] = tr2(L + L_WO + ta1, 1024); wf[2] = tr2(L + L_WO + ta0 + 8192, 1024); wf[3] = tr2(L + L_WO + ta1 + 8192, 1024);
    unsigned oba = L_OB + (n & 1) * 128 + g * 16;
    PIN(fy); PIN(wf[0]); PIN(wf[1]); PIN(wf[2]); PIN(wf[3]); PIN(oba);
    __builtin_amdgcn_s_setprio(3);
    ST(6);
    __builtin_amdgcn_s_barrier();
    asm volatile("" ::: "memory");
    ST(10);
    {
      const v4u b0 = *(LAS v4u*)(L + oba), b1 = *(LAS v4u*)(L + oba + 64);
      f32x4 y0 = mfma(wf[0], b0, f32x4{0, 0, 0, 0}), y1 = mfma(wf[1], b0, f32x4{0, 0, 0, 0});
      y0 = mfma(wf[2], b1, y0); y1 = mfma(wf[3], b1, y1);
      v4u gr;
#pragma unroll
      for (int r = 0; r < 4; ++r) gr[r] = tagf<12>(fmaf(gt[0], (n & 2) ? y1[r] : y0[r], fy[r]), t12);
      if (n < 4) st_sc1(yoff, gr, ws);
      else asm volatile("" ::: "memory");
    }
    ST(11);
  }
}

extern "C" void kernel_launch(void* const* d_in, const int* in_sizes, int n_in, void* d_out, int out_size, void* d_ws, size_t ws_size, hipStream_t stream) {
  (void)hipFuncSetAttribute((const void*)fused_decode, hipFuncAttributeMaxDynamicSharedMemorySize, L_END);
  fused_decode<<<256, 512, L_END, stream>>>((const u16*)d_in[0], (const u16*)d_in[9], (char*)d_ws, (const u16*)d_in[3], (const u16*)d_in[6], (const u16*)d_in[7],
                                            (const u16*)d_in[1], (const u16*)d_in[2], (const u16*)d_in[4], (const u16*)d_in[8], (u16*)d_out);
}
```

```cpp
#define TO 2600
#define TY 1750
#include <hip/hip_runtime.h>
#include <hip/hip_bf16.h>
#include <stdint.h>

#define STAMP 0
#define TAU 256
#define TK0 440
#define TAUL TAU
#define NL 0
#define TAU2 256
#define TT 2900
#define TQ 4300
#define TVE (TK0 + (35 - NL) * TAU + NL * TAUL)
#define WIN 12
#define CODE_PF 25

typedef unsigned short u16;
typedef unsigned long long u64;
typedef unsigned v4u __attribute__((ext_vector_type(4)));
typedef int v4i __attribute__((ext_vector_type(4)));
typedef unsigned v2u __attribute__((ext_vector_type(2)));
typedef short v4s __attribute__((ext_vector_type(4)));
typedef float f32x4 __attribute__((ext_vector_type(4)));
typedef float f32x2 __attribute__((ext_vector_type(2)));
typedef __bf16 v8bf __attribute__((ext_vector_type(8)));
typedef __bf16 v2bf __attribute__((ext_vector_type(2)));
#define GAS __attribute__((address_space(1)))
#define LAS __attribute__((address_space(3)))
#define SB() __builtin_amdgcn_sched_barrier(0)

constexpr unsigned WS_T = 1024;
constexpr unsigned WS_Q = 4096;
constexpr unsigned WS_H = WS_Q + 131072;
constexpr unsigned WS_ST = WS_H + 229376;
constexpr unsigned WS_O = WS_ST + 16384;
constexpr unsigned WS_Y = WS_O + 524288;
constexpr unsigned WS_STAMP = 4u << 20;
constexpr unsigned L_K = 0, L_V = 65536, L_WQ = L_V + 8 * 4096, L_WO = 131072, L_GU = L_WO, L_WD = L_WO + 16384, L_X = L_WD + 8192,
                   L_Q = L_X + 2048, QSTR = 320, L_P = L_Q + 4 * QSTR, PSTR = 576, L_OB = L_P + 4 * PSTR, L_HB = L_OB + 256, L_MX = L_HB + 128,
                   L_SUM = L_MX + 64, L_GATE = L_SUM + 64, L_FS = L_GATE + 16, L_FH = L_FS + 16, L_DT = L_FH + 16, L_TAG = L_DT + 4,
                   L_Z = L_DT + 16, L_END = L_Z + 512;
static_assert(L_END <= 163840, "LDS");
constexpr int SPIN_CAP = 1 << 17;

__device__ __forceinline__ float asf(unsigned u) { return __builtin_bit_cast(float, u); }
__device__ __forceinline__ unsigned asu(float f) { return __builtin_bit_cast(unsigned, f); }
__device__ __forceinline__ unsigned pk_bf16(float lo, float hi) { f32x2 x = {lo, hi}; return __builtin_bit_cast(unsigned, __builtin_convertvector(x, v2bf)); }
__device__ __forceinline__ float rbf(float f) { return asf(pk_bf16(f, f) << 16); }
template <int CTRL> __device__ __forceinline__ float dppf(float x) { return asf(__builtin_amdgcn_mov_dpp(asu(x), CTRL, 0xf, 0xf, true)); }
constexpr int XOR1 = 0xB1, XOR2 = 0x4E, HMIR = 0x141, ROR4 = 0x124, ROR8 = 0x128;
__device__ __forceinline__ float sw16(float a, float b) { auto s = __builtin_amdgcn_permlane16_swap(asu(a), asu(b), false, false); return asf(s[0]) + asf(s[1]); }
__device__ __forceinline__ float sw32(float a, float b) { auto s = __builtin_amdgcn_permlane32_swap(asu(a), asu(b), false, false); return asf(s[0]) + asf(s[1]); }
__device__ __forceinline__ float x16max(float x) { auto s = __builtin_amdgcn_permlane16_swap(asu(x), asu(x), false, false); return fmaxf(asf(s[0]), asf(s[1])); }
__device__ __forceinline__ float x32max(float x) { auto s = __builtin_amdgcn_permlane32_swap(asu(x), asu(x), false, false); return fmaxf(asf(s[0]), asf(s[1])); }
__device__ __forceinline__ float frcp(float x) { return __builtin_amdgcn_rcpf(x); }
__device__ __forceinline__ float fexp(float x) { return __builtin_amdgcn_exp2f(x * 1.4426950408889634f); }
__device__ __forceinline__ f32x4 mfma(v4u a, v4u b, f32x4 c) { return __builtin_amdgcn_mfma_f32_16x16x32_bf16(__builtin_bit_cast(v8bf, a), __builtin_bit_cast(v8bf, b), c, 0, 0, 0); }
__device__ __forceinline__ v2u trread(LAS char* p) { return __builtin_bit_cast(v2u, __builtin_amdgcn_ds_read_tr16_b64_v4i16((LAS v4s*)p)); }
__device__ __forceinline__ v4u tr2(LAS char* p, int d) { v2u a = trread(p), b = trread(p + d); return v4u{a.x, a.y, b.x, b.y}; }
template <int B> __device__ __forceinline__ unsigned tagt(float v, unsigned t) { return (asu(v) & ~((1u << B) - 1)) | t; }
template <int B> __device__ __forceinline__ unsigned tagf(float v, unsigned t) { return ((asu(v) + (1u << (B - 1))) & ~((1u << B) - 1)) | t; }
template <int B> __device__ __forceinline__ float untag(unsigned w) { return asf(w & ~((1u << B) - 1)); }
__device__ __forceinline__ unsigned neq(v4u g, unsigned t) { return __builtin_amdgcn_bitop3_b32(g.x, g.y, t, 0x7E) | __builtin_amdgcn_bitop3_b32(g.z, g.w, t, 0x7E); }
template <int B> __device__ __forceinline__ bool anybad(unsigned x) { return __builtin_amdgcn_ballot_w64((x & ((1u << B) - 1)) != 0) != 0; }
#define PIN(x) asm volatile("" : "+v"(x))
#define CNT(i)
#define CNT0(i)

#define DMA_S(MOD, voff, sbase, ldst) asm volatile("s_mov_b32 m0, %2\n\ts_nop 0\n\tglobal_load_lds_dwordx4 %0, %1" MOD :: "v"(voff), "s"(sbase), "s"(ldst) : "memory")
template <int N> __device__ __forceinline__ void vmcnt() { asm volatile("s_waitcnt vmcnt(%0)" ::"i"(N) : "memory"); }
#define LGKM0() asm volatile("s_waitcnt lgkmcnt(0)" ::: "memory")
__device__ __forceinline__ void ld1w(v4u& a, unsigned o0, const char* sb) {
  asm volatile("global_load_dwordx4 %0, %1, %2 sc1\n\ts_waitcnt vmcnt(0)" : "=&v"(a) : "v"(o0), "s"(sb) : "memory"); }
__device__ __forceinline__ void ld2w(v4u& a, v4u& b, unsigned o0, unsigned o1, const char* sb) {
  asm volatile("global_load_dwordx4 %0, %2, %4 sc1\n\tglobal_load_dwordx4 %1, %3, %4 sc1\n\ts_waitcnt vmcnt(0)" : "=&v"(a), "=&v"(b) : "v"(o0), "v"(o1), "s"(sb) : "memory"); }
__device__ __forceinline__ void ld4w(v4u& a, v4u& b, v4u& c, v4u& d, unsigned o0, const char* sb) {
  asm volatile("global_load_dwordx4 %0, %4, %5 sc1\n\tglobal_load_dwordx4 %1, %4, %5 offset:1024 sc1\n\tglobal_load_dwordx4 %2, %4, %5 offset:2048 sc1\n\t"
               "global_load_dwordx4 %3, %4, %5 offset:3072 sc1\n\ts_waitcnt vmcnt(0)" : "=&v"(a), "=&v"(b), "=&v"(c), "=&v"(d) : "v"(o0), "s"(sb) : "memory"); }
template <u64 MASK> __device__ __forceinline__ void st_sc1(unsigned voff, v4u d, char* sbase) {
  asm volatile("s_mov_b64 exec, %3\n\tglobal_store_dwordx4 %0, %1, %2 sc1\n\ts_mov_b64 exec, -1\n\ts_nop 0" ::"v"(voff), "v"(d), "s"(sbase), "s"(MASK) : "memory"); }

#define ST(i)

__device__ __forceinline__ int now16(unsigned t0) { return (int)(((unsigned)__builtin_amdgcn_s_memrealtime() - t0) * 16u); }
__device__ __forceinline__ int vm_inflight() { unsigned r; asm volatile("s_getreg_b32 %0, hwreg(HW_REG_IB_STS, 0, 4)" : "=s"(r)::"memory"); return (int)r; }
__device__ __forceinline__ int clock_offset(LAS char* L) { return __builtin_amdgcn_readfirstlane(*(volatile LAS int*)(L + L_DT)); }
__device__ __forceinline__ void wait_until(LAS char* L, unsigned t0, int t) {
#pragma unroll 1
  for (int it = 0; it < SPIN_CAP && now16(t0) - clock_offset(L) < t; ++it) __builtin_amdgcn_s_sleep(1);
}
__device__ __forceinline__ void spin_until(unsigned tick) {
#pragma unroll 1
  for (int it = 0; it < SPIN_CAP && (int)((unsigned)__builtin_amdgcn_s_memrealtime() - tick) < 0; ++it) {}
}
__device__ __forceinline__ void lds_set(LAS char* L, unsigned off, unsigned v) {
  asm volatile("s_mov_b64 exec, 1\n\tds_write_b32 %0, %1\n\ts_mov_b64 exec, -1" ::"v"((unsigned)(uintptr_t)(L + off)), "v"(v) : "memory");
}
template <int MASK, int SLEEP> __device__ __forceinline__ void wait4(LAS char* L, unsigned off, int n) {
#pragma unroll 1
  for (int it = 0; it < SPIN_CAP; ++it) {
    const v4i f = *(volatile LAS v4i*)(L + off);
    int m = 0x7fffffff;
    if (MASK & 1) m = min(m, f.x);
    if (MASK & 2) m = min(m, f.y);
    if (MASK & 4) m = min(m, f.z);
    if (MASK & 8) m = min(m, f.w);
    if (__builtin_amdgcn_readfirstlane(m) >= n) break;
    if (SLEEP) __builtin_amdgcn_s_sleep(SLEEP);
  }
  asm volatile("" ::: "memory");
}
#define TR_ADDR(w_) const int fr = (n >> 2) | ((g & 1) << 2); \
    unsigned ta0 = (8 * g + (n >> 2)) * 256 + ((((2 * (w_)) ^ fr) * 2 + ((n & 3) >> 1)) * 16) + (n & 1) * 8; \
    unsigned ta1 = (8 * g + (n >> 2)) * 256 + ((((2 * (w_) + 1) ^ fr) * 2 + ((n & 3) >> 1)) * 16) + (n & 1) * 8; PIN(ta0); PIN(ta1)

constexpr int C_V = 41, C_WD = 43, C_WO = 47;

__global__ void __launch_bounds__(512) __attribute__((amdgpu_waves_per_eu(2, 2)))
fused_decode(const u16* __restrict__ x, const u16* __restrict__ gw, char* __restrict__ ws, const u16* __restrict__ wq, const u16* __restrict__ wg,
             const u16* __restrict__ wu, const u16* __restrict__ kc, const u16* __restrict__ vc, const u16* __restrict__ wo, const u16* __restrict__ wd,
             u16* __restrict__ out) {
  extern __shared__ __attribute__((aligned(16))) char smem[];
  LAS char* L = (LAS char*)smem;
  const unsigned lbase = (unsigned)(uintptr_t)L;
  const int c = blockIdx.x, lane = threadIdx.x & 63, wv = __builtin_amdgcn_readfirstlane(threadIdx.x >> 6);
  const int xc = c & 7, j = c >> 3;
  const int kvh = xc, b = j & 1, sp = j >> 1;
  const int rg = 8 * xc + (j >> 2), cg = j & 3;
  const int qp = xc >> 1, qc = 32 * (xc & 1) + j;
  const bool has_f = j < 28;
  const int sk = has_f ? 0 : 4;
  const int g = lane >> 4, n = lane & 15;
  const int rot = (j >> 2) * 2 + (j & 1);
  const size_t kvoff = ((size_t)(b * 8 + kvh) * 16384 + 12288 + sp * 256) * 256;
  asm volatile("" ::"s"(x), "s"(kc), "s"(vc), "s"(wq), "s"(wo), "s"(wg), "s"(wu), "s"(wd), "s"(gw), "s"(out), "s"(ws));
  const unsigned t0 = (unsigned)__builtin_amdgcn_s_memrealtime();
  ST(0);
  if (wv == 4) {
    const char* pc;
    asm volatile("s_getpc_b64 %0" : "=s"(pc));
    asm volatile("s_mov_b32 m0, %2\n\ts_nop 0\n\tglobal_load_lds_dword %0, %1" ::"v"(min(j, CODE_PF) * 512 + (lane & 7) * 64), "s"((const char*)((uintptr_t)pc & ~(uintptr_t)255)), "s"(lbase + L_OB) : "memory");
  }

  if (wv >= 4) {
    const int hw = wv - 4;
    const unsigned fh = L_FH + 4 * hw;
    __builtin_amdgcn_s_setprio(2);
    if (hw == 1) {
      const char* a = lane == 0 ? (const char*)vc + kvoff : lane == 1 ? (const char*)wo + (size_t)rg * 65536 : lane == 2 ? (const char*)wd + (size_t)rg * 28672
                    : lane == 3 ? (const char*)out : (const char*)kc + kvoff;
      asm volatile("s_mov_b32 m0, %1\n\ts_nop 0\n\tglobal_load_lds_dword %0, off" ::"v"(a), "s"(lbase + L_OB) : "memory");
    }
    __builtin_amdgcn_s_barrier();
    wait4<15, 0>(L, L_FS, 1);
    const unsigned t32 = __builtin_amdgcn_readfirstlane(*(LAS unsigned*)(L + L_P + 1024)) + 1, t12 = t32 & 0xFFFu, t8 = t32 & 0xFFu;
    ST(1);
    if (hw < 2) {
      v4u xq[4];
#pragma unroll
      for (int s = 0; s < 4; ++s) xq[s] = *(LAS v4u*)(L + L_X + (n & 1) * 1024 + qp * 256 + s * 64 + g * 16);
      const int col = 64 * qc + 32 * hw + 16 * ((n >> 1) & 1) + 4 * g;
      const unsigned qoff = WS_Q + (((((col >> 9) * 2 + (n & 1)) * 4 + ((col >> 7) & 3)) * 4 + qp) * 128 + (col & 127)) * 4;
      LAS char* a = L + L_WQ + (8 * g + (n >> 2)) * 128 + hw * 64 + (n & 3) * 8;
      wait4<15, 0>(L, L_FS, 5);
      ST(2);
      v4u af[8];
#pragma unroll
      for (int s = 0; s < 4; ++s) { af[2 * s] = tr2(a + s * 4096, 512); af[2 * s + 1] = tr2(a + s * 4096 + 32, 512); }
      SB();
      f32x4 d0 = {0, 0, 0, 0}, d1 = {0, 0, 0, 0};
#pragma unroll
      for (int s = 0; s < 4; ++s) { d0 = mfma(af[2 * s], xq[s], d0); d1 = mfma(af[2 * s + 1], xq[s], d1); }
      v4u gr;
#pragma unroll
      for (int r = 0; r < 4; ++r) gr[r] = tagf<8>((n & 2) ? d1[r] : d0[r], t8);
      st_sc1<0x000F000F000F000Full>(qoff, gr, ws);
    } else {
      if (hw == 2 && c < 8) st_sc1<1>(WS_T + c * 128, v4u{t0, t32, t0, t32}, ws);
      {
        const v4u gwv = *(LAS v4u*)(L + L_P + lane * 16), xv = *(LAS v4u*)(L + L_X + (hw & 1) * 1024 + lane * 16);
        float s = 0.f;
#pragma unroll
        for (int k = 0; k < 4; ++k) {
          const unsigned pr = pk_bf16(asf(xv[k] << 16) * asf(gwv[k] << 16), asf(xv[k] & 0xFFFF0000u) * asf(gwv[k] & 0xFFFF0000u));
          s += asf(pr << 16) + asf(pr & 0xFFFF0000u);
        }
        s += dppf<XOR1>(s); s += dppf<XOR2>(s); s += dppf<ROR4>(s); s += dppf<ROR8>(s); s = sw16(s, s); s = sw32(s, s);
        const float gs = rbf(frcp(1.f + fexp(-rbf(s))));
        if (lane == 0) *(LAS f32x2*)(L + L_GATE + (hw & 1) * 8) = f32x2{gs, rbf(1.f - gs)};
        lds_set(L, L_TAG, t32);
      }
      v4u xg[2];
#pragma unroll
      for (int s = 0; s < 2; ++s) xg[s] = *(LAS v4u*)(L + L_X + (n & 1) * 1024 + xc * 128 + s * 64 + g * 16);
      const unsigned hoff = WS_H + ((((n & 1) * 2 + (hw & 1)) * 8 + xc) * 448 + 16 * j + 4 * ((n >> 1) & 3) + g) * 16;
      LAS char* a = L + L_GU + (hw & 1) * 8192 + (8 * g + (n >> 2)) * 128 + (n & 3) * 8;
      wait4<15, 0>(L, L_FS, 9 - sk);
      ST(2);
      v4u af[8];
#pragma unroll
      for (int s = 0; s < 2; ++s)
#pragma unroll
        for (int t = 0; t < 4; ++t) af[4 * s + t] = tr2(a + s * 4096 + t * 32, 512);
      SB();
      f32x4 d[4];
#pragma unroll
      for (int t = 0; t < 4; ++t) d[t] = mfma(af[4 + t], xg[1], mfma(af[t], xg[0], f32x4{0, 0, 0, 0}));
      v4u gr;
#pragma unroll
      for (int r = 0; r < 4; ++r) { const float lo = (n & 2) ? d[1][r] : d[0][r], hi = (n & 2) ? d[3][r] : d[2][r]; gr[r] = tagf<12>((n & 4) ? hi : lo, t12); }
      PIN(gr);
      if (has_f) st_sc1<0x00FF00FF00FF00FFull>(hoff, gr, ws);
    }
    lds_set(L, fh, 1);
    ST(3);
    if (hw == 1) {
      wait_until(L, t0, TT);
      v4u tv;
      ld1w(tv, WS_T + (lane & 7) * 128, ws);
      const bool ok = tv.y == t32 && tv.w == t32;
      float d = ok ? (float)(int)(tv.x - t0) : 0.f, k = ok ? 1.f : 0.f;
      d += dppf<XOR1>(d); k += dppf<XOR1>(k); d += dppf<XOR2>(d); k += dppf<XOR2>(k); d += dppf<HMIR>(d); k += dppf<HMIR>(k);
      lds_set(L, L_DT, (int)fminf(fmaxf(d * 16.f * frcp(fmaxf(k, 1.f)), -1600.f), 1600.f));
    }
    {
      const unsigned qo = WS_Q + ((kvh * 2 + b) * 4 + hw) * 2048 + lane * 16;
      wait_until(L, t0, TQ);
      v4u q0, q1;
      CNT0(14); CNT0(15);
#pragma unroll 1
      for (int it = 0; it < SPIN_CAP; ++it) { ld2w(q0, q1, qo, qo + 1024, ws); if (!anybad<8>(neq(q0, t8) | neq(q1, t8))) break; CNT(14); }
      float v[4];
#pragma unroll
      for (int r = 0; r < 4; ++r) { v[r] = untag<8>(q0[r]) + untag<8>(q1[r]); v[r] = sw32(v[r], v[r]); }
      if (lane < 32) *(LAS v2u*)(L + L_Q + hw * QSTR + lane * 8) = v2u{pk_bf16(v[0], v[1]), pk_bf16(v[2], v[3])};
      LGKM0(); lds_set(L, fh, 2);
      ST(4);
    }
    wait4<15, 1>(L, L_FH, 2);
    v4u qf[4], kf[16];
#pragma unroll
    for (int s = 0; s < 4; ++s) qf[s] = *(LAS v4u*)(L + L_Q + (n & 3) * QSTR + s * 64 + g * 16);
    wait4<15, 0>(L, L_FS, 13 + 4 * hw - sk);
    ST(6);
#pragma unroll
    for (int s = 0; s < 4; ++s)
#pragma unroll
      for (int t = 0; t < 4; ++t) kf[s * 4 + t] = *(LAS v4u*)(L + L_K + (4 * hw + t) * 4096 + n * 256 + (((4 * s + g) ^ n) * 16));
    SB();
    float sc[4];
    {
      f32x4 d[4];
#pragma unroll
      for (int t = 0; t < 4; ++t) d[t] = f32x4{0, 0, 0, 0};
#pragma unroll
      for (int s = 0; s < 4; ++s)
#pragma unroll
        for (int t = 0; t < 4; ++t) d[t] = mfma(kf[s * 4 + t], qf[s], d[t]);
      constexpr float SC = 0.08838834764831845f;
      float v[4];
#pragma unroll
      for (int r = 0; r < 4; ++r) { const float lo = (n & 4) ? d[1][r] : d[0][r], hi = (n & 4) ? d[3][r] : d[2][r]; v[r] = (n & 8) ? hi : lo; }
      const unsigned a = pk_bf16(v[0], v[1]), bq = pk_bf16(v[2], v[3]);
      const unsigned a2 = pk_bf16(asf(a << 16) * SC, asf(a & 0xFFFF0000u) * SC), b2 = pk_bf16(asf(bq << 16) * SC, asf(bq & 0xFFFF0000u) * SC);
      sc[0] = asf(a2 << 16); sc[1] = asf(a2 & 0xFFFF0000u); sc[2] = asf(b2 << 16); sc[3] = asf(b2 & 0xFFFF0000u);
    }
    float m = fmaxf(fmaxf(sc[0], sc[1]), fmaxf(sc[2], sc[3]));
    m = fmaxf(m, dppf<ROR4>(m)); m = fmaxf(m, dppf<ROR8>(m)); m = x16max(m); m = x32max(m);
    if (lane < 4) *(LAS float*)(L + L_MX + lane * 16 + hw * 4) = m;
    LGKM0(); lds_set(L, fh, 3);
    wait4<15, 0>(L, L_FH, 3);
    const int e = lane & 7;
    const unsigned ho = WS_H + (((hw & 1) * 16 + (lane >> 3)) * 448 + 7 * rg + (e < 7 ? e : 6)) * 16;
    if (hw >= 2) { DMA_S(" sc1", ho, ws, lbase + L_K + hw * 2048); DMA_S(" sc1", ho + 8 * 448 * 16, ws, lbase + L_K + hw * 2048 + 1024); }
    { const f32x4 mm = *(LAS f32x4*)(L + L_MX + (n & 3) * 16); m = fmaxf(fmaxf(mm[0], mm[1]), fmaxf(mm[2], mm[3])); }
    {
      float p[4];
#pragma unroll
      for (int r = 0; r < 4; ++r) p[r] = fexp(sc[r] - m);
      *(LAS v2u*)(L + L_P + (n & 3) * PSTR + (64 * hw + 16 * (n >> 2) + 4 * g) * 2) = v2u{pk_bf16(p[0], p[1]), pk_bf16(p[2], p[3])};
      float l = (p[0] + p[1]) + (p[2] + p[3]);
      l += dppf<ROR4>(l); l += dppf<ROR8>(l); l = sw16(l, l); l = sw32(l, l);
      if (lane < 4) *(LAS float*)(L + L_SUM + lane * 16 + hw * 4) = l;
    }
    LGKM0(); lds_set(L, fh, 4);
    ST(7);
    TR_ADDR(hw);
    unsigned ooff = WS_O + (((2 * (4 * kvh + (n & 3)) + (hw >> 1)) * 2 + b) * 16 + sp) * 256 + (hw & 1) * 128 + ((n >> 2) & 1) * 64 + g * 16;
    unsigned tv = t12;
    PIN(ooff); PIN(tv);
    const int tabs = t0 + ((clock_offset(L) + TVE) >> 4);
    wait4<15, 0>(L, L_FH, 4);
    v4u pa[8], pb[8];
    {
      const unsigned pp = L_P + (n & 3) * PSTR + g * 16, aa = n < 4 ? pp : L_Z, ab = (n >> 2) == 1 ? pp : L_Z;
#pragma unroll
      for (int u = 0; u < 8; ++u) { pa[u] = *(LAS v4u*)(L + aa + u * 64); pb[u] = *(LAS v4u*)(L + ab + u * 64); }
    }
    if (hw == 2) {
      const f32x4 ls = *(LAS f32x4*)(L + L_SUM + (n & 3) * 16);
      st_sc1<0xF>(WS_ST + (((kvh * 4 + (n & 3)) * 2 + b) * 16 + sp) * 16, v4u{tagt<12>(m, t12), tagf<12>((ls[0] + ls[1]) + (ls[2] + ls[3]), t12), t12, t12}, ws);
    }
    f32x4 o = {0, 0, 0, 0};
#define PVN(u0, nu) do { v4u va[nu], vb[nu]; _Pragma("unroll") for (int u = 0; u < nu; ++u) { va[u] = tr2(L + L_V + ta0 + (u0 + u) * 8192, 1024); vb[u] = tr2(L + L_V + ta1 + (u0 + u) * 8192, 1024); } SB(); \
      _Pragma("unroll") for (int u = 0; u < nu; ++u) { o = mfma(va[u], pa[u0 + u], o); o = mfma(vb[u], pb[u0 + u], o); } } while (0)
    wait4<15, 1>(L, L_FS, 33 - sk);
    PVN(0, 4); PIN(o);
    ST(8);
    if (hw >= 2) {
      if (hw == 2) vmcnt<1>(); else vmcnt<0>();
      v4u h0 = *(LAS v4u*)(L + L_K + hw * 2048 + lane * 16), h1 = *(LAS v4u*)(L + L_K + hw * 2048 + 1024 + lane * 16);
#pragma unroll 1
      for (int it = 0; it < SPIN_CAP && anybad<12>(neq(h0, t12) | neq(h1, t12)); ++it) { ld2w(h0, h1, ho, ho + 8 * 448 * 16, ws); CNT(14); }
      float v[4];
#pragma unroll
      for (int r = 0; r < 4; ++r) v[r] = sw32(untag<12>(h0[r]), untag<12>(h1[r]));
      const float a0 = sw16(v[0], v[1]), a1 = sw16(v[2], v[3]);
      const float b0 = a0 + dppf<ROR8>(a0), b1 = a1 + dppf<ROR8>(a1);
      const float gg = rbf((lane & 8) ? b1 : b0);
      const auto sw = __builtin_amdgcn_permlane32_swap(asu(gg), asu(gg), false, false);
      const float hv = rbf(gg * frcp(1.f + fexp(-gg))) * asf(sw[1]);
      if (lane < 32) *(LAS u16*)(L + L_HB + (hw & 1) * 64 + (4 * e + ((lane >> 2) & 2) + (lane >> 4)) * 2) = e < 7 ? (u16)(pk_bf16(hv, hv) & 0xFFFFu) : (u16)0;
      LGKM0(); lds_set(L, fh, 5);
      ST(5);
    }
    wait4<15, 0>(L, L_FS, 39 - sk);
    PVN(4, 3); PIN(o);
    const unsigned so = WS_ST + (((rg >> 1) * 2 + (hw & 1)) * 16 + 4 * (n & 3) + g) * 16;
    wait4<15, 0>(L, L_FS, C_V - sk);
    ST(9);
    PVN(7, 1);
    st_sc1<0x00FF00FF00FF00FFull>(ooff, v4u{tagt<12>(o[0], tv), tagt<12>(o[1], tv), tagt<12>(o[2], tv), tagt<12>(o[3], tv)}, ws);
    ST(10);
    if (hw < 2) {
      unsigned oo = WS_O + (rg * 2 + hw) * 4096 + lane * 16, oba = L_OB + hw * 128 + (4 * n + (g & 1) * 2 + (g >> 1)) * 2;
      DMA_S(" sc1", so, ws, lbase + L_K + hw * 1024);
      const float gate = *(LAS float*)(L + L_GATE + hw * 8);
      PIN(oo); PIN(oba);
      spin_until(tabs + (TO >> 4));
      ST(11);
      v4u a0, a1, a2, a3;
      asm volatile("global_load_dwordx4 %0, %4, %5 sc1\n\tglobal_load_dwordx4 %1, %4, %5 offset:1024 sc1\n\tglobal_load_dwordx4 %2, %4, %5 offset:2048 sc1\n\t"
                   "global_load_dwordx4 %3, %4, %5 offset:3072 sc1\n\ts_waitcnt vmcnt(4)" : "=&v"(a0), "=&v"(a1), "=&v"(a2), "=&v"(a3) : "v"(oo), "s"(ws) : "memory");
      v4u st = *(LAS v4u*)(L + L_K + hw * 1024 + lane * 16);
      if (anybad<12>(neq(st, t12))) {
#pragma unroll 1
        for (int it = 0; it < SPIN_CAP; ++it) { CNT(14); ld1w(st, so, ws); if (!anybad<12>(neq(st, t12))) break; }
      }
      const float ms = untag<12>(st.x), ls = untag<12>(st.y);
      float M = ms; M = fmaxf(M, dppf<XOR1>(M)); M = fmaxf(M, dppf<XOR2>(M)); M = x16max(M); M = x32max(M);
      const float ex = fexp(ms - M);
      float Ls = ex * ls; Ls += dppf<XOR1>(Ls); Ls += dppf<XOR2>(Ls); Ls = sw16(Ls, Ls); Ls = sw32(Ls, Ls);
      const float wv_ = gate * ex * frcp(Ls);
      const float wt0 = dppf<0x00>(wv_), wt1 = dppf<0x55>(wv_), wt2 = dppf<0xAA>(wv_), wt3 = dppf<0xFF>(wv_);
      f32x2 w0 = {wt0, wt0}, w1 = {wt1, wt1}, w2 = {wt2, wt2}, w3 = {wt3, wt3};
      PIN(w0); PIN(w1); PIN(w2); PIN(w3);
      asm volatile("s_waitcnt vmcnt(0)" : "+v"(a0), "+v"(a1), "+v"(a2), "+v"(a3)::"memory");
      ST(12);
#pragma unroll 1
      for (int it = 0; it < SPIN_CAP; ++it) {
#define UT2(a, i) f32x2{untag<12>(a[i]), untag<12>(a[i + 1])}
        const f32x2 lo = __builtin_elementwise_fma(w0, UT2(a0, 0), w1 * UT2(a1, 0)) + __builtin_elementwise_fma(w2, UT2(a2, 0), w3 * UT2(a3, 0));
        const f32x2 hi = __builtin_elementwise_fma(w0, UT2(a0, 2), w1 * UT2(a1, 2)) + __builtin_elementwise_fma(w2, UT2(a2, 2), w3 * UT2(a3, 2));
        const float t = sw16(sw32(lo[0], lo[1]), sw32(hi[0], hi[1]));
        *(volatile LAS u16*)(L + oba) = (u16)(pk_bf16(t, t) & 0xFFFFu);
        SB();
        if (!anybad<12>(neq(a0, t12) | neq(a1, t12) | neq(a2, t12) | neq(a3, t12))) break;
        CNT(15);
        ld4w(a0, a1, a2, a3, oo, ws);
      }
      LGKM0();
      ST(13);
      __builtin_amdgcn_s_barrier();
    } else if (hw == 2) {
      __builtin_amdgcn_s_barrier();
    } else {
      unsigned yo = WS_Y + (((xc >> 1) * 64 + lane) * 2 + (xc & 1)) * 512 + j * 16;
      unsigned op = ((xc & 1) * 512 + (xc >> 1) * 128 + 4 * j + (g & 1) * 2 + (g >> 1)) * 2;
      PIN(yo); PIN(op);
      __builtin_amdgcn_s_barrier();
      __builtin_amdgcn_s_sleep(4);
      spin_until(tabs + ((TO + TY) >> 4));
      ST(11);
      float t;
#pragma unroll 1
      for (int it = 0; it < SPIN_CAP; ++it) {
        v4u y;
        ld1w(y, yo, ws);
        ST(12);
        t = sw16(sw32(untag<12>(y.x), untag<12>(y.y)), sw32(untag<12>(y.z), untag<12>(y.w)));
        t += dppf<XOR1>(t); t += dppf<XOR2>(t); t += dppf<ROR4>(t); t += dppf<ROR8>(t);
        if (!anybad<12>(neq(y, t12))) break;
        CNT(15);
      }
      asm volatile("s_mov_b64 exec, %3\n\tglobal_store_short %0, %1, %2\n\ts_mov_b64 exec, -1" ::"v"(op), "v"(pk_bf16(t, t)), "s"(out), "s"(0x0001000100010001ull) : "memory");
      if (c == 0) asm volatile("s_mov_b64 exec, 1\n\tglobal_store_dword %0, %1, %2 sc1\n\ts_mov_b64 exec, -1" ::"v"(0), "v"(t32), "s"(ws) : "memory");
      ST(13);
    }
  } else {
    const int w = wv;
    const unsigned fs = L_FS + 4 * w;
    int issued = 5, landed = 0, due = TK0 + w * (TAU / 4), seen = 0, dt = 0;
#define PUBLISH(vm_) do { if (issued - (vm_) != landed) { landed = issued - (vm_); lds_set(L, fs, landed); } } while (0)
#define PACE() do { \
      const int v0_ = vm_inflight(); PUBLISH(v0_); \
      if (seen < due || v0_ >= WIN) { \
        const int d_ = *(volatile LAS int*)(L + L_DT); \
        _Pragma("unroll 1") for (int it_ = 0; it_ < SPIN_CAP; ++it_) { \
          seen = now16(t0) - dt; \
          const int vm_ = vm_inflight(); PUBLISH(vm_); \
          if (seen >= due && vm_ < WIN) break; } \
        const int dn_ = __builtin_amdgcn_readfirstlane(d_); seen += dt - dn_; dt = dn_; } \
      due += TAU; ++issued; } while (0)
    {
      const char* sb = w == 0 ? (const char*)x : w == 1 ? (const char*)x + 1024 : w == 2 ? (const char*)gw : (const char*)ws;
      DMA_S("", w == 3 ? 0 : lane * 16, sb, lbase + (w < 2 ? L_X : L_P - 2048) + w * 1024);
    }
    {
      const char* base = (const char*)wq + (size_t)(128 * qp + 8 * w) * 8192 + qc * 128;
      const unsigned voff = (lane >> 3) * 8192 + (lane & 7) * 16;
#pragma unroll
      for (int i = 0; i < 4; ++i) DMA_S(" nt", voff, base + (size_t)i * 32 * 8192, lbase + L_WQ + (4 * i + w) * 1024);
    }
    if (w == 0 && lane < 9) *(volatile LAS int*)(L + L_FS + 4 * lane) = lane < 8 ? -64 : 0;
    if (w == 1) *(volatile LAS v2u*)(L + L_Z + 8 * lane) = v2u{0, 0};
    ST(7);
    LGKM0();
    __builtin_amdgcn_s_barrier();
    if (has_f) {
      const unsigned voff = (lane >> 3) * 3584 + (lane & 7) * 16;
      const size_t off = (size_t)(64 * xc + 8 * w) * 3584 + j * 128;
#pragma unroll 1
      for (int i = 0; i < 4; ++i) { PACE(); DMA_S(" nt", voff, (const char*)(i < 2 ? wg : wu) + off + (i & 1) * (32 * 3584), lbase + L_GU + (4 * i + w) * 1024); }
    } else {
      due += 4 * TAU;
    }
    {
      const char* kb = (const char*)kc + kvoff + w * 1024;
      const unsigned voff = g * 256 + ((n ^ (4 * w + g)) * 16);
#pragma unroll 1
      for (int i = 0; i < 16; ++i) { PACE(); DMA_S(" nt", voff, kb + (((i + rot) & 15) << 12), lbase + L_K + i * 4096 + w * 1024); }
    }
    ST(1);
    {
      const unsigned voff = g * 256 + ((n ^ (2 * (g | ((w >> 1) << 2)))) * 16);
      const char* vb = (const char*)vc + kvoff + w * 1024;
#pragma unroll 1
      for (int i = 0; i < 16; ++i) {
        if (i == 8) wait4<3, 0>(L, L_FH, 1);
        if (i >= 16 - NL) due += TAUL - TAU;
        PACE(); DMA_S(" nt", voff, vb + (((i + rot) & 15) << 12), lbase + L_V + i * 4096 + w * 1024);
      }
    }
    ST(2);
    wait4<12, 0>(L, L_FH, 1);
    {
      const unsigned vo = g * 1024 + ((n ^ (2 * (g | ((w >> 1) << 2)))) * 16);
      const char* ob = (const char*)wo + (size_t)(64 * rg + 4 * w) * 1024 + cg * 256;
      const char* db = (const char*)wd + (size_t)(28 * rg) * 1024 + cg * 256;
#pragma unroll 1
      for (int i = 0; i < 6; ++i) {
        const char* src = i >= 2 ? ob + (i - 2) * 16384 : db + (i == 0 ? 4 * w : w < 3 ? 16 + 4 * w : 0) * 1024;
        due += TAU2 - TAU; PACE();
        DMA_S(" nt", vo, src, lbase + L_WO + (i >= 2 ? i - 2 : i + 4) * 4096 + w * 1024);
      }
    }
    ST(3);
#pragma unroll 1
    for (int it = 0; it < SPIN_CAP && landed < C_WD - sk; ++it) { const int vm = vm_inflight(); PUBLISH(vm); }
    ST(4);
    unsigned t12 = *(volatile LAS unsigned*)(L + L_TAG) & 0xFFFu;
    TR_ADDR(w);
    unsigned yoff = WS_Y + ((cg * 64 + rg) * 2 + (n & 1)) * 512 + w * 128 + ((n >> 1) & 1) * 64 + g * 16;
    unsigned oa = lbase + (n < 2 ? L_OB + n * 128 + g * 16 : L_Z), ob = lbase + ((n >> 1) == 1 ? L_OB + (n & 1) * 128 + g * 16 : L_Z);
    const float g1 = *(LAS float*)(L + L_GATE + (n & 1) * 8 + 4);
    wait4<12, 0>(L, L_FH, 5);
    const v4u ha = *(LAS v4u*)(L + (n < 2 ? L_HB + n * 64 + g * 16 : L_Z)), hb = *(LAS v4u*)(L + ((n >> 1) == 1 ? L_HB + (n & 1) * 64 + g * 16 : L_Z));
    wait4<15, 0>(L, L_FS, C_WD - sk);
    f32x4 y = mfma(tr2(L + L_WD + ta1, 1024), hb, mfma(tr2(L + L_WD + ta0, 1024), ha, f32x4{0, 0, 0, 0})) * g1;
    PIN(y);
    vmcnt<0>(); lds_set(L, fs, C_WO - sk);
    ST(5);
    wait4<15, 0>(L, L_FS, C_WO - sk);
    v4u wf[4];
    wf[0] = tr2(L + L_WO + ta0, 1024); wf[1] = tr2(L + L_WO + ta1, 1024); wf[2] = tr2(L + L_WO + ta0 + 8192, 1024); wf[3] = tr2(L + L_WO + ta1 + 8192, 1024);
    PIN(wf[0]); PIN(wf[1]); PIN(wf[2]); PIN(wf[3]); PIN(oa); PIN(ob); PIN(yoff); PIN(t12);
    __builtin_amdgcn_s_setprio(3);
    ST(6);
    __builtin_amdgcn_s_barrier();
    asm volatile("" ::: "memory");
    ST(10);
    {
      const v4u a0 = *(LAS v4u*)(uintptr_t)oa, b0 = *(LAS v4u*)(uintptr_t)ob, a1 = *(LAS v4u*)(uintptr_t)(oa + 64), b1 = *(LAS v4u*)(uintptr_t)(ob + 64);
      SB();
      y = mfma(wf[0], a0, y); y = mfma(wf[1], b0, y); y = mfma(wf[2], a1, y); y = mfma(wf[3], b1, y);
      st_sc1<0x000F000F000F000Full>(yoff, v4u{tagt<12>(y[0], t12), tagt<12>(y[1], t12), tagt<12>(y[2], t12), tagt<12>(y[3], t12)}, ws);
    }
    ST(11);
  }
}

extern "C" void kernel_launch(void* const* d_in, const int* in_sizes, int n_in, void* d_out, int out_size, void* d_ws, size_t ws_size, hipStream_t stream) {
  (void)hipFuncSetAttribute((const void*)fused_decode, hipFuncAttributeMaxDynamicSharedMemorySize, L_END);
  fused_decode<<<256, 512, L_END, stream>>>((const u16*)d_in[0], (const u16*)d_in[9], (char*)d_ws, (const u16*)d_in[3], (const u16*)d_in[6], (const u16*)d_in[7],
                                            (const u16*)d_in[1], (const u16*)d_in[2], (const u16*)d_in[4], (const u16*)d_in[8], (u16*)d_out);
}
```

```cpp
#define PROBE 2
#define PWG 0
#define PWAVE 4
#define SMASK 5
#define TO 2900
#define TY 1650
#include <hip/hip_runtime.h>
#include <hip/hip_bf16.h>
#include <stdint.h>

#define STAMP 0
#define TAU 256
#define TK0 440
#define TAUL TAU
#define NL 0
#define TAU2 256
#define TT 2900
#define TQ 4300
#define TVE (TK0 + (35 - NL) * TAU + NL * TAUL)
#define WIN 12
#define CODE_PF 25

typedef unsigned short u16;
typedef unsigned long long u64;
typedef unsigned v4u __attribute__((ext_vector_type(4)));
typedef int v4i __attribute__((ext_vector_type(4)));
typedef unsigned v2u __attribute__((ext_vector_type(2)));
typedef short v4s __attribute__((ext_vector_type(4)));
typedef float f32x4 __attribute__((ext_vector_type(4)));
typedef float f32x2 __attribute__((ext_vector_type(2)));
typedef __bf16 v8bf __attribute__((ext_vector_type(8)));
typedef __bf16 v2bf __attribute__((ext_vector_type(2)));
#define GAS __attribute__((address_space(1)))
#define LAS __attribute__((address_space(3)))
#define SB() __builtin_amdgcn_sched_barrier(0)

constexpr unsigned WS_T = 1024;
constexpr unsigned WS_Q = 4096;
constexpr unsigned WS_H = WS_Q + 131072;
constexpr unsigned WS_ST = WS_H + 229376;
constexpr unsigned WS_O = WS_ST + 16384;
constexpr unsigned WS_Y = WS_O + 524288;
constexpr unsigned WS_STAMP = 4u << 20;
constexpr unsigned L_K = 0, L_V = 65536, L_WQ = L_V + 8 * 4096, L_WO = 131072, L_GU = L_WO, L_WD = L_WO + 16384, L_X = L_WD + 8192,
                   L_Q = L_X + 2048, QSTR = 320, L_P = L_Q + 4 * QSTR, PSTR = 576, L_OB = L_P + 4 * PSTR, L_HB = L_OB + 256, L_MX = L_HB + 128,
                   L_SUM = L_MX + 64, L_GATE = L_SUM + 64, L_FS = L_GATE + 16, L_FH = L_FS + 16, L_DT = L_FH + 16, L_TAG = L_DT + 4,
                   L_Z = L_DT + 16, L_END = L_Z + 512;
static_assert(L_END <= 163840, "LDS");
constexpr int SPIN_CAP = 1 << 17;

__device__ __forceinline__ float asf(unsigned u) { return __builtin_bit_cast(float, u); }
__device__ __forceinline__ unsigned asu(float f) { return __builtin_bit_cast(unsigned, f); }
__device__ __forceinline__ unsigned pk_bf16(float lo, float hi) { f32x2 x = {lo, hi}; return __builtin_bit_cast(unsigned, __builtin_convertvector(x, v2bf)); }
__device__ __forceinline__ float rbf(float f) { return asf(pk_bf16(f, f) << 16); }
template <int CTRL> __device__ __forceinline__ float dppf(float x) { return asf(__builtin_amdgcn_mov_dpp(asu(x), CTRL, 0xf, 0xf, true)); }
constexpr int XOR1 = 0xB1, XOR2 = 0x4E, HMIR = 0x141, ROR4 = 0x124, ROR8 = 0x128;
__device__ __forceinline__ float sw16(float a, float b) { auto s = __builtin_amdgcn_permlane16_swap(asu(a), asu(b), false, false); return asf(s[0]) + asf(s[1]); }
__device__ __forceinline__ float sw32(float a, float b) { auto s = __builtin_amdgcn_permlane32_swap(asu(a), asu(b), false, false); return asf(s[0]) + asf(s[1]); }
__device__ __forceinline__ float x16max(float x) { auto s = __builtin_amdgcn_permlane16_swap(asu(x), asu(x), false, false); return fmaxf(asf(s[0]), asf(s[1])); }
__device__ __forceinline__ float x32max(float x) { auto s = __builtin_amdgcn_permlane32_swap(asu(x), asu(x), false, false); return fmaxf(asf(s[0]), asf(s[1])); }
__device__ __forceinline__ float frcp(float x) { return __builtin_amdgcn_rcpf(x); }
__device__ __forceinline__ float fexp(float x) { return __builtin_amdgcn_exp2f(x * 1.4426950408889634f); }
__device__ __forceinline__ f32x4 mfma(v4u a, v4u b, f32x4 c) { return __builtin_amdgcn_mfma_f32_16x16x32_bf16(__builtin_bit_cast(v8bf, a), __builtin_bit_cast(v8bf, b), c, 0, 0, 0); }
__device__ __forceinline__ v2u trread(LAS char* p) { return __builtin_bit_cast(v2u, __builtin_amdgcn_ds_read_tr16_b64_v4i16((LAS v4s*)p)); }
__device__ __forceinline__ v4u tr2(LAS char* p, int d) { v2u a = trread(p), b = trread(p + d); return v4u{a.x, a.y, b.x, b.y}; }
template <int B> __device__ __forceinline__ unsigned tagt(float v, unsigned t) { return (asu(v) & ~((1u << B) - 1)) | t; }
template <int B> __device__ __forceinline__ unsigned tagf(float v, unsigned t) { return ((asu(v) + (1u << (B - 1))) & ~((1u << B) - 1)) | t; }
template <int B> __device__ __forceinline__ float untag(unsigned w) { return asf(w & ~((1u << B) - 1)); }
__device__ __forceinline__ unsigned neq(v4u g, unsigned t) { return __builtin_amdgcn_bitop3_b32(g.x, g.y, t, 0x7E) | __builtin_amdgcn_bitop3_b32(g.z, g.w, t, 0x7E); }
template <int B> __device__ __forceinline__ bool anybad(unsigned x) { return __builtin_amdgcn_ballot_w64((x & ((1u << B) - 1)) != 0) != 0; }
#define PIN(x) asm volatile("" : "+v"(x))
#define CNT(i)
#define CNT0(i)

#define DMA_S(MOD, voff, sbase, ldst) asm volatile("s_mov_b32 m0, %2\n\ts_nop 0\n\tglobal_load_lds_dwordx4 %0, %1" MOD :: "v"(voff), "s"(sbase), "s"(ldst) : "memory")
template <int N> __device__ __forceinline__ void vmcnt() { asm volatile("s_waitcnt vmcnt(%0)" ::"i"(N) : "memory"); }
#define LGKM0() asm volatile("s_waitcnt lgkmcnt(0)" ::: "memory")
__device__ __forceinline__ void ld1w(v4u& a, unsigned o0, const char* sb) {
  asm volatile("global_load_dwordx4 %0, %1, %2 sc1\n\ts_waitcnt vmcnt(0)" : "=&v"(a) : "v"(o0), "s"(sb) : "memory"); }
__device__ __forceinline__ void ld2w(v4u& a, v4u& b, unsigned o0, unsigned o1, const char* sb) {
  asm volatile("global_load_dwordx4 %0, %2, %4 sc1\n\tglobal_load_dwordx4 %1, %3, %4 sc1\n\ts_waitcnt vmcnt(0)" : "=&v"(a), "=&v"(b) : "v"(o0), "v"(o1), "s"(sb) : "memory"); }
__device__ __forceinline__ void ld4w(v4u& a, v4u& b, v4u& c, v4u& d, unsigned o0, const char* sb) {
  asm volatile("global_load_dwordx4 %0, %4, %5 sc1\n\tglobal_load_dwordx4 %1, %4, %5 offset:1024 sc1\n\tglobal_load_dwordx4 %2, %4, %5 offset:2048 sc1\n\t"
               "global_load_dwordx4 %3, %4, %5 offset:3072 sc1\n\ts_waitcnt vmcnt(0)" : "=&v"(a), "=&v"(b), "=&v"(c), "=&v"(d) : "v"(o0), "s"(sb) : "memory"); }
template <u64 MASK> __device__ __forceinline__ void st_sc1(unsigned voff, v4u d, char* sbase) {
  asm volatile("s_mov_b64 exec, %3\n\tglobal_store_dwordx4 %0, %1, %2 sc1\n\ts_mov_b64 exec, -1\n\ts_nop 0" ::"v"(voff), "v"(d), "s"(sbase), "s"(MASK) : "memory"); }

#define ST(i) do { if ((SMASK >> (i)) & 1) { SB(); stamp[i] = (unsigned)__builtin_amdgcn_s_memrealtime(); SB(); } } while (0)

__device__ __forceinline__ int now16(unsigned t0) { return (int)(((unsigned)__builtin_amdgcn_s_memrealtime() - t0) * 16u); }
__device__ __forceinline__ int vm_inflight() { unsigned r; asm volatile("s_getreg_b32 %0, hwreg(HW_REG_IB_STS, 0, 4)" : "=s"(r)::"memory"); return (int)r; }
__device__ __forceinline__ int clock_offset(LAS char* L) { return __builtin_amdgcn_readfirstlane(*(volatile LAS int*)(L + L_DT)); }
__device__ __forceinline__ void wait_until(LAS char* L, unsigned t0, int t) {
#pragma unroll 1
  for (int it = 0; it < SPIN_CAP && now16(t0) - clock_offset(L) < t; ++it) __builtin_amdgcn_s_sleep(1);
}
__device__ __forceinline__ void spin_until(unsigned tick) {
#pragma unroll 1
  for (int it = 0; it < SPIN_CAP && (int)((unsigned)__builtin_amdgcn_s_memrealtime() - tick) < 0; ++it) {}
}
__device__ __forceinline__ void lds_set(LAS char* L, unsigned off, unsigned v) {
  asm volatile("s_mov_b64 exec, 1\n\tds_write_b32 %0, %1\n\ts_mov_b64 exec, -1" ::"v"((unsigned)(uintptr_t)(L + off)), "v"(v) : "memory");
}
template <int MASK, int SLEEP> __device__ __forceinline__ void wait4(LAS char* L, unsigned off, int n) {
#pragma unroll 1
  for (int it = 0; it < SPIN_CAP; ++it) {
    const v4i f = *(volatile LAS v4i*)(L + off);
    int m = 0x7fffffff;
    if (MASK & 1) m = min(m, f.x);
    if (MASK & 2) m = min(m, f.y);
    if (MASK & 4) m = min(m, f.z);
    if (MASK & 8) m = min(m, f.w);
    if (__builtin_amdgcn_readfirstlane(m) >= n) break;
    if (SLEEP) __builtin_amdgcn_s_sleep(SLEEP);
  }
  asm volatile("" ::: "memory");
}
#define TR_ADDR(w_) const int fr = (n >> 2) | ((g & 1) << 2); \
    unsigned ta0 = (8 * g + (n >> 2)) * 256 + ((((2 * (w_)) ^ fr) * 2 + ((n & 3) >> 1)) * 16) + (n & 1) * 8; \
    unsigned ta1 = (8 * g + (n >> 2)) * 256 + ((((2 * (w_) + 1) ^ fr) * 2 + ((n & 3) >> 1)) * 16) + (n & 1) * 8; PIN(ta0); PIN(ta1)

constexpr int C_V = 41, C_WD = 43, C_WO = 47;

__global__ void __launch_bounds__(512) __attribute__((amdgpu_waves_per_eu(2, 2)))
fused_decode(const u16* __restrict__ x, const u16* __restrict__ gw, char* __restrict__ ws, const u16* __restrict__ wq, const u16* __restrict__ wg,
             const u16* __restrict__ wu, const u16* __restrict__ kc, const u16* __restrict__ vc, const u16* __restrict__ wo, const u16* __restrict__ wd,
             u16* __restrict__ out) {
  extern __shared__ __attribute__((aligned(16))) char smem[];
  LAS char* L = (LAS char*)smem;
  const unsigned lbase = (unsigned)(uintptr_t)L;
  const int c = blockIdx.x, lane = threadIdx.x & 63, wv = __builtin_amdgcn_readfirstlane(threadIdx.x >> 6);
  const int xc = c & 7, j = c >> 3;
  const int kvh = xc, b = j & 1, sp = j >> 1;
  const int rg = 8 * xc + (j >> 2), cg = j & 3;
  const int qp = xc >> 1, qc = 32 * (xc & 1) + j;
  const bool has_f = j < 28;
  const int sk = has_f ? 0 : 4;
  const int g = lane >> 4, n = lane & 15;
  const int rot = (j >> 2) * 2 + (j & 1);
  const size_t kvoff = ((size_t)(b * 8 + kvh) * 16384 + 12288 + sp * 256) * 256;
  unsigned stamp[16];
  for (int i = 0; i < 16; ++i) stamp[i] = 0;
  asm volatile("" ::"s"(x), "s"(kc), "s"(vc), "s"(wq), "s"(wo), "s"(wg), "s"(wu), "s"(wd), "s"(gw), "s"(out), "s"(ws));
  const unsigned t0 = (unsigned)__builtin_amdgcn_s_memrealtime();
  ST(0);
  if (wv == 4) {
    const char* pc;
    asm volatile("s_getpc_b64 %0" : "=s"(pc));
    asm volatile("s_mov_b32 m0, %2\n\ts_nop 0\n\tglobal_load_lds_dword %0, %1" ::"v"(min(j, CODE_PF) * 512 + (lane & 7) * 64), "s"((const char*)((uintptr_t)pc & ~(uintptr_t)255)), "s"(lbase + L_OB) : "memory");
  }

  if (wv >= 4) {
    const int hw = wv - 4;
    const unsigned fh = L_FH + 4 * hw;
    __builtin_amdgcn_s_setprio(2);
    if (hw == 1) {
      const char* a = lane == 0 ? (const char*)vc + kvoff : lane == 1 ? (const char*)wo + (size_t)rg * 65536 : lane == 2 ? (const char*)wd + (size_t)rg * 28672
                    : lane == 3 ? (const char*)out : (const char*)kc + kvoff;
      asm volatile("s_mov_b32 m0, %1\n\ts_nop 0\n\tglobal_load_lds_dword %0, off" ::"v"(a), "s"(lbase + L_OB) : "memory");
    }
    __builtin_amdgcn_s_barrier();
    wait4<15, 0>(L, L_FS, 1);
    const unsigned t32 = __builtin_amdgcn_readfirstlane(*(LAS unsigned*)(L + L_P + 1024)) + 1, t12 = t32 & 0xFFFu, t8 = t32 & 0xFFu;
    ST(1);
    if (hw < 2) {
      v4u xq[4];
#pragma unroll
      for (int s = 0; s < 4; ++s) xq[s] = *(LAS v4u*)(L + L_X + (n & 1) * 1024 + qp * 256 + s * 64 + g * 16);
      const int col = 64 * qc + 32 * hw + 16 * ((n >> 1) & 1) + 4 * g;
      const unsigned qoff = WS_Q + (((((col >> 9) * 2 + (n & 1)) * 4 + ((col >> 7) & 3)) * 4 + qp) * 128 + (col & 127)) * 4;
      LAS char* a = L + L_WQ + (8 * g + (n >> 2)) * 128 + hw * 64 + (n & 3) * 8;
      wait4<15, 0>(L, L_FS, 5);
      ST(2);
      v4u af[8];
#pragma unroll
      for (int s = 0; s < 4; ++s) { af[2 * s] = tr2(a + s * 4096, 512); af[2 * s + 1] = tr2(a + s * 4096 + 32, 512); }
      SB();
      f32x4 d0 = {0, 0, 0, 0}, d1 = {0, 0, 0, 0};
#pragma unroll
      for (int s = 0; s < 4; ++s) { d0 = mfma(af[2 * s], xq[s], d0); d1 = mfma(af[2 * s + 1], xq[s], d1); }
      v4u gr;
#pragma unroll
      for (int r = 0; r < 4; ++r) gr[r] = tagf<8>((n & 2) ? d1[r] : d0[r], t8);
      st_sc1<0x000F000F000F000Full>(qoff, gr, ws);
    } else {
      if (hw == 2 && c < 8) st_sc1<1>(WS_T + c * 128, v4u{t0, t32, t0, t32}, ws);
      {
        const v4u gwv = *(LAS v4u*)(L + L_P + lane * 16), xv = *(LAS v4u*)(L + L_X + (hw & 1) * 1024 + lane * 16);
        float s = 0.f;
#pragma unroll
        for (int k = 0; k < 4; ++k) {
          const unsigned pr = pk_bf16(asf(xv[k] << 16) * asf(gwv[k] << 16), asf(xv[k] & 0xFFFF0000u) * asf(gwv[k] & 0xFFFF0000u));
          s += asf(pr << 16) + asf(pr & 0xFFFF0000u);
        }
        s += dppf<XOR1>(s); s += dppf<XOR2>(s); s += dppf<ROR4>(s); s += dppf<ROR8>(s); s = sw16(s, s); s = sw32(s, s);
        const float gs = rbf(frcp(1.f + fexp(-rbf(s))));
        if (lane == 0) *(LAS f32x2*)(L + L_GATE + (hw & 1) * 8) = f32x2{gs, rbf(1.f - gs)};
        lds_set(L, L_TAG, t32);
      }
      v4u xg[2];
#pragma unroll
      for (int s = 0; s < 2; ++s) xg[s] = *(LAS v4u*)(L + L_X + (n & 1) * 1024 + xc * 128 + s * 64 + g * 16);
      const unsigned hoff = WS_H + ((((n & 1) * 2 + (hw & 1)) * 8 + xc) * 448 + 16 * j + 4 * ((n >> 1) & 3) + g) * 16;
      LAS char* a = L + L_GU + (hw & 1) * 8192 + (8 * g + (n >> 2)) * 128 + (n & 3) * 8;
      wait4<15, 0>(L, L_FS, 9 - sk);
      ST(2);
      v4u af[8];
#pragma unroll
      for (int s = 0; s < 2; ++s)
#pragma unroll
        for (int t = 0; t < 4; ++t) af[4 * s + t] = tr2(a + s * 4096 + t * 32, 512);
      SB();
      f32x4 d[4];
#pragma unroll
      for (int t = 0; t < 4; ++t) d[t] = mfma(af[4 + t], xg[1], mfma(af[t], xg[0], f32x4{0, 0, 0, 0}));
      v4u gr;
#pragma unroll
      for (int r = 0; r < 4; ++r) { const float lo = (n & 2) ? d[1][r] : d[0][r], hi = (n & 2) ? d[3][r] : d[2][r]; gr[r] = tagf<12>((n & 4) ? hi : lo, t12); }
      PIN(gr);
      if (has_f) st_sc1<0x00FF00FF00FF00FFull>(hoff, gr, ws);
    }
    lds_set(L, fh, 1);
    ST(3);
    if (hw == 1) {
      wait_until(L, t0, TT);
      v4u tv;
      ld1w(tv, WS_T + (lane & 7) * 128, ws);
      const bool ok = tv.y == t32 && tv.w == t32;
      float d = ok ? (float)(int)(tv.x - t0) : 0.f, k = ok ? 1.f : 0.f;
      d += dppf<XOR1>(d); k += dppf<XOR1>(k); d += dppf<XOR2>(d); k += dppf<XOR2>(k); d += dppf<HMIR>(d); k += dppf<HMIR>(k);
      lds_set(L, L_DT, (int)fminf(fmaxf(d * 16.f * frcp(fmaxf(k, 1.f)), -1600.f), 1600.f));
    }
    {
      const unsigned qo = WS_Q + ((kvh * 2 + b) * 4 + hw) * 2048 + lane * 16;
      wait_until(L, t0, TQ);
      v4u q0, q1;
      CNT0(14); CNT0(15);
#pragma unroll 1
      for (int it = 0; it < SPIN_CAP; ++it) { ld2w(q0, q1, qo, qo + 1024, ws); if (!anybad<8>(neq(q0, t8) | neq(q1, t8))) break; CNT(14); }
      float v[4];
#pragma unroll
      for (int r = 0; r < 4; ++r) { v[r] = untag<8>(q0[r]) + untag<8>(q1[r]); v[r] = sw32(v[r], v[r]); }
      if (lane < 32) *(LAS v2u*)(L + L_Q + hw * QSTR + lane * 8) = v2u{pk_bf16(v[0], v[1]), pk_bf16(v[2], v[3])};
      LGKM0(); lds_set(L, fh, 2);
      ST(4);
    }
    wait4<15, 1>(L, L_FH, 2);
    v4u qf[4], kf[16];
#pragma unroll
    for (int s = 0; s < 4; ++s) qf[s] = *(LAS v4u*)(L + L_Q + (n & 3) * QSTR + s * 64 + g * 16);
    wait4<15, 0>(L, L_FS, 13 + 4 * hw - sk);
    ST(6);
#pragma unroll
    for (int s = 0; s < 4; ++s)
#pragma unroll
      for (int t = 0; t < 4; ++t) kf[s * 4 + t] = *(LAS v4u*)(L + L_K + (4 * hw + t) * 4096 + n * 256 + (((4 * s + g) ^ n) * 16));
    SB();
    float sc[4];
    {
      f32x4 d[4];
#pragma unroll
      for (int t = 0; t < 4; ++t) d[t] = f32x4{0, 0, 0, 0};
#pragma unroll
      for (int s = 0; s < 4; ++s)
#pragma unroll
        for (int t = 0; t < 4; ++t) d[t] = mfma(kf[s * 4 + t], qf[s], d[t]);
      constexpr float SC = 0.08838834764831845f;
      float v[4];
#pragma unroll
      for (int r = 0; r < 4; ++r) { const float lo = (n & 4) ? d[1][r] : d[0][r], hi = (n & 4) ? d[3][r] : d[2][r]; v[r] = (n & 8) ? hi : lo; }
      const unsigned a = pk_bf16(v[0], v[1]), bq = pk_bf16(v[2], v[3]);
      const unsigned a2 = pk_bf16(asf(a << 16) * SC, asf(a & 0xFFFF0000u) * SC), b2 = pk_bf16(asf(bq << 16) * SC, asf(bq & 0xFFFF0000u) * SC);
      sc[0] = asf(a2 << 16); sc[1] = asf(a2 & 0xFFFF0000u); sc[2] = asf(b2 << 16); sc[3] = asf(b2 & 0xFFFF0000u);
    }
    float m = fmaxf(fmaxf(sc[0], sc[1]), fmaxf(sc[2], sc[3]));
    m = fmaxf(m, dppf<ROR4>(m)); m = fmaxf(m, dppf<ROR8>(m)); m = x16max(m); m = x32max(m);
    if (lane < 4) *(LAS float*)(L + L_MX + lane * 16 + hw * 4) = m;
    LGKM0(); lds_set(L, fh, 3);
    wait4<15, 0>(L, L_FH, 3);
    const int e = lane & 7;
    const unsigned ho = WS_H + (((hw & 1) * 16 + (lane >> 3)) * 448 + 7 * rg + (e < 7 ? e : 6)) * 16;
    if (hw >= 2) { DMA_S(" sc1", ho, ws, lbase + L_K + hw * 2048); DMA_S(" sc1", ho + 8 * 448 * 16, ws, lbase + L_K + hw * 2048 + 1024); }
    { const f32x4 mm = *(LAS f32x4*)(L + L_MX + (n & 3) * 16); m = fmaxf(fmaxf(mm[0], mm[1]), fmaxf(mm[2], mm[3])); }
    {
      float p[4];
#pragma unroll
      for (int r = 0; r < 4; ++r) p[r] = fexp(sc[r] - m);
      *(LAS v2u*)(L + L_P + (n & 3) * PSTR + (64 * hw + 16 * (n >> 2) + 4 * g) * 2) = v2u{pk_bf16(p[0], p[1]), pk_bf16(p[2], p[3])};
      float l = (p[0] + p[1]) + (p[2] + p[3]);
      l += dppf<ROR4>(l); l += dppf<ROR8>(l); l = sw16(l, l); l = sw32(l, l);
      if (lane < 4) *(LAS float*)(L + L_SUM + lane * 16 + hw * 4) = l;
    }
    LGKM0(); lds_set(L, fh, 4);
    ST(7);
    TR_ADDR(hw);
    unsigned ooff = WS_O + (((2 * (4 * kvh + (n & 3)) + (hw >> 1)) * 2 + b) * 16 + sp) * 256 + (hw & 1) * 128 + ((n >> 2) & 1) * 64 + g * 16;
    unsigned tv = t12;
    PIN(ooff); PIN(tv);
    const int tabs = t0 + ((clock_offset(L) + TVE) >> 4);
    wait4<15, 0>(L, L_FH, 4);
    v4u pa[8], pb[8];
    {
      const unsigned pp = L_P + (n & 3) * PSTR + g * 16, aa = n < 4 ? pp : L_Z, ab = (n >> 2) == 1 ? pp : L_Z;
#pragma unroll
      for (int u = 0; u < 8; ++u) { pa[u] = *(LAS v4u*)(L + aa + u * 64); pb[u] = *(LAS v4u*)(L + ab + u * 64); }
    }
    if (hw == 2) {
      const f32x4 ls = *(LAS f32x4*)(L + L_SUM + (n & 3) * 16);
      st_sc1<0xF>(WS_ST + (((kvh * 4 + (n & 3)) * 2 + b) * 16 + sp) * 16, v4u{tagt<12>(m, t12), tagf<12>((ls[0] + ls[1]) + (ls[2] + ls[3]), t12), t12, t12}, ws);
    }
    f32x4 o = {0, 0, 0, 0};
#define PVN(u0, nu) do { v4u va[nu], vb[nu]; _Pragma("unroll") for (int u = 0; u < nu; ++u) { va[u] = tr2(L + L_V + ta0 + (u0 + u) * 8192, 1024); vb[u] = tr2(L + L_V + ta1 + (u0 + u) * 8192, 1024); } SB(); \
      _Pragma("unroll") for (int u = 0; u < nu; ++u) { o = mfma(va[u], pa[u0 + u], o); o = mfma(vb[u], pb[u0 + u], o); } } while (0)
    wait4<15, 1>(L, L_FS, 33 - sk);
    PVN(0, 4); PIN(o);
    ST(8);
    if (hw >= 2) {
      if (hw == 2) vmcnt<1>(); else vmcnt<0>();
      v4u h0 = *(LAS v4u*)(L + L_K + hw * 2048 + lane * 16), h1 = *(LAS v4u*)(L + L_K + hw * 2048 + 1024 + lane * 16);
#pragma unroll 1
      for (int it = 0; it < SPIN_CAP && anybad<12>(neq(h0, t12) | neq(h1, t12)); ++it) { ld2w(h0, h1, ho, ho + 8 * 448 * 16, ws); CNT(14); }
      float v[4];
#pragma unroll
      for (int r = 0; r < 4; ++r) v[r] = sw32(untag<12>(h0[r]), untag<12>(h1[r]));
      const float a0 = sw16(v[0], v[1]), a1 = sw16(v[2], v[3]);
      const float b0 = a0 + dppf<ROR8>(a0), b1 = a1 + dppf<ROR8>(a1);
      const float gg = rbf((lane & 8) ? b1 : b0);
      const auto sw = __builtin_amdgcn_permlane32_swap(asu(gg), asu(gg), false, false);
      const float hv = rbf(gg * frcp(1.f + fexp(-gg))) * asf(sw[1]);
      if (lane < 32) *(LAS u16*)(L + L_HB + (hw & 1) * 64 + (4 * e + ((lane >> 2) & 2) + (lane >> 4)) * 2) = e < 7 ? (u16)(pk_bf16(hv, hv) & 0xFFFFu) : (u16)0;
      LGKM0(); lds_set(L, fh, 5);
      ST(5);
    }
    wait4<15, 0>(L, L_FS, 39 - sk);
    PVN(4, 3); PIN(o);
    const unsigned so = WS_ST + (((rg >> 1) * 2 + (hw & 1)) * 16 + 4 * (n & 3) + g) * 16;
    wait4<15, 0>(L, L_FS, C_V - sk);
    ST(9);
    PVN(7, 1);
    st_sc1<0x00FF00FF00FF00FFull>(ooff, v4u{tagt<12>(o[0], tv), tagt<12>(o[1], tv), tagt<12>(o[2], tv), tagt<12>(o[3], tv)}, ws);
    ST(10);
    if (hw < 2) {
      unsigned oo = WS_O + (rg * 2 + hw) * 4096 + lane * 16, oba = L_OB + hw * 128 + (4 * n + (g & 1) * 2 + (g >> 1)) * 2;
      DMA_S(" sc1", so, ws, lbase + L_K + hw * 1024);
      const float gate = *(LAS float*)(L + L_GATE + hw * 8);
      PIN(oo); PIN(oba);
      spin_until(tabs + (TO >> 4));
      ST(11);
      v4u a0, a1, a2, a3;
      asm volatile("global_load_dwordx4 %0, %4, %5 sc1\n\tglobal_load_dwordx4 %1, %4, %5 offset:1024 sc1\n\tglobal_load_dwordx4 %2, %4, %5 offset:2048 sc1\n\t"
                   "global_load_dwordx4 %3, %4, %5 offset:3072 sc1\n\ts_waitcnt vmcnt(4)" : "=&v"(a0), "=&v"(a1), "=&v"(a2), "=&v"(a3) : "v"(oo), "s"(ws) : "memory");
      v4u st = *(LAS v4u*)(L + L_K + hw * 1024 + lane * 16);
      if (anybad<12>(neq(st, t12))) {
#pragma unroll 1
        for (int it = 0; it < SPIN_CAP; ++it) { CNT(14); ld1w(st, so, ws); if (!anybad<12>(neq(st, t12))) break; }
      }
      const float ms = untag<12>(st.x), ls = untag<12>(st.y);
      float M = ms; M = fmaxf(M, dppf<XOR1>(M)); M = fmaxf(M, dppf<XOR2>(M)); M = x16max(M); M = x32max(M);
      const float ex = fexp(ms - M);
      float Ls = ex * ls; Ls += dppf<XOR1>(Ls); Ls += dppf<XOR2>(Ls); Ls = sw16(Ls, Ls); Ls = sw32(Ls, Ls);
      const float wv_ = gate * ex * frcp(Ls);
      const float wt0 = dppf<0x00>(wv_), wt1 = dppf<0x55>(wv_), wt2 = dppf<0xAA>(wv_), wt3 = dppf<0xFF>(wv_);
      f32x2 w0 = {wt0, wt0}, w1 = {wt1, wt1}, w2 = {wt2, wt2}, w3 = {wt3, wt3};
      PIN(w0); PIN(w1); PIN(w2); PIN(w3);
      asm volatile("s_waitcnt vmcnt(0)" : "+v"(a0), "+v"(a1), "+v"(a2), "+v"(a3)::"memory");
      ST(12);
#pragma unroll 1
      for (int it = 0; it < SPIN_CAP; ++it) {
#define UT2(a, i) f32x2{untag<12>(a[i]), untag<12>(a[i + 1])}
        const f32x2 lo = __builtin_elementwise_fma(w0, UT2(a0, 0), w1 * UT2(a1, 0)) + __builtin_elementwise_fma(w2, UT2(a2, 0), w3 * UT2(a3, 0));
        const f32x2 hi = __builtin_elementwise_fma(w0, UT2(a0, 2), w1 * UT2(a1, 2)) + __builtin_elementwise_fma(w2, UT2(a2, 2), w3 * UT2(a3, 2));
        const float t = sw16(sw32(lo[0], lo[1]), sw32(hi[0], hi[1]));
        *(volatile LAS u16*)(L + oba) = (u16)(pk_bf16(t, t) & 0xFFFFu);
        SB();
        if (!anybad<12>(neq(a0, t12) | neq(a1, t12) | neq(a2, t12) | neq(a3, t12))) break;
        CNT(15);
        ld4w(a0, a1, a2, a3, oo, ws);
      }
      LGKM0();
      ST(13);
      __builtin_amdgcn_s_barrier();
    } else if (hw == 2) {
      __builtin_amdgcn_s_barrier();
    } else {
      unsigned yo = WS_Y + (((xc >> 1) * 64 + lane) * 2 + (xc & 1)) * 512 + j * 16;
      unsigned op = ((xc & 1) * 512 + (xc >> 1) * 128 + 4 * j + (g & 1) * 2 + (g >> 1)) * 2;
      PIN(yo); PIN(op);
      __builtin_amdgcn_s_barrier();
      __builtin_amdgcn_s_sleep(4);
      spin_until(tabs + ((TO + TY) >> 4));
      ST(11);
      float t;
#pragma unroll 1
      for (int it = 0; it < SPIN_CAP; ++it) {
        v4u y;
        ld1w(y, yo, ws);
        ST(12);
        t = sw16(sw32(untag<12>(y.x), untag<12>(y.y)), sw32(untag<12>(y.z), untag<12>(y.w)));
        t += dppf<XOR1>(t); t += dppf<XOR2>(t); t += dppf<ROR4>(t); t += dppf<ROR8>(t);
        if (!anybad<12>(neq(y, t12))) break;
        CNT(15);
      }
      asm volatile("s_mov_b64 exec, %3\n\tglobal_store_short %0, %1, %2\n\ts_mov_b64 exec, -1" ::"v"(op), "v"(pk_bf16(t, t)), "s"(out), "s"(0x0001000100010001ull) : "memory");
      if (c == 0) asm volatile("s_mov_b64 exec, 1\n\tglobal_store_dword %0, %1, %2 sc1\n\ts_mov_b64 exec, -1" ::"v"(0), "v"(t32), "s"(ws) : "memory");
      ST(13);
    }
  } else {
    const int w = wv;
    const unsigned fs = L_FS + 4 * w;
    int issued = 5, landed = 0, due = TK0 + w * (TAU / 4), seen = 0, dt = 0;
#define PUBLISH(vm_) do { if (issued - (vm_) != landed) { landed = issued - (vm_); lds_set(L, fs, landed); } } while (0)
#define PACE() do { \
      const int v0_ = vm_inflight(); PUBLISH(v0_); \
      if (seen < due || v0_ >= WIN) { \
        const int d_ = *(volatile LAS int*)(L + L_DT); \
        _Pragma("unroll 1") for (int it_ = 0; it_ < SPIN_CAP; ++it_) { \
          seen = now16(t0) - dt; \
          const int vm_ = vm_inflight(); PUBLISH(vm_); \
          if (seen >= due && vm_ < WIN) break; } \
        const int dn_ = __builtin_amdgcn_readfirstlane(d_); seen += dt - dn_; dt = dn_; } \
      due += TAU; ++issued; } while (0)
    {
      const char* sb = w == 0 ? (const char*)x : w == 1 ? (const char*)x + 1024 : w == 2 ? (const char*)gw : (const char*)ws;
      DMA_S("", w == 3 ? 0 : lane * 16, sb, lbase + (w < 2 ? L_X : L_P - 2048) + w * 1024);
    }
    {
      const char* base = (const char*)wq + (size_t)(128 * qp + 8 * w) * 8192 + qc * 128;
      const unsigned voff = (lane >> 3) * 8192 + (lane & 7) * 16;
#pragma unroll
      for (int i = 0; i < 4; ++i) DMA_S(" nt", voff, base + (size_t)i * 32 * 8192, lbase + L_WQ + (4 * i + w) * 1024);
    }
    if (w == 0 && lane < 9) *(volatile LAS int*)(L + L_FS + 4 * lane) = lane < 8 ? -64 : 0;
    if (w == 1) *(volatile LAS v2u*)(L + L_Z + 8 * lane) = v2u{0, 0};
    ST(7);
    LGKM0();
    __builtin_amdgcn_s_barrier();
    if (has_f) {
      const unsigned voff = (lane >> 3) * 3584 + (lane & 7) * 16;
      const size_t off = (size_t)(64 * xc + 8 * w) * 3584 + j * 128;
#pragma unroll 1
      for (int i = 0; i < 4; ++i) { PACE(); DMA_S(" nt", voff, (const char*)(i < 2 ? wg : wu) + off + (i & 1) * (32 * 3584), lbase + L_GU + (4 * i + w) * 1024); }
    } else {
      due += 4 * TAU;
    }
    {
      const char* kb = (const char*)kc + kvoff + w * 1024;
      const unsigned voff = g * 256 + ((n ^ (4 * w + g)) * 16);
#pragma unroll 1
      for (int i = 0; i < 16; ++i) { PACE(); DMA_S(" nt", voff, kb + (((i + rot) & 15) << 12), lbase + L_K + i * 4096 + w * 1024); }
    }
    ST(1);
    {
      const unsigned voff = g * 256 + ((n ^ (2 * (g | ((w >> 1) << 2)))) * 16);
      const char* vb = (const char*)vc + kvoff + w * 1024;
#pragma unroll 1
      for (int i = 0; i < 16; ++i) {
        if (i == 8) wait4<3, 0>(L, L_FH, 1);
        if (i >= 16 - NL) due += TAUL - TAU;
        PACE(); DMA_S(" nt", voff, vb + (((i + rot) & 15) << 12), lbase + L_V + i * 4096 + w * 1024);
      }
    }
    ST(2);
    wait4<12, 0>(L, L_FH, 1);
    {
      const unsigned vo = g * 1024 + ((n ^ (2 * (g | ((w >> 1) << 2)))) * 16);
      const char* ob = (const char*)wo + (size_t)(64 * rg + 4 * w) * 1024 + cg * 256;
      const char* db = (const char*)wd + (size_t)(28 * rg) * 1024 + cg * 256;
#pragma unroll 1
      for (int i = 0; i < 6; ++i) {
        const char* src = i >= 2 ? ob + (i - 2) * 16384 : db + (i == 0 ? 4 * w : w < 3 ? 16 + 4 * w : 0) * 1024;
        due += TAU2 - TAU; PACE();
        DMA_S(" nt", vo, src, lbase + L_WO + (i >= 2 ? i - 2 : i + 4) * 4096 + w * 1024);
      }
    }
    ST(3);
#pragma unroll 1
    for (int it = 0; it < SPIN_CAP && landed < C_WD - sk; ++it) { const int vm = vm_inflight(); PUBLISH(vm); }
    ST(4);
    unsigned t12 = *(volatile LAS unsigned*)(L + L_TAG) & 0xFFFu;
    TR_ADDR(w);
    unsigned yoff = WS_Y + ((cg * 64 + rg) * 2 + (n & 1)) * 512 + w * 128 + ((n >> 1) & 1) * 64 + g * 16;
    unsigned oa = lbase + (n < 2 ? L_OB + n * 128 + g * 16 : L_Z), ob = lbase + ((n >> 1) == 1 ? L_OB + (n & 1) * 128 + g * 16 : L_Z);
    const float g1 = *(LAS float*)(L + L_GATE + (n & 1) * 8 + 4);
    wait4<12, 0>(L, L_FH, 5);
    const v4u ha = *(LAS v4u*)(L + (n < 2 ? L_HB + n * 64 + g * 16 : L_Z)), hb = *(LAS v4u*)(L + ((n >> 1) == 1 ? L_HB + (n & 1) * 64 + g * 16 : L_Z));
    wait4<15, 0>(L, L_FS, C_WD - sk);
    f32x4 y = mfma(tr2(L + L_WD + ta1, 1024), hb, mfma(tr2(L + L_WD + ta0, 1024), ha, f32x4{0, 0, 0, 0})) * g1;
    PIN(y);
    vmcnt<0>(); lds_set(L, fs, C_WO - sk);
    ST(5);
    wait4<15, 0>(L, L_FS, C_WO - sk);
    v4u wf[4];
    wf[0] = tr2(L + L_WO + ta0, 1024); wf[1] = tr2(L + L_WO + ta1, 1024); wf[2] = tr2(L + L_WO + ta0 + 8192, 1024); wf[3] = tr2(L + L_WO + ta1 + 8192, 1024);
    PIN(wf[0]); PIN(wf[1]); PIN(wf[2]); PIN(wf[3]); PIN(oa); PIN(ob); PIN(yoff); PIN(t12);
    __builtin_amdgcn_s_setprio(3);
    ST(6);
    __builtin_amdgcn_s_barrier();
    asm volatile("" ::: "memory");
    ST(10);
    {
      const v4u a0 = *(LAS v4u*)(uintptr_t)oa, b0 = *(LAS v4u*)(uintptr_t)ob, a1 = *(LAS v4u*)(uintptr_t)(oa + 64), b1 = *(LAS v4u*)(uintptr_t)(ob + 64);
      SB();
      y = mfma(wf[0], a0, y); y = mfma(wf[1], b0, y); y = mfma(wf[2], a1, y); y = mfma(wf[3], b1, y);
      st_sc1<0x000F000F000F000Full>(yoff, v4u{tagt<12>(y[0], t12), tagt<12>(y[1], t12), tagt<12>(y[2], t12), tagt<12>(y[3], t12)}, ws);
    }
    ST(11);
  }
  if (c == PWG && wv == PWAVE) {
    const unsigned tgt = 4000u + (PROBE >= 0 ? 8u * (unsigned)(stamp[PROBE >= 0 ? PROBE : 0] - stamp[0]) : 0u);
    for (int it = 0; it < 1000000; ++it) { if ((unsigned)__builtin_amdgcn_s_memrealtime() - stamp[0] >= tgt) break; }
  }
}

extern "C" void kernel_launch(void* const* d_in, const int* in_sizes, int n_in, void* d_out, int out_size, void* d_ws, size_t ws_size, hipStream_t stream) {
  (void)hipFuncSetAttribute((const void*)fused_decode, hipFuncAttributeMaxDynamicSharedMemorySize, L_END);
  fused_decode<<<256, 512, L_END, stream>>>((const u16*)d_in[0], (const u16*)d_in[9], (char*)d_ws, (const u16*)d_in[3], (const u16*)d_in[6], (const u16*)d_in[7],
                                            (const u16*)d_in[1], (const u16*)d_in[2], (const u16*)d_in[4], (const u16*)d_in[8], (u16*)d_out);
}
```

```cpp
#define PROBE 15
#define PSK -1
#define PWG 0
#define PWAVE 6
#define SMASK 1
#define TO 2900
#define TY 1650
#include <hip/hip_runtime.h>
#include <hip/hip_bf16.h>
#include <stdint.h>

#define STAMP 0
#define TAU 256
#define TK0 440
#define TAUL TAU
#define NL 0
#define TAU2 256
#define TT 2900
#define TQ 4300
#define TVE (TK0 + (35 - NL) * TAU + NL * TAUL)
#define WIN 12
#define CODE_PF 25

typedef unsigned short u16;
typedef unsigned long long u64;
typedef unsigned v4u __attribute__((ext_vector_type(4)));
typedef int v4i __attribute__((ext_vector_type(4)));
typedef unsigned v2u __attribute__((ext_vector_type(2)));
typedef short v4s __attribute__((ext_vector_type(4)));
typedef float f32x4 __attribute__((ext_vector_type(4)));
typedef float f32x2 __attribute__((ext_vector_type(2)));
typedef __bf16 v8bf __attribute__((ext_vector_type(8)));
typedef __bf16 v2bf __attribute__((ext_vector_type(2)));
#define GAS __attribute__((address_space(1)))
#define LAS __attribute__((address_space(3)))
#define SB() __builtin_amdgcn_sched_barrier(0)

constexpr unsigned WS_T = 1024;
constexpr unsigned WS_Q = 4096;
constexpr unsigned WS_H = WS_Q + 131072;
constexpr unsigned WS_ST = WS_H + 229376;
constexpr unsigned WS_O = WS_ST + 16384;
constexpr unsigned WS_Y = WS_O + 524288;
constexpr unsigned WS_STAMP = 4u << 20;
constexpr unsigned L_K = 0, L_V = 65536, L_WQ = L_V + 8 * 4096, L_WO = 131072, L_GU = L_WO, L_WD = L_WO + 16384, L_X = L_WD + 8192,
                   L_Q = L_X + 2048, QSTR = 320, L_P = L_Q + 4 * QSTR, PSTR = 576, L_OB = L_P + 4 * PSTR, L_HB = L_OB + 256, L_MX = L_HB + 128,
                   L_SUM = L_MX + 64, L_GATE = L_SUM + 64, L_FS = L_GATE + 16, L_FH = L_FS + 16, L_DT = L_FH + 16, L_TAG = L_DT + 4,
                   L_Z = L_DT + 16, L_END = L_Z + 512;
static_assert(L_END <= 163840, "LDS");
constexpr int SPIN_CAP = 1 << 17;

__device__ __forceinline__ float asf(unsigned u) { return __builtin_bit_cast(float, u); }
__device__ __forceinline__ unsigned asu(float f) { return __builtin_bit_cast(unsigned, f); }
__device__ __forceinline__ unsigned pk_bf16(float lo, float hi) { f32x2 x = {lo, hi}; return __builtin_bit_cast(unsigned, __builtin_convertvector(x, v2bf)); }
__device__ __forceinline__ float rbf(float f) { return asf(pk_bf16(f, f) << 16); }
template <int CTRL> __device__ __forceinline__ float dppf(float x) { return asf(__builtin_amdgcn_mov_dpp(asu(x), CTRL, 0xf, 0xf, true)); }
constexpr int XOR1 = 0xB1, XOR2 = 0x4E, HMIR = 0x141, ROR4 = 0x124, ROR8 = 0x128;
__device__ __forceinline__ float sw16(float a, float b) { auto s = __builtin_amdgcn_permlane16_swap(asu(a), asu(b), false, false); return asf(s[0]) + asf(s[1]); }
__device__ __forceinline__ float sw32(float a, float b) { auto s = __builtin_amdgcn_permlane32_swap(asu(a), asu(b), false, false); return asf(s[0]) + asf(s[1]); }
__device__ __forceinline__ float x16max(float x) { auto s = __builtin_amdgcn_permlane16_swap(asu(x), asu(x), false, false); return fmaxf(asf(s[0]), asf(s[1])); }
__device__ __forceinline__ float x32max(float x) { auto s = __builtin_amdgcn_permlane32_swap(asu(x), asu(x), false, false); return fmaxf(asf(s[0]), asf(s[1])); }
__device__ __forceinline__ float frcp(float x) { return __builtin_amdgcn_rcpf(x); }
__device__ __forceinline__ float fexp(float x) { return __builtin_amdgcn_exp2f(x * 1.4426950408889634f); }
__device__ __forceinline__ f32x4 mfma(v4u a, v4u b, f32x4 c) { return __builtin_amdgcn_mfma_f32_16x16x32_bf16(__builtin_bit_cast(v8bf, a), __builtin_bit_cast(v8bf, b), c, 0, 0, 0); }
__device__ __forceinline__ v2u trread(LAS char* p) { return __builtin_bit_cast(v2u, __builtin_amdgcn_ds_read_tr16_b64_v4i16((LAS v4s*)p)); }
__device__ __forceinline__ v4u tr2(LAS char* p, int d) { v2u a = trread(p), b = trread(p + d); return v4u{a.x, a.y, b.x, b.y}; }
template <int B> __device__ __forceinline__ unsigned tagt(float v, unsigned t) { return (asu(v) & ~((1u << B) - 1)) | t; }
template <int B> __device__ __forceinline__ unsigned tagf(float v, unsigned t) { return ((asu(v) + (1u << (B - 1))) & ~((1u << B) - 1)) | t; }
template <int B> __device__ __forceinline__ float untag(unsigned w) { return asf(w & ~((1u << B) - 1)); }
__device__ __forceinline__ unsigned neq(v4u g, unsigned t) { return __builtin_amdgcn_bitop3_b32(g.x, g.y, t, 0x7E) | __builtin_amdgcn_bitop3_b32(g.z, g.w, t, 0x7E); }
template <int B> __device__ __forceinline__ bool anybad(unsigned x) { return __builtin_amdgcn_ballot_w64((x & ((1u << B) - 1)) != 0) != 0; }
#define PIN(x) asm volatile("" : "+v"(x))
#define CNT(i)
#define CNT0(i)

#define DMA_S(MOD, voff, sbase, ldst) asm volatile("s_mov_b32 m0, %2\n\ts_nop 0\n\tglobal_load_lds_dwordx4 %0, %1" MOD :: "v"(voff), "s"(sbase), "s"(ldst) : "memory")
template <int N> __device__ __forceinline__ void vmcnt() { asm volatile("s_waitcnt vmcnt(%0)" ::"i"(N) : "memory"); }
#define LGKM0() asm volatile("s_waitcnt lgkmcnt(0)" ::: "memory")
__device__ __forceinline__ void ld1w(v4u& a, unsigned o0, const char* sb) {
  asm volatile("global_load_dwordx4 %0, %1, %2 sc1\n\ts_waitcnt vmcnt(0)" : "=&v"(a) : "v"(o0), "s"(sb) : "memory"); }
__device__ __forceinline__ void ld2w(v4u& a, v4u& b, unsigned o0, unsigned o1, const char* sb) {
  asm volatile("global_load_dwordx4 %0, %2, %4 sc1\n\tglobal_load_dwordx4 %1, %3, %4 sc1\n\ts_waitcnt vmcnt(0)" : "=&v"(a), "=&v"(b) : "v"(o0), "v"(o1), "s"(sb) : "memory"); }
__device__ __forceinline__ void ld4w(v4u& a, v4u& b, v4u& c, v4u& d, unsigned o0, const char* sb) {
  asm volatile("global_load_dwordx4 %0, %4, %5 sc1\n\tglobal_load_dwordx4 %1, %4, %5 offset:1024 sc1\n\tglobal_load_dwordx4 %2, %4, %5 offset:2048 sc1\n\t"
               "global_load_dwordx4 %3, %4, %5 offset:3072 sc1\n\ts_waitcnt vmcnt(0)" : "=&v"(a), "=&v"(b), "=&v"(c), "=&v"(d) : "v"(o0), "s"(sb) : "memory"); }
template <u64 MASK> __device__ __forceinline__ void st_sc1(unsigned voff, v4u d, char* sbase) {
  asm volatile("s_mov_b64 exec, %3\n\tglobal_store_dwordx4 %0, %1, %2 sc1\n\ts_mov_b64 exec, -1\n\ts_nop 0" ::"v"(voff), "v"(d), "s"(sbase), "s"(MASK) : "memory"); }

#define ST(i) do { if ((SMASK >> (i)) & 1) { SB(); stamp[i] = (unsigned)__builtin_amdgcn_s_memrealtime(); SB(); } } while (0)

__device__ __forceinline__ int now16(unsigned t0) { return (int)(((unsigned)__builtin_amdgcn_s_memrealtime() - t0) * 16u); }
__device__ __forceinline__ int vm_inflight() { unsigned r; asm volatile("s_getreg_b32 %0, hwreg(HW_REG_IB_STS, 0, 4)" : "=s"(r)::"memory"); return (int)r; }
__device__ __forceinline__ int clock_offset(LAS char* L) { return __builtin_amdgcn_readfirstlane(*(volatile LAS int*)(L + L_DT)); }
__device__ __forceinline__ void wait_until(LAS char* L, unsigned t0, int t) {
#pragma unroll 1
  for (int it = 0; it < SPIN_CAP && now16(t0) - clock_offset(L) < t; ++it) __builtin_amdgcn_s_sleep(1);
}
__device__ __forceinline__ void spin_until(unsigned tick) {
#pragma unroll 1
  for (int it = 0; it < SPIN_CAP && (int)((unsigned)__builtin_amdgcn_s_memrealtime() - tick) < 0; ++it) {}
}
__device__ __forceinline__ void lds_set(LAS char* L, unsigned off, unsigned v) {
  asm volatile("s_mov_b64 exec, 1\n\tds_write_b32 %0, %1\n\ts_mov_b64 exec, -1" ::"v"((unsigned)(uintptr_t)(L + off)), "v"(v) : "memory");
}
template <int MASK, int SLEEP> __device__ __forceinline__ void wait4(LAS char* L, unsigned off, int n) {
#pragma unroll 1
  for (int it = 0; it < SPIN_CAP; ++it) {
    const v4i f = *(volatile LAS v4i*)(L + off);
    int m = 0x7fffffff;
    if (MASK & 1) m = min(m, f.x);
    if (MASK & 2) m = min(m, f.y);
    if (MASK & 4) m = min(m, f.z);
    if (MASK & 8) m = min(m, f.w);
    if (__builtin_amdgcn_readfirstlane(m) >= n) break;
    if (SLEEP) __builtin_amdgcn_s_sleep(SLEEP);
  }
  asm volatile("" ::: "memory");
}
#define TR_ADDR(w_) const int fr = (n >> 2) | ((g & 1) << 2); \
    unsigned ta0 = (8 * g + (n >> 2)) * 256 + ((((2 * (w_)) ^ fr) * 2 + ((n & 3) >> 1)) * 16) + (n & 1) * 8; \
    unsigned ta1 = (8 * g + (n >> 2)) * 256 + ((((2 * (w_) + 1) ^ fr) * 2 + ((n & 3) >> 1)) * 16) + (n & 1) * 8; PIN(ta0); PIN(ta1)

constexpr int C_V = 41, C_WD = 43, C_WO = 47;

__global__ void __launch_bounds__(512) __attribute__((amdgpu_waves_per_eu(2, 2)))
fused_decode(const u16* __restrict__ x, const u16* __restrict__ gw, char* __restrict__ ws, const u16* __restrict__ wq, const u16* __restrict__ wg,
             const u16* __restrict__ wu, const u16* __restrict__ kc, const u16* __restrict__ vc, const u16* __restrict__ wo, const u16* __restrict__ wd,
             u16* __restrict__ out) {
  extern __shared__ __attribute__((aligned(16))) char smem[];
  LAS char* L = (LAS char*)smem;
  const unsigned lbase = (unsigned)(uintptr_t)L;
  const int c = blockIdx.x, lane = threadIdx.x & 63, wv = __builtin_amdgcn_readfirstlane(threadIdx.x >> 6);
  const int xc = c & 7, j = c >> 3;
  const int kvh = xc, b = j & 1, sp = j >> 1;
  const int rg = 8 * xc + (j >> 2), cg = j & 3;
  const int qp = xc >> 1, qc = 32 * (xc & 1) + j;
  const bool has_f = j < 28;
  const int sk = has_f ? 0 : 4;
  const int g = lane >> 4, n = lane & 15;
  const int rot = (j >> 2) * 2 + (j & 1);
  const size_t kvoff = ((size_t)(b * 8 + kvh) * 16384 + 12288 + sp * 256) * 256;
  unsigned stamp[16];
  for (int i = 0; i < 16; ++i) stamp[i] = 0;
  asm volatile("" ::"s"(x), "s"(kc), "s"(vc), "s"(wq), "s"(wo), "s"(wg), "s"(wu), "s"(wd), "s"(gw), "s"(out), "s"(ws));
  const unsigned t0 = (unsigned)__builtin_amdgcn_s_memrealtime();
  ST(0);
  if (wv == 4) {
    const char* pc;
    asm volatile("s_getpc_b64 %0" : "=s"(pc));
    asm volatile("s_mov_b32 m0, %2\n\ts_nop 0\n\tglobal_load_lds_dword %0, %1" ::"v"(min(j, CODE_PF) * 512 + (lane & 7) * 64), "s"((const char*)((uintptr_t)pc & ~(uintptr_t)255)), "s"(lbase + L_OB) : "memory");
  }

  if (wv >= 4) {
    const int hw = wv - 4;
    const unsigned fh = L_FH + 4 * hw;
    __builtin_amdgcn_s_setprio(2);
    if (hw == 1) {
      const char* a = lane == 0 ? (const char*)vc + kvoff : lane == 1 ? (const char*)wo + (size_t)rg * 65536 : lane == 2 ? (const char*)wd + (size_t)rg * 28672
                    : lane == 3 ? (const char*)out : (const char*)kc + kvoff;
      asm volatile("s_mov_b32 m0, %1\n\ts_nop 0\n\tglobal_load_lds_dword %0, off" ::"v"(a), "s"(lbase + L_OB) : "memory");
    }
    __builtin_amdgcn_s_barrier();
    wait4<15, 0>(L, L_FS, 1);
    const unsigned t32 = __builtin_amdgcn_readfirstlane(*(LAS unsigned*)(L + L_P + 1024)) + 1, t12 = t32 & 0xFFFu, t8 = t32 & 0xFFu;
    ST(1);
    if (hw < 2) {
      v4u xq[4];
#pragma unroll
      for (int s = 0; s < 4; ++s) xq[s] = *(LAS v4u*)(L + L_X + (n & 1) * 1024 + qp * 256 + s * 64 + g * 16);
      const int col = 64 * qc + 32 * hw + 16 * ((n >> 1) & 1) + 4 * g;
      const unsigned qoff = WS_Q + (((((col >> 9) * 2 + (n & 1)) * 4 + ((col >> 7) & 3)) * 4 + qp) * 128 + (col & 127)) * 4;
      LAS char* a = L + L_WQ + (8 * g + (n >> 2)) * 128 + hw * 64 + (n & 3) * 8;
      wait4<15, 0>(L, L_FS, 5);
      ST(2);
      v4u af[8];
#pragma unroll
      for (int s = 0; s < 4; ++s) { af[2 * s] = tr2(a + s * 4096, 512); af[2 * s + 1] = tr2(a + s * 4096 + 32, 512); }
      SB();
      f32x4 d0 = {0, 0, 0, 0}, d1 = {0, 0, 0, 0};
#pragma unroll
      for (int s = 0; s < 4; ++s) { d0 = mfma(af[2 * s], xq[s], d0); d1 = mfma(af[2 * s + 1], xq[s], d1); }
      v4u gr;
#pragma unroll
      for (int r = 0; r < 4; ++r) gr[r] = tagf<8>((n & 2) ? d1[r] : d0[r], t8);
      st_sc1<0x000F000F000F000Full>(qoff, gr, ws);
    } else {
      if (hw == 2 && c < 8) st_sc1<1>(WS_T + c * 128, v4u{t0, t32, t0, t32}, ws);
      {
        const v4u gwv = *(LAS v4u*)(L + L_P + lane * 16), xv = *(LAS v4u*)(L + L_X + (hw & 1) * 1024 + lane * 16);
        float s = 0.f;
#pragma unroll
        for (int k = 0; k < 4; ++k) {
          const unsigned pr = pk_bf16(asf(xv[k] << 16) * asf(gwv[k] << 16), asf(xv[k] & 0xFFFF0000u) * asf(gwv[k] & 0xFFFF0000u));
          s += asf(pr << 16) + asf(pr & 0xFFFF0000u);
        }
        s += dppf<XOR1>(s); s += dppf<XOR2>(s); s += dppf<ROR4>(s); s += dppf<ROR8>(s); s = sw16(s, s); s = sw32(s, s);
        const float gs = rbf(frcp(1.f + fexp(-rbf(s))));
        if (lane == 0) *(LAS f32x2*)(L + L_GATE + (hw & 1) * 8) = f32x2{gs, rbf(1.f - gs)};
        lds_set(L, L_TAG, t32);
      }
      v4u xg[2];
#pragma unroll
      for (int s = 0; s < 2; ++s) xg[s] = *(LAS v4u*)(L + L_X + (n & 1) * 1024 + xc * 128 + s * 64 + g * 16);
      const unsigned hoff = WS_H + ((((n & 1) * 2 + (hw & 1)) * 8 + xc) * 448 + 16 * j + 4 * ((n >> 1) & 3) + g) * 16;
      LAS char* a = L + L_GU + (hw & 1) * 8192 + (8 * g + (n >> 2)) * 128 + (n & 3) * 8;
      wait4<15, 0>(L, L_FS, 9 - sk);
      ST(2);
      v4u af[8];
#pragma unroll
      for (int s = 0; s < 2; ++s)
#pragma unroll
        for (int t = 0; t < 4; ++t) af[4 * s + t] = tr2(a + s * 4096 + t * 32, 512);
      SB();
      f32x4 d[4];
#pragma unroll
      for (int t = 0; t < 4; ++t) d[t] = mfma(af[4 + t], xg[1], mfma(af[t], xg[0], f32x4{0, 0, 0, 0}));
      v4u gr;
#pragma unroll
      for (int r = 0; r < 4; ++r) { const float lo = (n & 2) ? d[1][r] : d[0][r], hi = (n & 2) ? d[3][r] : d[2][r]; gr[r] = tagf<12>((n & 4) ? hi : lo, t12); }
      PIN(gr);
      if (has_f) st_sc1<0x00FF00FF00FF00FFull>(hoff, gr, ws);
    }
    lds_set(L, fh, 1);
    ST(3);
    if (hw == 1) {
      wait_until(L, t0, TT);
      v4u tv;
      ld1w(tv, WS_T + (lane & 7) * 128, ws);
      const bool ok = tv.y == t32 && tv.w == t32;
      float d = ok ? (float)(int)(tv.x - t0) : 0.f, k = ok ? 1.f : 0.f;
      d += dppf<XOR1>(d); k += dppf<XOR1>(k); d += dppf<XOR2>(d); k += dppf<XOR2>(k); d += dppf<HMIR>(d); k += dppf<HMIR>(k);
      lds_set(L, L_DT, (int)fminf(fmaxf(d * 16.f * frcp(fmaxf(k, 1.f)), -1600.f), 1600.f));
    }
    {
      const unsigned qo = WS_Q + ((kvh * 2 + b) * 4 + hw) * 2048 + lane * 16;
      wait_until(L, t0, TQ);
      v4u q0, q1;
      CNT0(14); CNT0(15);
#pragma unroll 1
      for (int it = 0; it < SPIN_CAP; ++it) { ld2w(q0, q1, qo, qo + 1024, ws); if (!anybad<8>(neq(q0, t8) | neq(q1, t8))) break; CNT(14); }
      float v[4];
#pragma unroll
      for (int r = 0; r < 4; ++r) { v[r] = untag<8>(q0[r]) + untag<8>(q1[r]); v[r] = sw32(v[r], v[r]); }
      if (lane < 32) *(LAS v2u*)(L + L_Q + hw * QSTR + lane * 8) = v2u{pk_bf16(v[0], v[1]), pk_bf16(v[2], v[3])};
      LGKM0(); lds_set(L, fh, 2);
      ST(4);
    }
    wait4<15, 1>(L, L_FH, 2);
    v4u qf[4], kf[16];
#pragma unroll
    for (int s = 0; s < 4; ++s) qf[s] = *(LAS v4u*)(L + L_Q + (n & 3) * QSTR + s * 64 + g * 16);
    wait4<15, 0>(L, L_FS, 13 + 4 * hw - sk);
    ST(6);
#pragma unroll
    for (int s = 0; s < 4; ++s)
#pragma unroll
      for (int t = 0; t < 4; ++t) kf[s * 4 + t] = *(LAS v4u*)(L + L_K + (4 * hw + t) * 4096 + n * 256 + (((4 * s + g) ^ n) * 16));
    SB();
    float sc[4];
    {
      f32x4 d[4];
#pragma unroll
      for (int t = 0; t < 4; ++t) d[t] = f32x4{0, 0, 0, 0};
#pragma unroll
      for (int s = 0; s < 4; ++s)
#pragma unroll
        for (int t = 0; t < 4; ++t) d[t] = mfma(kf[s * 4 + t], qf[s], d[t]);
      constexpr float SC = 0.08838834764831845f;
      float v[4];
#pragma unroll
      for (int r = 0; r < 4; ++r) { const float lo = (n & 4) ? d[1][r] : d[0][r], hi = (n & 4) ? d[3][r] : d[2][r]; v[r] = (n & 8) ? hi : lo; }
      const unsigned a = pk_bf16(v[0], v[1]), bq = pk_bf16(v[2], v[3]);
      const unsigned a2 = pk_bf16(asf(a << 16) * SC, asf(a & 0xFFFF0000u) * SC), b2 = pk_bf16(asf(bq << 16) * SC, asf(bq & 0xFFFF0000u) * SC);
      sc[0] = asf(a2 << 16); sc[1] = asf(a2 & 0xFFFF0000u); sc[2] = asf(b2 << 16); sc[3] = asf(b2 & 0xFFFF0000u);
    }
    float m = fmaxf(fmaxf(sc[0], sc[1]), fmaxf(sc[2], sc[3]));
    m = fmaxf(m, dppf<ROR4>(m)); m = fmaxf(m, dppf<ROR8>(m)); m = x16max(m); m = x32max(m);
    if (lane < 4) *(LAS float*)(L + L_MX + lane * 16 + hw * 4) = m;
    LGKM0(); lds_set(L, fh, 3);
    wait4<15, 0>(L, L_FH, 3);
    const int e = lane & 7;
    const unsigned ho = WS_H + (((hw & 1) * 16 + (lane >> 3)) * 448 + 7 * rg + (e < 7 ? e : 6)) * 16;
    if (hw >= 2) { DMA_S(" sc1", ho, ws, lbase + L_K + hw * 2048); DMA_S(" sc1", ho + 8 * 448 * 16, ws, lbase + L_K + hw * 2048 + 1024); }
    { const f32x4 mm = *(LAS f32x4*)(L + L_MX + (n & 3) * 16); m = fmaxf(fmaxf(mm[0], mm[1]), fmaxf(mm[2], mm[3])); }
    {
      float p[4];
#pragma unroll
      for (int r = 0; r < 4; ++r) p[r] = fexp(sc[r] - m);
      *(LAS v2u*)(L + L_P + (n & 3) * PSTR + (64 * hw + 16 * (n >> 2) + 4 * g) * 2) = v2u{pk_bf16(p[0], p[1]), pk_bf16(p[2], p[3])};
      float l = (p[0] + p[1]) + (p[2] + p[3]);
      l += dppf<ROR4>(l); l += dppf<ROR8>(l); l = sw16(l, l); l = sw32(l, l);
      if (lane < 4) *(LAS float*)(L + L_SUM + lane * 16 + hw * 4) = l;
    }
    LGKM0(); lds_set(L, fh, 4);
    ST(7);
    TR_ADDR(hw);
    unsigned ooff = WS_O + (((2 * (4 * kvh + (n & 3)) + (hw >> 1)) * 2 + b) * 16 + sp) * 256 + (hw & 1) * 128 + ((n >> 2) & 1) * 64 + g * 16;
    unsigned tv = t12;
    PIN(ooff); PIN(tv);
    const int tabs = t0 + ((clock_offset(L) + TVE) >> 4);
    wait4<15, 0>(L, L_FH, 4);
    v4u pa[8], pb[8];
    {
      const unsigned pp = L_P + (n & 3) * PSTR + g * 16, aa = n < 4 ? pp : L_Z, ab = (n >> 2) == 1 ? pp : L_Z;
#pragma unroll
      for (int u = 0; u < 8; ++u) { pa[u] = *(LAS v4u*)(L + aa + u * 64); pb[u] = *(LAS v4u*)(L + ab + u * 64); }
    }
    if (hw == 2) {
      const f32x4 ls = *(LAS f32x4*)(L + L_SUM + (n & 3) * 16);
      st_sc1<0xF>(WS_ST + (((kvh * 4 + (n & 3)) * 2 + b) * 16 + sp) * 16, v4u{tagt<12>(m, t12), tagf<12>((ls[0] + ls[1]) + (ls[2] + ls[3]), t12), t12, t12}, ws);
    }
    f32x4 o = {0, 0, 0, 0};
#define PVN(u0, nu) do { v4u va[nu], vb[nu]; _Pragma("unroll") for (int u = 0; u < nu; ++u) { va[u] = tr2(L + L_V + ta0 + (u0 + u) * 8192, 1024); vb[u] = tr2(L + L_V + ta1 + (u0 + u) * 8192, 1024); } SB(); \
      _Pragma("unroll") for (int u = 0; u < nu; ++u) { o = mfma(va[u], pa[u0 + u], o); o = mfma(vb[u], pb[u0 + u], o); } } while (0)
    wait4<15, 1>(L, L_FS, 33 - sk);
    PVN(0, 4); PIN(o);
    ST(8);
    if (hw >= 2) {
      if (hw == 2) vmcnt<1>(); else vmcnt<0>();
      v4u h0 = *(LAS v4u*)(L + L_K + hw * 2048 + lane * 16), h1 = *(LAS v4u*)(L + L_K + hw * 2048 + 1024 + lane * 16);
#pragma unroll 1
      for (int it = 0; it < SPIN_CAP && anybad<12>(neq(h0, t12) | neq(h1, t12)); ++it) { ld2w(h0, h1, ho, ho + 8 * 448 * 16, ws); CNT(14); }
      float v[4];
#pragma unroll
      for (int r = 0; r < 4; ++r) v[r] = sw32(untag<12>(h0[r]), untag<12>(h1[r]));
      const float a0 = sw16(v[0], v[1]), a1 = sw16(v[2], v[3]);
      const float b0 = a0 + dppf<ROR8>(a0), b1 = a1 + dppf<ROR8>(a1);
      const float gg = rbf((lane & 8) ? b1 : b0);
      const auto sw = __builtin_amdgcn_permlane32_swap(asu(gg), asu(gg), false, false);
      const float hv = rbf(gg * frcp(1.f + fexp(-gg))) * asf(sw[1]);
      if (lane < 32) *(LAS u16*)(L + L_HB + (hw & 1) * 64 + (4 * e + ((lane >> 2) & 2) + (lane >> 4)) * 2) = e < 7 ? (u16)(pk_bf16(hv, hv) & 0xFFFFu) : (u16)0;
      LGKM0(); lds_set(L, fh, 5);
      ST(5);
    }
    wait4<15, 0>(L, L_FS, 39 - sk);
    PVN(4, 3); PIN(o);
    const unsigned so = WS_ST + (((rg >> 1) * 2 + (hw & 1)) * 16 + 4 * (n & 3) + g) * 16;
    wait4<15, 0>(L, L_FS, C_V - sk);
    ST(9);
    PVN(7, 1);
    st_sc1<0x00FF00FF00FF00FFull>(ooff, v4u{tagt<12>(o[0], tv), tagt<12>(o[1], tv), tagt<12>(o[2], tv), tagt<12>(o[3], tv)}, ws);
    ST(10);
    if (hw < 2) {
      unsigned oo = WS_O + (rg * 2 + hw) * 4096 + lane * 16, oba = L_OB + hw * 128 + (4 * n + (g & 1) * 2 + (g >> 1)) * 2;
      DMA_S(" sc1", so, ws, lbase + L_K + hw * 1024);
      const float gate = *(LAS float*)(L + L_GATE + hw * 8);
      PIN(oo); PIN(oba);
      spin_until(tabs + (TO >> 4));
      ST(11);
      v4u a0, a1, a2, a3;
      asm volatile("global_load_dwordx4 %0, %4, %5 sc1\n\tglobal_load_dwordx4 %1, %4, %5 offset:1024 sc1\n\tglobal_load_dwordx4 %2, %4, %5 offset:2048 sc1\n\t"
                   "global_load_dwordx4 %3, %4, %5 offset:3072 sc1\n\ts_waitcnt vmcnt(4)" : "=&v"(a0), "=&v"(a1), "=&v"(a2), "=&v"(a3) : "v"(oo), "s"(ws) : "memory");
      v4u st = *(LAS v4u*)(L + L_K + hw * 1024 + lane * 16);
      if (anybad<12>(neq(st, t12))) {
#pragma unroll 1
        for (int it = 0; it < SPIN_CAP; ++it) { CNT(14); ld1w(st, so, ws); if (!anybad<12>(neq(st, t12))) break; }
      }
      const float ms = untag<12>(st.x), ls = untag<12>(st.y);
      float M = ms; M = fmaxf(M, dppf<XOR1>(M)); M = fmaxf(M, dppf<XOR2>(M)); M = x16max(M); M = x32max(M);
      const float ex = fexp(ms - M);
      float Ls = ex * ls; Ls += dppf<XOR1>(Ls); Ls += dppf<XOR2>(Ls); Ls = sw16(Ls, Ls); Ls = sw32(Ls, Ls);
      const float wv_ = gate * ex * frcp(Ls);
      const float wt0 = dppf<0x00>(wv_), wt1 = dppf<0x55>(wv_), wt2 = dppf<0xAA>(wv_), wt3 = dppf<0xFF>(wv_);
      f32x2 w0 = {wt0, wt0}, w1 = {wt1, wt1}, w2 = {wt2, wt2}, w3 = {wt3, wt3};
      PIN(w0); PIN(w1); PIN(w2); PIN(w3);
      asm volatile("s_waitcnt vmcnt(0)" : "+v"(a0), "+v"(a1), "+v"(a2), "+v"(a3)::"memory");
      ST(12);
#pragma unroll 1
      for (int it = 0; it < SPIN_CAP; ++it) {
#define UT2(a, i) f32x2{untag<12>(a[i]), untag<12>(a[i + 1])}
        const f32x2 lo = __builtin_elementwise_fma(w0, UT2(a0, 0), w1 * UT2(a1, 0)) + __builtin_elementwise_fma(w2, UT2(a2, 0), w3 * UT2(a3, 0));
        const f32x2 hi = __builtin_elementwise_fma(w0, UT2(a0, 2), w1 * UT2(a1, 2)) + __builtin_elementwise_fma(w2, UT2(a2, 2), w3 * UT2(a3, 2));
        const float t = sw16(sw32(lo[0], lo[1]), sw32(hi[0], hi[1]));
        *(volatile LAS u16*)(L + oba) = (u16)(pk_bf16(t, t) & 0xFFFFu);
        SB();
        if (!anybad<12>(neq(a0, t12) | neq(a1, t12) | neq(a2, t12) | neq(a3, t12))) break;
        CNT(15);
        ld4w(a0, a1, a2, a3, oo, ws);
      }
      LGKM0();
      ST(13);
      __builtin_amdgcn_s_barrier();
    } else if (hw == 2) {
      __builtin_amdgcn_s_barrier();
      { v4u tv; ld1w(tv, WS_T + (lane & 7) * 128, ws);
        int v[8];
        for (int k = 0; k < 8; ++k) v[k] = __builtin_amdgcn_readlane((int)(tv.x - t0), k);
        for (int a = 0; a < 8; ++a) for (int z = 0; z < 7; ++z) { int lo = min(v[z], v[z + 1]), hi = max(v[z], v[z + 1]); v[z] = lo; v[z + 1] = hi; }
        stamp[15] = stamp[0] + (PSK < 0 ? -v[0] : v[PSK < 0 ? 0 : PSK] - v[0]); }
    } else {
      unsigned yo = WS_Y + (((xc >> 1) * 64 + lane) * 2 + (xc & 1)) * 512 + j * 16;
      unsigned op = ((xc & 1) * 512 + (xc >> 1) * 128 + 4 * j + (g & 1) * 2 + (g >> 1)) * 2;
      PIN(yo); PIN(op);
      __builtin_amdgcn_s_barrier();
      __builtin_amdgcn_s_sleep(4);
      spin_until(tabs + ((TO + TY) >> 4));
      ST(11);
      float t;
#pragma unroll 1
      for (int it = 0; it < SPIN_CAP; ++it) {
        v4u y;
        ld1w(y, yo, ws);
        ST(12);
        t = sw16(sw32(untag<12>(y.x), untag<12>(y.y)), sw32(untag<12>(y.z), untag<12>(y.w)));
        t += dppf<XOR1>(t); t += dppf<XOR2>(t); t += dppf<ROR4>(t); t += dppf<ROR8>(t);
        if (!anybad<12>(neq(y, t12))) break;
        CNT(15);
      }
      asm volatile("s_mov_b64 exec, %3\n\tglobal_store_short %0, %1, %2\n\ts_mov_b64 exec, -1" ::"v"(op), "v"(pk_bf16(t, t)), "s"(out), "s"(0x0001000100010001ull) : "memory");
      if (c == 0) asm volatile("s_mov_b64 exec, 1\n\tglobal_store_dword %0, %1, %2 sc1\n\ts_mov_b64 exec, -1" ::"v"(0), "v"(t32), "s"(ws) : "memory");
      ST(13);
    }
  } else {
    const int w = wv;
    const unsigned fs = L_FS + 4 * w;
    int issued = 5, landed = 0, due = TK0 + w * (TAU / 4), seen = 0, dt = 0;
#define PUBLISH(vm_) do { if (issued - (vm_) != landed) { landed = issued - (vm_); lds_set(L, fs, landed); } } while (0)
#define PACE() do { \
      const int v0_ = vm_inflight(); PUBLISH(v0_); \
      if (seen < due || v0_ >= WIN) { \
        const int d_ = *(volatile LAS int*)(L + L_DT); \
        _Pragma("unroll 1") for (int it_ = 0; it_ < SPIN_CAP; ++it_) { \
          seen = now16(t0) - dt; \
          const int vm_ = vm_inflight(); PUBLISH(vm_); \
          if (seen >= due && vm_ < WIN) break; } \
        const int dn_ = __builtin_amdgcn_readfirstlane(d_); seen += dt - dn_; dt = dn_; } \
      due += TAU; ++issued; } while (0)
    {
      const char* sb = w == 0 ? (const char*)x : w == 1 ? (const char*)x + 1024 : w == 2 ? (const char*)gw : (const char*)ws;
      DMA_S("", w == 3 ? 0 : lane * 16, sb, lbase + (w < 2 ? L_X : L_P - 2048) + w * 1024);
    }
    {
      const char* base = (const char*)wq + (size_t)(128 * qp + 8 * w) * 8192 + qc * 128;
      const unsigned voff = (lane >> 3) * 8192 + (lane & 7) * 16;
#pragma unroll
      for (int i = 0; i < 4; ++i) DMA_S(" nt", voff, base + (size_t)i * 32 * 8192, lbase + L_WQ + (4 * i + w) * 1024);
    }
    if (w == 0 && lane < 9) *(volatile LAS int*)(L + L_FS + 4 * lane) = lane < 8 ? -64 : 0;
    if (w == 1) *(volatile LAS v2u*)(L + L_Z + 8 * lane) = v2u{0, 0};
    ST(7);
    LGKM0();
    __builtin_amdgcn_s_barrier();
    if (has_f) {
      const unsigned voff = (lane >> 3) * 3584 + (lane & 7) * 16;
      const size_t off = (size_t)(64 * xc + 8 * w) * 3584 + j * 128;
#pragma unroll 1
      for (int i = 0; i < 4; ++i) { PACE(); DMA_S(" nt", voff, (const char*)(i < 2 ? wg : wu) + off + (i & 1) * (32 * 3584), lbase + L_GU + (4 * i + w) * 1024); }
    } else {
      due += 4 * TAU;
    }
    {
      const char* kb = (const char*)kc + kvoff + w * 1024;
      const unsigned voff = g * 256 + ((n ^ (4 * w + g)) * 16);
#pragma unroll 1
      for (int i = 0; i < 16; ++i) { PACE(); DMA_S(" nt", voff, kb + (((i + rot) & 15) << 12), lbase + L_K + i * 4096 + w * 1024); }
    }
    ST(1);
    {
      const unsigned voff = g * 256 + ((n ^ (2 * (g | ((w >> 1) << 2)))) * 16);
      const char* vb = (const char*)vc + kvoff + w * 1024;
#pragma unroll 1
      for (int i = 0; i < 16; ++i) {
        if (i == 8) wait4<3, 0>(L, L_FH, 1);
        if (i >= 16 - NL) due += TAUL - TAU;
        PACE(); DMA_S(" nt", voff, vb + (((i + rot) & 15) << 12), lbase + L_V + i * 4096 + w * 1024);
      }
    }
    ST(2);
    wait4<12, 0>(L, L_FH, 1);
    {
      const unsigned vo = g * 1024 + ((n ^ (2 * (g | ((w >> 1) << 2)))) * 16);
      const char* ob = (const char*)wo + (size_t)(64 * rg + 4 * w) * 1024 + cg * 256;
      const char* db = (const char*)wd + (size_t)(28 * rg) * 1024 + cg * 256;
#pragma unroll 1
      for (int i = 0; i < 6; ++i) {
        const char* src = i >= 2 ? ob + (i - 2) * 16384 : db + (i == 0 ? 4 * w : w < 3 ? 16 + 4 * w : 0) * 1024;
        due += TAU2 - TAU; PACE();
        DMA_S(" nt", vo, src, lbase + L_WO + (i >= 2 ? i - 2 : i + 4) * 4096 + w * 1024);
      }
    }
    ST(3);
#pragma unroll 1
    for (int it = 0; it < SPIN_CAP && landed < C_WD - sk; ++it) { const int vm = vm_inflight(); PUBLISH(vm); }
    ST(4);
    unsigned t12 = *(volatile LAS unsigned*)(L + L_TAG) & 0xFFFu;
    TR_ADDR(w);
    unsigned yoff = WS_Y + ((cg * 64 + rg) * 2 + (n & 1)) * 512 + w * 128 + ((n >> 1) & 1) * 64 + g * 16;
    unsigned oa = lbase + (n < 2 ? L_OB + n * 128 + g * 16 : L_Z), ob = lbase + ((n >> 1) == 1 ? L_OB + (n & 1) * 128 + g * 16 : L_Z);
    const float g1 = *(LAS float*)(L + L_GATE + (n & 1) * 8 + 4);
    wait4<12, 0>(L, L_FH, 5);
    const v4u ha = *(LAS v4u*)(L + (n < 2 ? L_HB + n * 64 + g * 16 : L_Z)), hb = *(LAS v4u*)(L + ((n >> 1) == 1 ? L_HB + (n & 1) * 64 + g * 16 : L_Z));
    wait4<15, 0>(L, L_FS, C_WD - sk);
    f32x4 y = mfma(tr2(L + L_WD + ta1, 1024), hb, mfma(tr2(L + L_WD + ta0, 1024), ha, f32x4{0, 0, 0, 0})) * g1;
    PIN(y);
    vmcnt<0>(); lds_set(L, fs, C_WO - sk);
    ST(5);
    wait4<15, 0>(L, L_FS, C_WO - sk);
    v4u wf[4];
    wf[0] = tr2(L + L_WO + ta0, 1024); wf[1] = tr2(L + L_WO + ta1, 1024); wf[2] = tr2(L + L_WO + ta0 + 8192, 1024); wf[3] = tr2(L + L_WO + ta1 + 8192, 1024);
    PIN(wf[0]); PIN(wf[1]); PIN(wf[2]); PIN(wf[3]); PIN(oa); PIN(ob); PIN(yoff); PIN(t12);
    __builtin_amdgcn_s_setprio(3);
    ST(6);
    __builtin_amdgcn_s_barrier();
    asm volatile("" ::: "memory");
    ST(10);
    {
      const v4u a0 = *(LAS v4u*)(uintptr_t)oa, b0 = *(LAS v4u*)(uintptr_t)ob, a1 = *(LAS v4u*)(uintptr_t)(oa + 64), b1 = *(LAS v4u*)(uintptr_t)(ob + 64);
      SB();
      y = mfma(wf[0], a0, y); y = mfma(wf[1], b0, y); y = mfma(wf[2], a1, y); y = mfma(wf[3], b1, y);
      st_sc1<0x000F000F000F000Full>(yoff, v4u{tagt<12>(y[0], t12), tagt<12>(y[1], t12), tagt<12>(y[2], t12), tagt<12>(y[3], t12)}, ws);
    }
    ST(11);
  }
  if (c == PWG && wv == PWAVE) {
    const unsigned tgt = 4000u + (PROBE >= 0 ? 8u * (unsigned)(stamp[PROBE >= 0 ? PROBE : 0] - stamp[0]) : 0u);
    for (int it = 0; it < 1000000; ++it) { if ((unsigned)__builtin_amdgcn_s_memrealtime() - stamp[0] >= tgt) break; }
  }
}

extern "C" void kernel_launch(void* const* d_in, const int* in_sizes, int n_in, void* d_out, int out_size, void* d_ws, size_t ws_size, hipStream_t stream) {
  (void)hipFuncSetAttribute((const void*)fused_decode, hipFuncAttributeMaxDynamicSharedMemorySize, L_END);
  fused_decode<<<256, 512, L_END, stream>>>((const u16*)d_in[0], (const u16*)d_in[9], (char*)d_ws, (const u16*)d_in[3], (const u16*)d_in[6], (const u16*)d_in[7],
                                            (const u16*)d_in[1], (const u16*)d_in[2], (const u16*)d_in[4], (const u16*)d_in[8], (u16*)d_out);
}
```

```cpp
#define PROBE 0
#define PWAVE 7
#define SMASK 1
#define TO 3000
#define TY 1750
#include <hip/hip_runtime.h>
#include <hip/hip_bf16.h>
#include <stdint.h>

#define STAMP 0
#define TAU 256
#define TK0 440
#define TAUL TAU
#define NL 0
#define TAU2 256
#define TT 2900
#define TQ 4300
#define TVE (TK0 + (35 - NL) * TAU + NL * TAUL)
#define WIN 12
#define CODE_PF 25

typedef unsigned short u16;
typedef unsigned long long u64;
typedef unsigned v4u __attribute__((ext_vector_type(4)));
typedef int v4i __attribute__((ext_vector_type(4)));
typedef unsigned v2u __attribute__((ext_vector_type(2)));
typedef short v4s __attribute__((ext_vector_type(4)));
typedef float f32x4 __attribute__((ext_vector_type(4)));
typedef float f32x2 __attribute__((ext_vector_type(2)));
typedef __bf16 v8bf __attribute__((ext_vector_type(8)));
typedef __bf16 v2bf __attribute__((ext_vector_type(2)));
#define GAS __attribute__((address_space(1)))
#define LAS __attribute__((address_space(3)))
#define SB() __builtin_amdgcn_sched_barrier(0)

constexpr unsigned WS_T = 1024;
constexpr unsigned WS_Q = 4096;
constexpr unsigned WS_H = WS_Q + 131072;
constexpr unsigned WS_ST = WS_H + 229376;
constexpr unsigned WS_O = WS_ST + 16384;
constexpr unsigned WS_Y = WS_O + 524288;
constexpr unsigned WS_STAMP = 4u << 20;
constexpr unsigned L_K = 0, L_V = 65536, L_WQ = L_V + 8 * 4096, L_WO = 131072, L_GU = L_WO, L_WD = L_WO + 16384, L_X = L_WD + 8192,
                   L_Q = L_X + 2048, QSTR = 320, L_P = L_Q + 4 * QSTR, PSTR = 576, L_OB = L_P + 4 * PSTR, L_HB = L_OB + 256, L_MX = L_HB + 128,
                   L_SUM = L_MX + 64, L_GATE = L_SUM + 64, L_FS = L_GATE + 16, L_FH = L_FS + 16, L_DT = L_FH + 16, L_TAG = L_DT + 4,
                   L_Z = L_DT + 16, L_END = L_Z + 512;
static_assert(L_END <= 163840, "LDS");
constexpr int SPIN_CAP = 1 << 17;

__device__ __forceinline__ float asf(unsigned u) { return __builtin_bit_cast(float, u); }
__device__ __forceinline__ unsigned asu(float f) { return __builtin_bit_cast(unsigned, f); }
__device__ __forceinline__ unsigned pk_bf16(float lo, float hi) { f32x2 x = {lo, hi}; return __builtin_bit_cast(unsigned, __builtin_convertvector(x, v2bf)); }
__device__ __forceinline__ float rbf(float f) { return asf(pk_bf16(f, f) << 16); }
template <int CTRL> __device__ __forceinline__ float dppf(float x) { return asf(__builtin_amdgcn_mov_dpp(asu(x), CTRL, 0xf, 0xf, true)); }
constexpr int XOR1 = 0xB1, XOR2 = 0x4E, HMIR = 0x141, ROR4 = 0x124, ROR8 = 0x128;
__device__ __forceinline__ float sw16(float a, float b) { auto s = __builtin_amdgcn_permlane16_swap(asu(a), asu(b), false, false); return asf(s[0]) + asf(s[1]); }
__device__ __forceinline__ float sw32(float a, float b) { auto s = __builtin_amdgcn_permlane32_swap(asu(a), asu(b), false, false); return asf(s[0]) + asf(s[1]); }
__device__ __forceinline__ float x16max(float x) { auto s = __builtin_amdgcn_permlane16_swap(asu(x), asu(x), false, false); return fmaxf(asf(s[0]), asf(s[1])); }
__device__ __forceinline__ float x32max(float x) { auto s = __builtin_amdgcn_permlane32_swap(asu(x), asu(x), false, false); return fmaxf(asf(s[0]), asf(s[1])); }
__device__ __forceinline__ float frcp(float x) { return __builtin_amdgcn_rcpf(x); }
__device__ __forceinline__ float fexp(float x) { return __builtin_amdgcn_exp2f(x * 1.4426950408889634f); }
__device__ __forceinline__ f32x4 mfma(v4u a, v4u b, f32x4 c) { return __builtin_amdgcn_mfma_f32_16x16x32_bf16(__builtin_bit_cast(v8bf, a), __builtin_bit_cast(v8bf, b), c, 0, 0, 0); }
__device__ __forceinline__ v2u trread(LAS char* p) { return __builtin_bit_cast(v2u, __builtin_amdgcn_ds_read_tr16_b64_v4i16((LAS v4s*)p)); }
__device__ __forceinline__ v4u tr2(LAS char* p, int d) { v2u a = trread(p), b = trread(p + d); return v4u{a.x, a.y, b.x, b.y}; }
template <int B> __device__ __forceinline__ unsigned tagt(float v, unsigned t) { return (asu(v) & ~((1u << B) - 1)) | t; }
template <int B> __device__ __forceinline__ unsigned tagf(float v, unsigned t) { return ((asu(v) + (1u << (B - 1))) & ~((1u << B) - 1)) | t; }
template <int B> __device__ __forceinline__ float untag(unsigned w) { return asf(w & ~((1u << B) - 1)); }
__device__ __forceinline__ unsigned neq(v4u g, unsigned t) { return __builtin_amdgcn_bitop3_b32(g.x, g.y, t, 0x7E) | __builtin_amdgcn_bitop3_b32(g.z, g.w, t, 0x7E); }
template <int B> __device__ __forceinline__ bool anybad(unsigned x) { return __builtin_amdgcn_ballot_w64((x & ((1u << B) - 1)) != 0) != 0; }
#define PIN(x) asm volatile("" : "+v"(x))
#define CNT(i)
#define CNT0(i)

#define DMA_S(MOD, voff, sbase, ldst) asm volatile("s_mov_b32 m0, %2\n\ts_nop 0\n\tglobal_load_lds_dwordx4 %0, %1" MOD :: "v"(voff), "s"(sbase), "s"(ldst) : "memory")
template <int N> __device__ __forceinline__ void vmcnt() { asm volatile("s_waitcnt vmcnt(%0)" ::"i"(N) : "memory"); }
#define LGKM0() asm volatile("s_waitcnt lgkmcnt(0)" ::: "memory")
__device__ __forceinline__ void ld1w(v4u& a, unsigned o0, const char* sb) {
  asm volatile("global_load_dwordx4 %0, %1, %2 sc1\n\ts_waitcnt vmcnt(0)" : "=&v"(a) : "v"(o0), "s"(sb) : "memory"); }
__device__ __forceinline__ void ld2w(v4u& a, v4u& b, unsigned o0, unsigned o1, const char* sb) {
  asm volatile("global_load_dwordx4 %0, %2, %4 sc1\n\tglobal_load_dwordx4 %1, %3, %4 sc1\n\ts_waitcnt vmcnt(0)" : "=&v"(a), "=&v"(b) : "v"(o0), "v"(o1), "s"(sb) : "memory"); }
__device__ __forceinline__ void ld4w(v4u& a, v4u& b, v4u& c, v4u& d, unsigned o0, const char* sb) {
  asm volatile("global_load_dwordx4 %0, %4, %5 sc1\n\tglobal_load_dwordx4 %1, %4, %5 offset:1024 sc1\n\tglobal_load_dwordx4 %2, %4, %5 offset:2048 sc1\n\t"
               "global_load_dwordx4 %3, %4, %5 offset:3072 sc1\n\ts_waitcnt vmcnt(0)" : "=&v"(a), "=&v"(b), "=&v"(c), "=&v"(d) : "v"(o0), "s"(sb) : "memory"); }
template <u64 MASK> __device__ __forceinline__ void st_sc1(unsigned voff, v4u d, char* sbase) {
  asm volatile("s_mov_b64 exec, %3\n\tglobal_store_dwordx4 %0, %1, %2 sc1\n\ts_mov_b64 exec, -1\n\ts_nop 0" ::"v"(voff), "v"(d), "s"(sbase), "s"(MASK) : "memory"); }

#define ST(i) do { if ((SMASK >> (i)) & 1) { SB(); stamp[i] = (unsigned)__builtin_amdgcn_s_memrealtime(); SB(); } } while (0)

__device__ __forceinline__ int now16(unsigned t0) { return (int)(((unsigned)__builtin_amdgcn_s_memrealtime() - t0) * 16u); }
__device__ __forceinline__ int vm_inflight() { unsigned r; asm volatile("s_getreg_b32 %0, hwreg(HW_REG_IB_STS, 0, 4)" : "=s"(r)::"memory"); return (int)r; }
__device__ __forceinline__ int clock_offset(LAS char* L) { return __builtin_amdgcn_readfirstlane(*(volatile LAS int*)(L + L_DT)); }
__device__ __forceinline__ void wait_until(LAS char* L, unsigned t0, int t) {
#pragma unroll 1
  for (int it = 0; it < SPIN_CAP && now16(t0) - clock_offset(L) < t; ++it) __builtin_amdgcn_s_sleep(1);
}
__device__ __forceinline__ void spin_until(unsigned tick) {
#pragma unroll 1
  for (int it = 0; it < SPIN_CAP && (int)((unsigned)__builtin_amdgcn_s_memrealtime() - tick) < 0; ++it) {}
}
__device__ __forceinline__ void lds_set(LAS char* L, unsigned off, unsigned v) {
  asm volatile("s_mov_b64 exec, 1\n\tds_write_b32 %0, %1\n\ts_mov_b64 exec, -1" ::"v"((unsigned)(uintptr_t)(L + off)), "v"(v) : "memory");
}
template <int MASK, int SLEEP> __device__ __forceinline__ void wait4(LAS char* L, unsigned off, int n) {
#pragma unroll 1
  for (int it = 0; it < SPIN_CAP; ++it) {
    const v4i f = *(volatile LAS v4i*)(L + off);
    int m = 0x7fffffff;
    if (MASK & 1) m = min(m, f.x);
    if (MASK & 2) m = min(m, f.y);
    if (MASK & 4) m = min(m, f.z);
    if (MASK & 8) m = min(m, f.w);
    if (__builtin_amdgcn_readfirstlane(m) >= n) break;
    if (SLEEP) __builtin_amdgcn_s_sleep(SLEEP);
  }
  asm volatile("" ::: "memory");
}
#define TR_ADDR(w_) const int fr = (n >> 2) | ((g & 1) << 2); \
    unsigned ta0 = (8 * g + (n >> 2)) * 256 + ((((2 * (w_)) ^ fr) * 2 + ((n & 3) >> 1)) * 16) + (n & 1) * 8; \
    unsigned ta1 = (8 * g + (n >> 2)) * 256 + ((((2 * (w_) + 1) ^ fr) * 2 + ((n & 3) >> 1)) * 16) + (n & 1) * 8; PIN(ta0); PIN(ta1)

constexpr int C_V = 41, C_WD = 43, C_WO = 47;

__global__ void __launch_bounds__(512) __attribute__((amdgpu_waves_per_eu(2, 2)))
fused_decode(const u16* __restrict__ x, const u16* __restrict__ gw, char* __restrict__ ws, const u16* __restrict__ wq, const u16* __restrict__ wg,
             const u16* __restrict__ wu, const u16* __restrict__ kc, const u16* __restrict__ vc, const u16* __restrict__ wo, const u16* __restrict__ wd,
             u16* __restrict__ out) {
  extern __shared__ __attribute__((aligned(16))) char smem[];
  LAS char* L = (LAS char*)smem;
  const unsigned lbase = (unsigned)(uintptr_t)L;
  const int c = blockIdx.x, lane = threadIdx.x & 63, wv = __builtin_amdgcn_readfirstlane(threadIdx.x >> 6);
  const int xc = c & 7, j = c >> 3;
  const int kvh = xc, b = j & 1, sp = j >> 1;
  const int rg = 8 * xc + (j >> 2), cg = j & 3;
  const int qp = xc >> 1, qc = 32 * (xc & 1) + j;
  const bool has_f = j < 28;
  const int sk = has_f ? 0 : 4;
  const int g = lane >> 4, n = lane & 15;
  const int rot = (j >> 2) * 2 + (j & 1);
  const size_t kvoff = ((size_t)(b * 8 + kvh) * 16384 + 12288 + sp * 256) * 256;
  unsigned stamp[16];
  for (int i = 0; i < 16; ++i) stamp[i] = 0;
  asm volatile("" ::"s"(x), "s"(kc), "s"(vc), "s"(wq), "s"(wo), "s"(wg), "s"(wu), "s"(wd), "s"(gw), "s"(out), "s"(ws));
  const unsigned t0 = (unsigned)__builtin_amdgcn_s_memrealtime();
  ST(0);
  if (wv == 4) {
    const char* pc;
    asm volatile("s_getpc_b64 %0" : "=s"(pc));
    asm volatile("s_mov_b32 m0, %2\n\ts_nop 0\n\tglobal_load_lds_dword %0, %1" ::"v"(min(j, CODE_PF) * 512 + (lane & 7) * 64), "s"((const char*)((uintptr_t)pc & ~(uintptr_t)255)), "s"(lbase + L_OB) : "memory");
  }

  if (wv >= 4) {
    const int hw = wv - 4;
    const unsigned fh = L_FH + 4 * hw;
    __builtin_amdgcn_s_setprio(2);
    if (hw == 1) {
      const char* a = lane == 0 ? (const char*)vc + kvoff : lane == 1 ? (const char*)wo + (size_t)rg * 65536 : lane == 2 ? (const char*)wd + (size_t)rg * 28672
                    : lane == 3 ? (const char*)out : (const char*)kc + kvoff;
      asm volatile("s_mov_b32 m0, %1\n\ts_nop 0\n\tglobal_load_lds_dword %0, off" ::"v"(a), "s"(lbase + L_OB) : "memory");
    }
    __builtin_amdgcn_s_barrier();
    wait4<15, 0>(L, L_FS, 1);
    const unsigned t32 = __builtin_amdgcn_readfirstlane(*(LAS unsigned*)(L + L_P + 1024)) + 1, t12 = t32 & 0xFFFu, t8 = t32 & 0xFFu;
    ST(1);
    if (hw < 2) {
      v4u xq[4];
#pragma unroll
      for (int s = 0; s < 4; ++s) xq[s] = *(LAS v4u*)(L + L_X + (n & 1) * 1024 + qp * 256 + s * 64 + g * 16);
      const int col = 64 * qc + 32 * hw + 16 * ((n >> 1) & 1) + 4 * g;
      const unsigned qoff = WS_Q + (((((col >> 9) * 2 + (n & 1)) * 4 + ((col >> 7) & 3)) * 4 + qp) * 128 + (col & 127)) * 4;
      LAS char* a = L + L_WQ + (8 * g + (n >> 2)) * 128 + hw * 64 + (n & 3) * 8;
      wait4<15, 0>(L, L_FS, 5);
      ST(2);
      v4u af[8];
#pragma unroll
      for (int s = 0; s < 4; ++s) { af[2 * s] = tr2(a + s * 4096, 512); af[2 * s + 1] = tr2(a + s * 4096 + 32, 512); }
      SB();
      f32x4 d0 = {0, 0, 0, 0}, d1 = {0, 0, 0, 0};
#pragma unroll
      for (int s = 0; s < 4; ++s) { d0 = mfma(af[2 * s], xq[s], d0); d1 = mfma(af[2 * s + 1], xq[s], d1); }
      v4u gr;
#pragma unroll
      for (int r = 0; r < 4; ++r) gr[r] = tagf<8>((n & 2) ? d1[r] : d0[r], t8);
      st_sc1<0x000F000F000F000Full>(qoff, gr, ws);
    } else {
      if (hw == 2 && c < 8) st_sc1<1>(WS_T + c * 128, v4u{t0, t32, t0, t32}, ws);
      {
        const v4u gwv = *(LAS v4u*)(L + L_P + lane * 16), xv = *(LAS v4u*)(L + L_X + (hw & 1) * 1024 + lane * 16);
        float s = 0.f;
#pragma unroll
        for (int k = 0; k < 4; ++k) {
          const unsigned pr = pk_bf16(asf(xv[k] << 16) * asf(gwv[k] << 16), asf(xv[k] & 0xFFFF0000u) * asf(gwv[k] & 0xFFFF0000u));
          s += asf(pr << 16) + asf(pr & 0xFFFF0000u);
        }
        s += dppf<XOR1>(s); s += dppf<XOR2>(s); s += dppf<ROR4>(s); s += dppf<ROR8>(s); s = sw16(s, s); s = sw32(s, s);
        const float gs = rbf(frcp(1.f + fexp(-rbf(s))));
        if (lane == 0) *(LAS f32x2*)(L + L_GATE + (hw & 1) * 8) = f32x2{gs, rbf(1.f - gs)};
        lds_set(L, L_TAG, t32);
      }
      v4u xg[2];
#pragma unroll
      for (int s = 0; s < 2; ++s) xg[s] = *(LAS v4u*)(L + L_X + (n & 1) * 1024 + xc * 128 + s * 64 + g * 16);
      const unsigned hoff = WS_H + ((((n & 1) * 2 + (hw & 1)) * 8 + xc) * 448 + 16 * j + 4 * ((n >> 1) & 3) + g) * 16;
      LAS char* a = L + L_GU + (hw & 1) * 8192 + (8 * g + (n >> 2)) * 128 + (n & 3) * 8;
      wait4<15, 0>(L, L_FS, 9 - sk);
      ST(2);
      v4u af[8];
#pragma unroll
      for (int s = 0; s < 2; ++s)
#pragma unroll
        for (int t = 0; t < 4; ++t) af[4 * s + t] = tr2(a + s * 4096 + t * 32, 512);
      SB();
      f32x4 d[4];
#pragma unroll
      for (int t = 0; t < 4; ++t) d[t] = mfma(af[4 + t], xg[1], mfma(af[t], xg[0], f32x4{0, 0, 0, 0}));
      v4u gr;
#pragma unroll
      for (int r = 0; r < 4; ++r) { const float lo = (n & 2) ? d[1][r] : d[0][r], hi = (n & 2) ? d[3][r] : d[2][r]; gr[r] = tagf<12>((n & 4) ? hi : lo, t12); }
      PIN(gr);
      if (has_f) st_sc1<0x00FF00FF00FF00FFull>(hoff, gr, ws);
    }
    lds_set(L, fh, 1);
    ST(3);
    if (hw == 1) {
      wait_until(L, t0, TT);
      v4u tv;
      ld1w(tv, WS_T + (lane & 7) * 128, ws);
      const bool ok = tv.y == t32 && tv.w == t32;
      float d = ok ? (float)(int)(tv.x - t0) : 0.f, k = ok ? 1.f : 0.f;
      d += dppf<XOR1>(d); k += dppf<XOR1>(k); d += dppf<XOR2>(d); k += dppf<XOR2>(k); d += dppf<HMIR>(d); k += dppf<HMIR>(k);
      lds_set(L, L_DT, (int)fminf(fmaxf(d * 16.f * frcp(fmaxf(k, 1.f)), -1600.f), 1600.f));
    }
    {
      const unsigned qo = WS_Q + ((kvh * 2 + b) * 4 + hw) * 2048 + lane * 16;
      wait_until(L, t0, TQ);
      v4u q0, q1;
      CNT0(14); CNT0(15);
#pragma unroll 1
      for (int it = 0; it < SPIN_CAP; ++it) { ld2w(q0, q1, qo, qo + 1024, ws); if (!anybad<8>(neq(q0, t8) | neq(q1, t8))) break; CNT(14); }
      float v[4];
#pragma unroll
      for (int r = 0; r < 4; ++r) { v[r] = untag<8>(q0[r]) + untag<8>(q1[r]); v[r] = sw32(v[r], v[r]); }
      if (lane < 32) *(LAS v2u*)(L + L_Q + hw * QSTR + lane * 8) = v2u{pk_bf16(v[0], v[1]), pk_bf16(v[2], v[3])};
      LGKM0(); lds_set(L, fh, 2);
      ST(4);
    }
    wait4<15, 1>(L, L_FH, 2);
    v4u qf[4], kf[16];
#pragma unroll
    for (int s = 0; s < 4; ++s) qf[s] = *(LAS v4u*)(L + L_Q + (n & 3) * QSTR + s * 64 + g * 16);
    wait4<15, 0>(L, L_FS, 13 + 4 * hw - sk);
    ST(6);
#pragma unroll
    for (int s = 0; s < 4; ++s)
#pragma unroll
      for (int t = 0; t < 4; ++t) kf[s * 4 + t] = *(LAS v4u*)(L + L_K + (4 * hw + t) * 4096 + n * 256 + (((4 * s + g) ^ n) * 16));
    SB();
    float sc[4];
    {
      f32x4 d[4];
#pragma unroll
      for (int t = 0; t < 4; ++t) d[t] = f32x4{0, 0, 0, 0};
#pragma unroll
      for (int s = 0; s < 4; ++s)
#pragma unroll
        for (int t = 0; t < 4; ++t) d[t] = mfma(kf[s * 4 + t], qf[s], d[t]);
      constexpr float SC = 0.08838834764831845f;
      float v[4];
#pragma unroll
      for (int r = 0; r < 4; ++r) { const float lo = (n & 4) ? d[1][r] : d[0][r], hi = (n & 4) ? d[3][r] : d[2][r]; v[r] = (n & 8) ? hi : lo; }
      const unsigned a = pk_bf16(v[0], v[1]), bq = pk_bf16(v[2], v[3]);
      const unsigned a2 = pk_bf16(asf(a << 16) * SC, asf(a & 0xFFFF0000u) * SC), b2 = pk_bf16(asf(bq << 16) * SC, asf(bq & 0xFFFF0000u) * SC);
      sc[0] = asf(a2 << 16); sc[1] = asf(a2 & 0xFFFF0000u); sc[2] = asf(b2 << 16); sc[3] = asf(b2 & 0xFFFF0000u);
    }
    float m = fmaxf(fmaxf(sc[0], sc[1]), fmaxf(sc[2], sc[3]));
    m = fmaxf(m, dppf<ROR4>(m)); m = fmaxf(m, dppf<ROR8>(m)); m = x16max(m); m = x32max(m);
    if (lane < 4) *(LAS float*)(L + L_MX + lane * 16 + hw * 4) = m;
    LGKM0(); lds_set(L, fh, 3);
    wait4<15, 0>(L, L_FH, 3);
    const int e = lane & 7;
    const unsigned ho = WS_H + (((hw & 1) * 16 + (lane >> 3)) * 448 + 7 * rg + (e < 7 ? e : 6)) * 16;
    if (hw >= 2) { DMA_S(" sc1", ho, ws, lbase + L_K + hw * 2048); DMA_S(" sc1", ho + 8 * 448 * 16, ws, lbase + L_K + hw * 2048 + 1024); }
    { const f32x4 mm = *(LAS f32x4*)(L + L_MX + (n & 3) * 16); m = fmaxf(fmaxf(mm[0], mm[1]), fmaxf(mm[2], mm[3])); }
    {
      float p[4];
#pragma unroll
      for (int r = 0; r < 4; ++r) p[r] = fexp(sc[r] - m);
      *(LAS v2u*)(L + L_P + (n & 3) * PSTR + (64 * hw + 16 * (n >> 2) + 4 * g) * 2) = v2u{pk_bf16(p[0], p[1]), pk_bf16(p[2], p[3])};
      float l = (p[0] + p[1]) + (p[2] + p[3]);
      l += dppf<ROR4>(l); l += dppf<ROR8>(l); l = sw16(l, l); l = sw32(l, l);
      if (lane < 4) *(LAS float*)(L + L_SUM + lane * 16 + hw * 4) = l;
    }
    LGKM0(); lds_set(L, fh, 4);
    ST(7);
    TR_ADDR(hw);
    unsigned ooff = WS_O + (((2 * (4 * kvh + (n & 3)) + (hw >> 1)) * 2 + b) * 16 + sp) * 256 + (hw & 1) * 128 + ((n >> 2) & 1) * 64 + g * 16;
    unsigned tv = t12;
    PIN(ooff); PIN(tv);
    const int tabs = t0 + ((clock_offset(L) + TVE) >> 4);
    wait4<15, 0>(L, L_FH, 4);
    v4u pa[8], pb[8];
    {
      const unsigned pp = L_P + (n & 3) * PSTR + g * 16, aa = n < 4 ? pp : L_Z, ab = (n >> 2) == 1 ? pp : L_Z;
#pragma unroll
      for (int u = 0; u < 8; ++u) { pa[u] = *(LAS v4u*)(L + aa + u * 64); pb[u] = *(LAS v4u*)(L + ab + u * 64); }
    }
    if (hw == 2) {
      const f32x4 ls = *(LAS f32x4*)(L + L_SUM + (n & 3) * 16);
      st_sc1<0xF>(WS_ST + (((kvh * 4 + (n & 3)) * 2 + b) * 16 + sp) * 16, v4u{tagt<12>(m, t12), tagf<12>((ls[0] + ls[1]) + (ls[2] + ls[3]), t12), t12, t12}, ws);
    }
    f32x4 o = {0, 0, 0, 0};
#define PVN(u0, nu) do { v4u va[nu], vb[nu]; _Pragma("unroll") for (int u = 0; u < nu; ++u) { va[u] = tr2(L + L_V + ta0 + (u0 + u) * 8192, 1024); vb[u] = tr2(L + L_V + ta1 + (u0 + u) * 8192, 1024); } SB(); \
      _Pragma("unroll") for (int u = 0; u < nu; ++u) { o = mfma(va[u], pa[u0 + u], o); o = mfma(vb[u], pb[u0 + u], o); } } while (0)
    wait4<15, 1>(L, L_FS, 33 - sk);
    PVN(0, 4); PIN(o);
    ST(8);
    if (hw >= 2) {
      if (hw == 2) vmcnt<1>(); else vmcnt<0>();
      v4u h0 = *(LAS v4u*)(L + L_K + hw * 2048 + lane * 16), h1 = *(LAS v4u*)(L + L_K + hw * 2048 + 1024 + lane * 16);
#pragma unroll 1
      for (int it = 0; it < SPIN_CAP && anybad<12>(neq(h0, t12) | neq(h1, t12)); ++it) { ld2w(h0, h1, ho, ho + 8 * 448 * 16, ws); CNT(14); }
      float v[4];
#pragma unroll
      for (int r = 0; r < 4; ++r) v[r] = sw32(untag<12>(h0[r]), untag<12>(h1[r]));
      const float a0 = sw16(v[0], v[1]), a1 = sw16(v[2], v[3]);
      const float b0 = a0 + dppf<ROR8>(a0), b1 = a1 + dppf<ROR8>(a1);
      const float gg = rbf((lane & 8) ? b1 : b0);
      const auto sw = __builtin_amdgcn_permlane32_swap(asu(gg), asu(gg), false, false);
      const float hv = rbf(gg * frcp(1.f + fexp(-gg))) * asf(sw[1]);
      if (lane < 32) *(LAS u16*)(L + L_HB + (hw & 1) * 64 + (4 * e + ((lane >> 2) & 2) + (lane >> 4)) * 2) = e < 7 ? (u16)(pk_bf16(hv, hv) & 0xFFFFu) : (u16)0;
      LGKM0(); lds_set(L, fh, 5);
      ST(5);
    }
    wait4<15, 0>(L, L_FS, 39 - sk);
    PVN(4, 3); PIN(o);
    const unsigned so = WS_ST + (((rg >> 1) * 2 + (hw & 1)) * 16 + 4 * (n & 3) + g) * 16;
    wait4<15, 0>(L, L_FS, C_V - sk);
    ST(9);
    PVN(7, 1);
    st_sc1<0x00FF00FF00FF00FFull>(ooff, v4u{tagt<12>(o[0], tv), tagt<12>(o[1], tv), tagt<12>(o[2], tv), tagt<12>(o[3], tv)}, ws);
    ST(10);
    if (hw < 2) {
      unsigned oo = WS_O + (rg * 2 + hw) * 4096 + lane * 16, oba = L_OB + hw * 128 + (4 * n + (g & 1) * 2 + (g >> 1)) * 2;
      DMA_S(" sc1", so, ws, lbase + L_K + hw * 1024);
      const float gate = *(LAS float*)(L + L_GATE + hw * 8);
      PIN(oo); PIN(oba);
      spin_until(tabs + (TO >> 4));
      ST(11);
      v4u a0, a1, a2, a3;
      asm volatile("global_load_dwordx4 %0, %4, %5 sc1\n\tglobal_load_dwordx4 %1, %4, %5 offset:1024 sc1\n\tglobal_load_dwordx4 %2, %4, %5 offset:2048 sc1\n\t"
                   "global_load_dwordx4 %3, %4, %5 offset:3072 sc1\n\ts_waitcnt vmcnt(4)" : "=&v"(a0), "=&v"(a1), "=&v"(a2), "=&v"(a3) : "v"(oo), "s"(ws) : "memory");
      v4u st = *(LAS v4u*)(L + L_K + hw * 1024 + lane * 16);
      if (anybad<12>(neq(st, t12))) {
#pragma unroll 1
        for (int it = 0; it < SPIN_CAP; ++it) { CNT(14); ld1w(st, so, ws); if (!anybad<12>(neq(st, t12))) break; }
      }
      const float ms = untag<12>(st.x), ls = untag<12>(st.y);
      float M = ms; M = fmaxf(M, dppf<XOR1>(M)); M = fmaxf(M, dppf<XOR2>(M)); M = x16max(M); M = x32max(M);
      const float ex = fexp(ms - M);
      float Ls = ex * ls; Ls += dppf<XOR1>(Ls); Ls += dppf<XOR2>(Ls); Ls = sw16(Ls, Ls); Ls = sw32(Ls, Ls);
      const float wv_ = gate * ex * frcp(Ls);
      const float wt0 = dppf<0x00>(wv_), wt1 = dppf<0x55>(wv_), wt2 = dppf<0xAA>(wv_), wt3 = dppf<0xFF>(wv_);
      f32x2 w0 = {wt0, wt0}, w1 = {wt1, wt1}, w2 = {wt2, wt2}, w3 = {wt3, wt3};
      PIN(w0); PIN(w1); PIN(w2); PIN(w3);
      asm volatile("s_waitcnt vmcnt(0)" : "+v"(a0), "+v"(a1), "+v"(a2), "+v"(a3)::"memory");
      ST(12);
#pragma unroll 1
      for (int it = 0; it < SPIN_CAP; ++it) {
#define UT2(a, i) f32x2{untag<12>(a[i]), untag<12>(a[i + 1])}
        const f32x2 lo = __builtin_elementwise_fma(w0, UT2(a0, 0), w1 * UT2(a1, 0)) + __builtin_elementwise_fma(w2, UT2(a2, 0), w3 * UT2(a3, 0));
        const f32x2 hi = __builtin_elementwise_fma(w0, UT2(a0, 2), w1 * UT2(a1, 2)) + __builtin_elementwise_fma(w2, UT2(a2, 2), w3 * UT2(a3, 2));
        const float t = sw16(sw32(lo[0], lo[1]), sw32(hi[0], hi[1]));
        *(volatile LAS u16*)(L + oba) = (u16)(pk_bf16(t, t) & 0xFFFFu);
        SB();
        if (!anybad<12>(neq(a0, t12) | neq(a1, t12) | neq(a2, t12) | neq(a3, t12))) break;
        CNT(15);
        ld4w(a0, a1, a2, a3, oo, ws);
      }
      LGKM0();
      ST(13);
      __builtin_amdgcn_s_barrier();
    } else if (hw == 2) {
      __builtin_amdgcn_s_barrier();
    } else {
      unsigned yo = WS_Y + (((xc >> 1) * 64 + lane) * 2 + (xc & 1)) * 512 + j * 16;
      unsigned op = ((xc & 1) * 512 + (xc >> 1) * 128 + 4 * j + (g & 1) * 2 + (g >> 1)) * 2;
      PIN(yo); PIN(op);
      __builtin_amdgcn_s_barrier();
      __builtin_amdgcn_s_sleep(4);
      spin_until(tabs + ((TO + TY) >> 4));
      ST(11);
      float t;
#pragma unroll 1
      for (int it = 0; it < SPIN_CAP; ++it) {
        v4u y;
        ld1w(y, yo, ws);
        ST(12);
        t = sw16(sw32(untag<12>(y.x), untag<12>(y.y)), sw32(untag<12>(y.z), untag<12>(y.w)));
        t += dppf<XOR1>(t); t += dppf<XOR2>(t); t += dppf<ROR4>(t); t += dppf<ROR8>(t);
        if (!anybad<12>(neq(y, t12))) break;
        CNT(15);
      }
      asm volatile("s_mov_b64 exec, %3\n\tglobal_store_short %0, %1, %2\n\ts_mov_b64 exec, -1" ::"v"(op), "v"(pk_bf16(t, t)), "s"(out), "s"(0x0001000100010001ull) : "memory");
      if (c == 0) asm volatile("s_mov_b64 exec, 1\n\tglobal_store_dword %0, %1, %2 sc1\n\ts_mov_b64 exec, -1" ::"v"(0), "v"(t32), "s"(ws) : "memory");
      ST(13);
    }
  } else {
    const int w = wv;
    const unsigned fs = L_FS + 4 * w;
    int issued = 5, landed = 0, due = TK0 + w * (TAU / 4), seen = 0, dt = 0;
#define PUBLISH(vm_) do { if (issued - (vm_) != landed) { landed = issued - (vm_); lds_set(L, fs, landed); } } while (0)
#define PACE() do { \
      const int v0_ = vm_inflight(); PUBLISH(v0_); \
      if (seen < due || v0_ >= WIN) { \
        const int d_ = *(volatile LAS int*)(L + L_DT); \
        _Pragma("unroll 1") for (int it_ = 0; it_ < SPIN_CAP; ++it_) { \
          seen = now16(t0) - dt; \
          const int vm_ = vm_inflight(); PUBLISH(vm_); \
          if (seen >= due && vm_ < WIN) break; } \
        const int dn_ = __builtin_amdgcn_readfirstlane(d_); seen += dt - dn_; dt = dn_; } \
      due += TAU; ++issued; } while (0)
    {
      const char* sb = w == 0 ? (const char*)x : w == 1 ? (const char*)x + 1024 : w == 2 ? (const char*)gw : (const char*)ws;
      DMA_S("", w == 3 ? 0 : lane * 16, sb, lbase + (w < 2 ? L_X : L_P - 2048) + w * 1024);
    }
    {
      const char* base = (const char*)wq + (size_t)(128 * qp + 8 * w) * 8192 + qc * 128;
      const unsigned voff = (lane >> 3) * 8192 + (lane & 7) * 16;
#pragma unroll
      for (int i = 0; i < 4; ++i) DMA_S(" nt", voff, base + (size_t)i * 32 * 8192, lbase + L_WQ + (4 * i + w) * 1024);
    }
    if (w == 0 && lane < 9) *(volatile LAS int*)(L + L_FS + 4 * lane) = lane < 8 ? -64 : 0;
    if (w == 1) *(volatile LAS v2u*)(L + L_Z + 8 * lane) = v2u{0, 0};
    ST(7);
    LGKM0();
    __builtin_amdgcn_s_barrier();
    if (has_f) {
      const unsigned voff = (lane >> 3) * 3584 + (lane & 7) * 16;
      const size_t off = (size_t)(64 * xc + 8 * w) * 3584 + j * 128;
#pragma unroll 1
      for (int i = 0; i < 4; ++i) { PACE(); DMA_S(" nt", voff, (const char*)(i < 2 ? wg : wu) + off + (i & 1) * (32 * 3584), lbase + L_GU + (4 * i + w) * 1024); }
    } else {
      due += 4 * TAU;
    }
    {
      const char* kb = (const char*)kc + kvoff + w * 1024;
      const unsigned voff = g * 256 + ((n ^ (4 * w + g)) * 16);
#pragma unroll 1
      for (int i = 0; i < 16; ++i) { PACE(); DMA_S(" nt", voff, kb + (((i + rot) & 15) << 12), lbase + L_K + i * 4096 + w * 1024); }
    }
    ST(1);
    {
      const unsigned voff = g * 256 + ((n ^ (2 * (g | ((w >> 1) << 2)))) * 16);
      const char* vb = (const char*)vc + kvoff + w * 1024;
#pragma unroll 1
      for (int i = 0; i < 16; ++i) {
        if (i == 8) wait4<3, 0>(L, L_FH, 1);
        if (i >= 16 - NL) due += TAUL - TAU;
        PACE(); DMA_S(" nt", voff, vb + (((i + rot) & 15) << 12), lbase + L_V + i * 4096 + w * 1024);
      }
    }
    ST(2);
    wait4<12, 0>(L, L_FH, 1);
    {
      const unsigned vo = g * 1024 + ((n ^ (2 * (g | ((w >> 1) << 2)))) * 16);
      const char* ob = (const char*)wo + (size_t)(64 * rg + 4 * w) * 1024 + cg * 256;
      const char* db = (const char*)wd + (size_t)(28 * rg) * 1024 + cg * 256;
#pragma unroll 1
      for (int i = 0; i < 6; ++i) {
        const char* src = i >= 2 ? ob + (i - 2) * 16384 : db + (i == 0 ? 4 * w : w < 3 ? 16 + 4 * w : 0) * 1024;
        due += TAU2 - TAU; PACE();
        DMA_S(" nt", vo, src, lbase + L_WO + (i >= 2 ? i - 2 : i + 4) * 4096 + w * 1024);
      }
    }
    ST(3);
#pragma unroll 1
    for (int it = 0; it < SPIN_CAP && landed < C_WD - sk; ++it) { const int vm = vm_inflight(); PUBLISH(vm); }
    ST(4);
    unsigned t12 = *(volatile LAS unsigned*)(L + L_TAG) & 0xFFFu;
    TR_ADDR(w);
    unsigned yoff = WS_Y + ((cg * 64 + rg) * 2 + (n & 1)) * 512 + w * 128 + ((n >> 1) & 1) * 64 + g * 16;
    unsigned oa = lbase + (n < 2 ? L_OB + n * 128 + g * 16 : L_Z), ob = lbase + ((n >> 1) == 1 ? L_OB + (n & 1) * 128 + g * 16 : L_Z);
    const float g1 = *(LAS float*)(L + L_GATE + (n & 1) * 8 + 4);
    wait4<12, 0>(L, L_FH, 5);
    const v4u ha = *(LAS v4u*)(L + (n < 2 ? L_HB + n * 64 + g * 16 : L_Z)), hb = *(LAS v4u*)(L + ((n >> 1) == 1 ? L_HB + (n & 1) * 64 + g * 16 : L_Z));
    wait4<15, 0>(L, L_FS, C_WD - sk);
    f32x4 y = mfma(tr2(L + L_WD + ta1, 1024), hb, mfma(tr2(L + L_WD + ta0, 1024), ha, f32x4{0, 0, 0, 0})) * g1;
    PIN(y);
    vmcnt<0>(); lds_set(L, fs, C_WO - sk);
    ST(5);
    wait4<15, 0>(L, L_FS, C_WO - sk);
    v4u wf[4];
    wf[0] = tr2(L + L_WO + ta0, 1024); wf[1] = tr2(L + L_WO + ta1, 1024); wf[2] = tr2(L + L_WO + ta0 + 8192, 1024); wf[3] = tr2(L + L_WO + ta1 + 8192, 1024);
    PIN(wf[0]); PIN(wf[1]); PIN(wf[2]); PIN(wf[3]); PIN(oa); PIN(ob); PIN(yoff); PIN(t12);
    __builtin_amdgcn_s_setprio(3);
    ST(6);
    __builtin_amdgcn_s_barrier();
    asm volatile("" ::: "memory");
    ST(10);
    {
      const v4u a0 = *(LAS v4u*)(uintptr_t)oa, b0 = *(LAS v4u*)(uintptr_t)ob, a1 = *(LAS v4u*)(uintptr_t)(oa + 64), b1 = *(LAS v4u*)(uintptr_t)(ob + 64);
      SB();
      y = mfma(wf[0], a0, y); y = mfma(wf[1], b0, y); y = mfma(wf[2], a1, y); y = mfma(wf[3], b1, y);
      st_sc1<0x000F000F000F000Full>(yoff, v4u{tagt<12>(y[0], t12), tagt<12>(y[1], t12), tagt<12>(y[2], t12), tagt<12>(y[3], t12)}, ws);
    }
    ST(11);
  }
  if (wv == PWAVE) {
    const unsigned tg = __builtin_amdgcn_readfirstlane(*(volatile LAS unsigned*)(L + L_TAG));
    st_sc1<1>(WS_STAMP + c * 128, v4u{stamp[PROBE], tg, stamp[0], tg}, ws);
    if (c == 0) {
      for (int it = 0; it < 1000000; ++it) { if ((unsigned)__builtin_amdgcn_s_memrealtime() - stamp[0] >= 2500u) break; }
      int mx = -100000, mn = 100000;
      for (int k = 0; k < 4; ++k) { v4u e; ld1w(e, WS_STAMP + (lane + 64 * k) * 128, ws);
        if (e.y == tg) { mx = max(mx, (int)(e.x - stamp[0])); mn = min(mn, (int)(e.z - stamp[0])); } }
      for (int o = 32; o > 0; o >>= 1) { mx = max(mx, __shfl_xor(mx, o)); mn = min(mn, __shfl_xor(mn, o)); }
      const unsigned tgt = 4000u + 8u * (unsigned)(__builtin_amdgcn_readfirstlane(mx) - __builtin_amdgcn_readfirstlane(mn));
      for (int it = 0; it < 1000000; ++it) { if ((unsigned)__builtin_amdgcn_s_memrealtime() - stamp[0] >= tgt) break; }
    }
  }
}

extern "C" void kernel_launch(void* const* d_in, const int* in_sizes, int n_in, void* d_out, int out_size, void* d_ws, size_t ws_size, hipStream_t stream) {
  (void)hipFuncSetAttribute((const void*)fused_decode, hipFuncAttributeMaxDynamicSharedMemorySize, L_END);
  fused_decode<<<256, 512, L_END, stream>>>((const u16*)d_in[0], (const u16*)d_in[9], (char*)d_ws, (const u16*)d_in[3], (const u16*)d_in[6], (const u16*)d_in[7],
                                            (const u16*)d_in[1], (const u16*)d_in[2], (const u16*)d_in[4], (const u16*)d_in[8], (u16*)d_out);
}
```

```cpp
#define PROBE 15
#define PMODE 1
#define AMP 0u
#define PWAVE 7
#define SMASK 1
#define TO 3000
#define TY 1750
#include <hip/hip_runtime.h>
#include <hip/hip_bf16.h>
#include <stdint.h>

#define STAMP 0
#define TAU 256
#define TK0 440
#define TAUL TAU
#define NL 0
#define TAU2 256
#define TT 2900
#define TQ 4300
#define TVE (TK0 + (35 - NL) * TAU + NL * TAUL)
#define WIN 12
#define CODE_PF 25

typedef unsigned short u16;
typedef unsigned long long u64;
typedef unsigned v4u __attribute__((ext_vector_type(4)));
typedef int v4i __attribute__((ext_vector_type(4)));
typedef unsigned v2u __attribute__((ext_vector_type(2)));
typedef short v4s __attribute__((ext_vector_type(4)));
typedef float f32x4 __attribute__((ext_vector_type(4)));
typedef float f32x2 __attribute__((ext_vector_type(2)));
typedef __bf16 v8bf __attribute__((ext_vector_type(8)));
typedef __bf16 v2bf __attribute__((ext_vector_type(2)));
#define GAS __attribute__((address_space(1)))
#define LAS __attribute__((address_space(3)))
#define SB() __builtin_amdgcn_sched_barrier(0)

constexpr unsigned WS_T = 1024;
constexpr unsigned WS_Q = 4096;
constexpr unsigned WS_H = WS_Q + 131072;
constexpr unsigned WS_ST = WS_H + 229376;
constexpr unsigned WS_O = WS_ST + 16384;
constexpr unsigned WS_Y = WS_O + 524288;
constexpr unsigned WS_STAMP = 4u << 20;
constexpr unsigned L_K = 0, L_V = 65536, L_WQ = L_V + 8 * 4096, L_WO = 131072, L_GU = L_WO, L_WD = L_WO + 16384, L_X = L_WD + 8192,
                   L_Q = L_X + 2048, QSTR = 320, L_P = L_Q + 4 * QSTR, PSTR = 576, L_OB = L_P + 4 * PSTR, L_HB = L_OB + 256, L_MX = L_HB + 128,
                   L_SUM = L_MX + 64, L_GATE = L_SUM + 64, L_FS = L_GATE + 16, L_FH = L_FS + 16, L_DT = L_FH + 16, L_TAG = L_DT + 4,
                   L_Z = L_DT + 16, L_END = L_Z + 512;
static_assert(L_END <= 163840, "LDS");
constexpr int SPIN_CAP = 1 << 17;

__device__ __forceinline__ float asf(unsigned u) { return __builtin_bit_cast(float, u); }
__device__ __forceinline__ unsigned asu(float f) { return __builtin_bit_cast(unsigned, f); }
__device__ __forceinline__ unsigned pk_bf16(float lo, float hi) { f32x2 x = {lo, hi}; return __builtin_bit_cast(unsigned, __builtin_convertvector(x, v2bf)); }
__device__ __forceinline__ float rbf(float f) { return asf(pk_bf16(f, f) << 16); }
template <int CTRL> __device__ __forceinline__ float dppf(float x) { return asf(__builtin_amdgcn_mov_dpp(asu(x), CTRL, 0xf, 0xf, true)); }
constexpr int XOR1 = 0xB1, XOR2 = 0x4E, HMIR = 0x141, ROR4 = 0x124, ROR8 = 0x128;
__device__ __forceinline__ float sw16(float a, float b) { auto s = __builtin_amdgcn_permlane16_swap(asu(a), asu(b), false, false); return asf(s[0]) + asf(s[1]); }
__device__ __forceinline__ float sw32(float a, float b) { auto s = __builtin_amdgcn_permlane32_swap(asu(a), asu(b), false, false); return asf(s[0]) + asf(s[1]); }
__device__ __forceinline__ float x16max(float x) { auto s = __builtin_amdgcn_permlane16_swap(asu(x), asu(x), false, false); return fmaxf(asf(s[0]), asf(s[1])); }
__device__ __forceinline__ float x32max(float x) { auto s = __builtin_amdgcn_permlane32_swap(asu(x), asu(x), false, false); return fmaxf(asf(s[0]), asf(s[1])); }
__device__ __forceinline__ float frcp(float x) { return __builtin_amdgcn_rcpf(x); }
__device__ __forceinline__ float fexp(float x) { return __builtin_amdgcn_exp2f(x * 1.4426950408889634f); }
__device__ __forceinline__ f32x4 mfma(v4u a, v4u b, f32x4 c) { return __builtin_amdgcn_mfma_f32_16x16x32_bf16(__builtin_bit_cast(v8bf, a), __builtin_bit_cast(v8bf, b), c, 0, 0, 0); }
__device__ __forceinline__ v2u trread(LAS char* p) { return __builtin_bit_cast(v2u, __builtin_amdgcn_ds_read_tr16_b64_v4i16((LAS v4s*)p)); }
__device__ __forceinline__ v4u tr2(LAS char* p, int d) { v2u a = trread(p), b = trread(p + d); return v4u{a.x, a.y, b.x, b.y}; }
template <int B> __device__ __forceinline__ unsigned tagt(float v, unsigned t) { return (asu(v) & ~((1u << B) - 1)) | t; }
template <int B> __device__ __forceinline__ unsigned tagf(float v, unsigned t) { return ((asu(v) + (1u << (B - 1))) & ~((1u << B) - 1)) | t; }
template <int B> __device__ __forceinline__ float untag(unsigned w) { return asf(w & ~((1u << B) - 1)); }
__device__ __forceinline__ unsigned neq(v4u g, unsigned t) { return __builtin_amdgcn_bitop3_b32(g.x, g.y, t, 0x7E) | __builtin_amdgcn_bitop3_b32(g.z, g.w, t, 0x7E); }
template <int B> __device__ __forceinline__ bool anybad(unsigned x) { return __builtin_amdgcn_ballot_w64((x & ((1u << B) - 1)) != 0) != 0; }
#define PIN(x) asm volatile("" : "+v"(x))
#define CNT(i)
#define CNT0(i)

#define DMA_S(MOD, voff, sbase, ldst) asm volatile("s_mov_b32 m0, %2\n\ts_nop 0\n\tglobal_load_lds_dwordx4 %0, %1" MOD :: "v"(voff), "s"(sbase), "s"(ldst) : "memory")
template <int N> __device__ __forceinline__ void vmcnt() { asm volatile("s_waitcnt vmcnt(%0)" ::"i"(N) : "memory"); }
#define LGKM0() asm volatile("s_waitcnt lgkmcnt(0)" ::: "memory")
__device__ __forceinline__ void ld1w(v4u& a, unsigned o0, const char* sb) {
  asm volatile("global_load_dwordx4 %0, %1, %2 sc1\n\ts_waitcnt vmcnt(0)" : "=&v"(a) : "v"(o0), "s"(sb) : "memory"); }
__device__ __forceinline__ void ld2w(v4u& a, v4u& b, unsigned o0, unsigned o1, const char* sb) {
  asm volatile("global_load_dwordx4 %0, %2, %4 sc1\n\tglobal_load_dwordx4 %1, %3, %4 sc1\n\ts_waitcnt vmcnt(0)" : "=&v"(a), "=&v"(b) : "v"(o0), "v"(o1), "s"(sb) : "memory"); }
__device__ __forceinline__ void ld4w(v4u& a, v4u& b, v4u& c, v4u& d, unsigned o0, const char* sb) {
  asm volatile("global_load_dwordx4 %0, %4, %5 sc1\n\tglobal_load_dwordx4 %1, %4, %5 offset:1024 sc1\n\tglobal_load_dwordx4 %2, %4, %5 offset:2048 sc1\n\t"
               "global_load_dwordx4 %3, %4, %5 offset:3072 sc1\n\ts_waitcnt vmcnt(0)" : "=&v"(a), "=&v"(b), "=&v"(c), "=&v"(d) : "v"(o0), "s"(sb) : "memory"); }
template <u64 MASK> __device__ __forceinline__ void st_sc1(unsigned voff, v4u d, char* sbase) {
  asm volatile("s_mov_b64 exec, %3\n\tglobal_store_dwordx4 %0, %1, %2 sc1\n\ts_mov_b64 exec, -1\n\ts_nop 0" ::"v"(voff), "v"(d), "s"(sbase), "s"(MASK) : "memory"); }

#define ST(i) do { if ((SMASK >> (i)) & 1) { SB(); stamp[i] = (unsigned)__builtin_amdgcn_s_memrealtime(); SB(); } } while (0)

__device__ __forceinline__ int now16(unsigned t0) { return (int)(((unsigned)__builtin_amdgcn_s_memrealtime() - t0) * 16u); }
__device__ __forceinline__ int vm_inflight() { unsigned r; asm volatile("s_getreg_b32 %0, hwreg(HW_REG_IB_STS, 0, 4)" : "=s"(r)::"memory"); return (int)r; }
__device__ __forceinline__ int clock_offset(LAS char* L) { return __builtin_amdgcn_readfirstlane(*(volatile LAS int*)(L + L_DT)); }
__device__ __forceinline__ void wait_until(LAS char* L, unsigned t0, int t) {
#pragma unroll 1
  for (int it = 0; it < SPIN_CAP && now16(t0) - clock_offset(L) < t; ++it) __builtin_amdgcn_s_sleep(1);
}
__device__ __forceinline__ void spin_until(unsigned tick) {
#pragma unroll 1
  for (int it = 0; it < SPIN_CAP && (int)((unsigned)__builtin_amdgcn_s_memrealtime() - tick) < 0; ++it) {}
}
__device__ __forceinline__ void lds_set(LAS char* L, unsigned off, unsigned v) {
  asm volatile("s_mov_b64 exec, 1\n\tds_write_b32 %0, %1\n\ts_mov_b64 exec, -1" ::"v"((unsigned)(uintptr_t)(L + off)), "v"(v) : "memory");
}
template <int MASK, int SLEEP> __device__ __forceinline__ void wait4(LAS char* L, unsigned off, int n) {
#pragma unroll 1
  for (int it = 0; it < SPIN_CAP; ++it) {
    const v4i f = *(volatile LAS v4i*)(L + off);
    int m = 0x7fffffff;
    if (MASK & 1) m = min(m, f.x);
    if (MASK & 2) m = min(m, f.y);
    if (MASK & 4) m = min(m, f.z);
    if (MASK & 8) m = min(m, f.w);
    if (__builtin_amdgcn_readfirstlane(m) >= n) break;
    if (SLEEP) __builtin_amdgcn_s_sleep(SLEEP);
  }
  asm volatile("" ::: "memory");
}
#define TR_ADDR(w_) const int fr = (n >> 2) | ((g & 1) << 2); \
    unsigned ta0 = (8 * g + (n >> 2)) * 256 + ((((2 * (w_)) ^ fr) * 2 + ((n & 3) >> 1)) * 16) + (n & 1) * 8; \
    unsigned ta1 = (8 * g + (n >> 2)) * 256 + ((((2 * (w_) + 1) ^ fr) * 2 + ((n & 3) >> 1)) * 16) + (n & 1) * 8; PIN(ta0); PIN(ta1)

constexpr int C_V = 41, C_WD = 43, C_WO = 47;

__global__ void __launch_bounds__(512) __attribute__((amdgpu_waves_per_eu(2, 2)))
fused_decode(const u16* a0_, const u16* a1_, char* a2_, const u16* a3_, const u16* a4_, const u16* a5_, const u16* a6_, const u16* a7_, const u16* a8_, const u16* a9_, u16* a10_) {
  u64 ta_;
  typedef const __attribute__((address_space(4))) u64* kptr;
  kptr ka = (kptr)__builtin_amdgcn_kernarg_segment_ptr();
  asm volatile("s_memrealtime %0\n\ts_waitcnt lgkmcnt(0)" : "=s"(ta_), "+s"(ka));
  const u16* x = (const u16*)ka[0]; const u16* gw = (const u16*)ka[1]; char* ws = (char*)ka[2]; const u16* wq = (const u16*)ka[3]; const u16* wg = (const u16*)ka[4];
  const u16* wu = (const u16*)ka[5]; const u16* kc = (const u16*)ka[6]; const u16* vc = (const u16*)ka[7]; const u16* wo = (const u16*)ka[8]; const u16* wd = (const u16*)ka[9]; u16* out = (u16*)ka[10];
  extern __shared__ __attribute__((aligned(16))) char smem[];
  LAS char* L = (LAS char*)smem;
  const unsigned lbase = (unsigned)(uintptr_t)L;
  const int c = blockIdx.x, lane = threadIdx.x & 63, wv = __builtin_amdgcn_readfirstlane(threadIdx.x >> 6);
  const int xc = c & 7, j = c >> 3;
  const int kvh = xc, b = j & 1, sp = j >> 1;
  const int rg = 8 * xc + (j >> 2), cg = j & 3;
  const int qp = xc >> 1, qc = 32 * (xc & 1) + j;
  const bool has_f = j < 28;
  const int sk = has_f ? 0 : 4;
  const int g = lane >> 4, n = lane & 15;
  const int rot = (j >> 2) * 2 + (j & 1);
  const size_t kvoff = ((size_t)(b * 8 + kvh) * 16384 + 12288 + sp * 256) * 256;
  unsigned stamp[16];
  for (int i = 0; i < 16; ++i) stamp[i] = 0;
  asm volatile("" ::"s"(x), "s"(kc), "s"(vc), "s"(wq), "s"(wo), "s"(wg), "s"(wu), "s"(wd), "s"(gw), "s"(out), "s"(ws));
  const unsigned t0 = (unsigned)__builtin_amdgcn_s_memrealtime();
  stamp[0] = (unsigned)ta_; stamp[15] = t0;
  if (wv == 4) {
    const char* pc;
    asm volatile("s_getpc_b64 %0" : "=s"(pc));
    asm volatile("s_mov_b32 m0, %2\n\ts_nop 0\n\tglobal_load_lds_dword %0, %1" ::"v"(min(j, CODE_PF) * 512 + (lane & 7) * 64), "s"((const char*)((uintptr_t)pc & ~(uintptr_t)255)), "s"(lbase + L_OB) : "memory");
  }

  if (wv >= 4) {
    const int hw = wv - 4;
    const unsigned fh = L_FH + 4 * hw;
    __builtin_amdgcn_s_setprio(2);
    if (hw == 1) {
      const char* a = lane == 0 ? (const char*)vc + kvoff : lane == 1 ? (const char*)wo + (size_t)rg * 65536 : lane == 2 ? (const char*)wd + (size_t)rg * 28672
                    : lane == 3 ? (const char*)out : (const char*)kc + kvoff;
      asm volatile("s_mov_b32 m0, %1\n\ts_nop 0\n\tglobal_load_lds_dword %0, off" ::"v"(a), "s"(lbase + L_OB) : "memory");
    }
    __builtin_amdgcn_s_barrier();
    wait4<15, 0>(L, L_FS, 1);
    const unsigned t32 = __builtin_amdgcn_readfirstlane(*(LAS unsigned*)(L + L_P + 1024)) + 1, t12 = t32 & 0xFFFu, t8 = t32 & 0xFFu;
    ST(1);
    if (hw < 2) {
      v4u xq[4];
#pragma unroll
      for (int s = 0; s < 4; ++s) xq[s] = *(LAS v4u*)(L + L_X + (n & 1) * 1024 + qp * 256 + s * 64 + g * 16);
      const int col = 64 * qc + 32 * hw + 16 * ((n >> 1) & 1) + 4 * g;
      const unsigned qoff = WS_Q + (((((col >> 9) * 2 + (n & 1)) * 4 + ((col >> 7) & 3)) * 4 + qp) * 128 + (col & 127)) * 4;
      LAS char* a = L + L_WQ + (8 * g + (n >> 2)) * 128 + hw * 64 + (n & 3) * 8;
      wait4<15, 0>(L, L_FS, 5);
      ST(2);
      v4u af[8];
#pragma unroll
      for (int s = 0; s < 4; ++s) { af[2 * s] = tr2(a + s * 4096, 512); af[2 * s + 1] = tr2(a + s * 4096 + 32, 512); }
      SB();
      f32x4 d0 = {0, 0, 0, 0}, d1 = {0, 0, 0, 0};
#pragma unroll
      for (int s = 0; s < 4; ++s) { d0 = mfma(af[2 * s], xq[s], d0); d1 = mfma(af[2 * s + 1], xq[s], d1); }
      v4u gr;
#pragma unroll
      for (int r = 0; r < 4; ++r) gr[r] = tagf<8>((n & 2) ? d1[r] : d0[r], t8);
      st_sc1<0x000F000F000F000Full>(qoff, gr, ws);
    } else {
      if (hw == 2 && c < 8) st_sc1<1>(WS_T + c * 128, v4u{t0, t32, t0, t32}, ws);
      {
        const v4u gwv = *(LAS v4u*)(L + L_P + lane * 16), xv = *(LAS v4u*)(L + L_X + (hw & 1) * 1024 + lane * 16);
        float s = 0.f;
#pragma unroll
        for (int k = 0; k < 4; ++k) {
          const unsigned pr = pk_bf16(asf(xv[k] << 16) * asf(gwv[k] << 16), asf(xv[k] & 0xFFFF0000u) * asf(gwv[k] & 0xFFFF0000u));
          s += asf(pr << 16) + asf(pr & 0xFFFF0000u);
        }
        s += dppf<XOR1>(s); s += dppf<XOR2>(s); s += dppf<ROR4>(s); s += dppf<ROR8>(s); s = sw16(s, s); s = sw32(s, s);
        const float gs = rbf(frcp(1.f + fexp(-rbf(s))));
        if (lane == 0) *(LAS f32x2*)(L + L_GATE + (hw & 1) * 8) = f32x2{gs, rbf(1.f - gs)};
        lds_set(L, L_TAG, t32);
      }
      v4u xg[2];
#pragma unroll
      for (int s = 0; s < 2; ++s) xg[s] = *(LAS v4u*)(L + L_X + (n & 1) * 1024 + xc * 128 + s * 64 + g * 16);
      const unsigned hoff = WS_H + ((((n & 1) * 2 + (hw & 1)) * 8 + xc) * 448 + 16 * j + 4 * ((n >> 1) & 3) + g) * 16;
      LAS char* a = L + L_GU + (hw & 1) * 8192 + (8 * g + (n >> 2)) * 128 + (n & 3) * 8;
      wait4<15, 0>(L, L_FS, 9 - sk);
      ST(2);
      v4u af[8];
#pragma unroll
      for (int s = 0; s < 2; ++s)
#pragma unroll
        for (int t = 0; t < 4; ++t) af[4 * s + t] = tr2(a + s * 4096 + t * 32, 512);
      SB();
      f32x4 d[4];
#pragma unroll
      for (int t = 0; t < 4; ++t) d[t] = mfma(af[4 + t], xg[1], mfma(af[t], xg[0], f32x4{0, 0, 0, 0}));
      v4u gr;
#pragma unroll
      for (int r = 0; r < 4; ++r) { const float lo = (n & 2) ? d[1][r] : d[0][r], hi = (n & 2) ? d[3][r] : d[2][r]; gr[r] = tagf<12>((n & 4) ? hi : lo, t12); }
      PIN(gr);
      if (has_f) st_sc1<0x00FF00FF00FF00FFull>(hoff, gr, ws);
    }
    lds_set(L, fh, 1);
    ST(3);
    if (hw == 1) {
      wait_until(L, t0, TT);
      v4u tv;
      ld1w(tv, WS_T + (lane & 7) * 128, ws);
      const bool ok = tv.y == t32 && tv.w == t32;
      float d = ok ? (float)(int)(tv.x - t0) : 0.f, k = ok ? 1.f : 0.f;
      d += dppf<XOR1>(d); k += dppf<XOR1>(k); d += dppf<XOR2>(d); k += dppf<XOR2>(k); d += dppf<HMIR>(d); k += dppf<HMIR>(k);
      lds_set(L, L_DT, (int)fminf(fmaxf(d * 16.f * frcp(fmaxf(k, 1.f)), -1600.f), 1600.f));
    }
    {
      const unsigned qo = WS_Q + ((kvh * 2 + b) * 4 + hw) * 2048 + lane * 16;
      wait_until(L, t0, TQ);
      v4u q0, q1;
      CNT0(14); CNT0(15);
#pragma unroll 1
      for (int it = 0; it < SPIN_CAP; ++it) { ld2w(q0, q1, qo, qo + 1024, ws); if (!anybad<8>(neq(q0, t8) | neq(q1, t8))) break; CNT(14); }
      float v[4];
#pragma unroll
      for (int r = 0; r < 4; ++r) { v[r] = untag<8>(q0[r]) + untag<8>(q1[r]); v[r] = sw32(v[r], v[r]); }
      if (lane < 32) *(LAS v2u*)(L + L_Q + hw * QSTR + lane * 8) = v2u{pk_bf16(v[0], v[1]), pk_bf16(v[2], v[3])};
      LGKM0(); lds_set(L, fh, 2);
      ST(4);
    }
    wait4<15, 1>(L, L_FH, 2);
    v4u qf[4], kf[16];
#pragma unroll
    for (int s = 0; s < 4; ++s) qf[s] = *(LAS v4u*)(L + L_Q + (n & 3) * QSTR + s * 64 + g * 16);
    wait4<15, 0>(L, L_FS, 13 + 4 * hw - sk);
    ST(6);
#pragma unroll
    for (int s = 0; s < 4; ++s)
#pragma unroll
      for (int t = 0; t < 4; ++t) kf[s * 4 + t] = *(LAS v4u*)(L + L_K + (4 * hw + t) * 4096 + n * 256 + (((4 * s + g) ^ n) * 16));
    SB();
    float sc[4];
    {
      f32x4 d[4];
#pragma unroll
      for (int t = 0; t < 4; ++t) d[t] = f32x4{0, 0, 0, 0};
#pragma unroll
      for (int s = 0; s < 4; ++s)
#pragma unroll
        for (int t = 0; t < 4; ++t) d[t] = mfma(kf[s * 4 + t], qf[s], d[t]);
      constexpr float SC = 0.08838834764831845f;
      float v[4];
#pragma unroll
      for (int r = 0; r < 4; ++r) { const float lo = (n & 4) ? d[1][r] : d[0][r], hi = (n & 4) ? d[3][r] : d[2][r]; v[r] = (n & 8) ? hi : lo; }
      const unsigned a = pk_bf16(v[0], v[1]), bq = pk_bf16(v[2], v[3]);
      const unsigned a2 = pk_bf16(asf(a << 16) * SC, asf(a & 0xFFFF0000u) * SC), b2 = pk_bf16(asf(bq << 16) * SC, asf(bq & 0xFFFF0000u) * SC);
      sc[0] = asf(a2 << 16); sc[1] = asf(a2 & 0xFFFF0000u); sc[2] = asf(b2 << 16); sc[3] = asf(b2 & 0xFFFF0000u);
    }
    float m = fmaxf(fmaxf(sc[0], sc[1]), fmaxf(sc[2], sc[3]));
    m = fmaxf(m, dppf<ROR4>(m)); m = fmaxf(m, dppf<ROR8>(m)); m = x16max(m); m = x32max(m);
    if (lane < 4) *(LAS float*)(L + L_MX + lane * 16 + hw * 4) = m;
    LGKM0(); lds_set(L, fh, 3);
    wait4<15, 0>(L, L_FH, 3);
    const int e = lane & 7;
    const unsigned ho = WS_H + (((hw & 1) * 16 + (lane >> 3)) * 448 + 7 * rg + (e < 7 ? e : 6)) * 16;
    if (hw >= 2) { DMA_S(" sc1", ho, ws, lbase + L_K + hw * 2048); DMA_S(" sc1", ho + 8 * 448 * 16, ws, lbase + L_K + hw * 2048 + 1024); }
    { const f32x4 mm = *(LAS f32x4*)(L + L_MX + (n & 3) * 16); m = fmaxf(fmaxf(mm[0], mm[1]), fmaxf(mm[2], mm[3])); }
    {
      float p[4];
#pragma unroll
      for (int r = 0; r < 4; ++r) p[r] = fexp(sc[r] - m);
      *(LAS v2u*)(L + L_P + (n & 3) * PSTR + (64 * hw + 16 * (n >> 2) + 4 * g) * 2) = v2u{pk_bf16(p[0], p[1]), pk_bf16(p[2], p[3])};
      float l = (p[0] + p[1]) + (p[2] + p[3]);
      l += dppf<ROR4>(l); l += dppf<ROR8>(l); l = sw16(l, l); l = sw32(l, l);
      if (lane < 4) *(LAS float*)(L + L_SUM + lane * 16 + hw * 4) = l;
    }
    LGKM0(); lds_set(L, fh, 4);
    ST(7);
    TR_ADDR(hw);
    unsigned ooff = WS_O + (((2 * (4 * kvh + (n & 3)) + (hw >> 1)) * 2 + b) * 16 + sp) * 256 + (hw & 1) * 128 + ((n >> 2) & 1) * 64 + g * 16;
    unsigned tv = t12;
    PIN(ooff); PIN(tv);
    const int tabs = t0 + ((clock_offset(L) + TVE) >> 4);
    wait4<15, 0>(L, L_FH, 4);
    v4u pa[8], pb[8];
    {
      const unsigned pp = L_P + (n & 3) * PSTR + g * 16, aa = n < 4 ? pp : L_Z, ab = (n >> 2) == 1 ? pp : L_Z;
#pragma unroll
      for (int u = 0; u < 8; ++u) { pa[u] = *(LAS v4u*)(L + aa + u * 64); pb[u] = *(LAS v4u*)(L + ab + u * 64); }
    }
    if (hw == 2) {
      const f32x4 ls = *(LAS f32x4*)(L + L_SUM + (n & 3) * 16);
      st_sc1<0xF>(WS_ST + (((kvh * 4 + (n & 3)) * 2 + b) * 16 + sp) * 16, v4u{tagt<12>(m, t12), tagf<12>((ls[0] + ls[1]) + (ls[2] + ls[3]), t12), t12, t12}, ws);
    }
    f32x4 o = {0, 0, 0, 0};
#define PVN(u0, nu) do { v4u va[nu], vb[nu]; _Pragma("unroll") for (int u = 0; u < nu; ++u) { va[u] = tr2(L + L_V + ta0 + (u0 + u) * 8192, 1024); vb[u] = tr2(L + L_V + ta1 + (u0 + u) * 8192, 1024); } SB(); \
      _Pragma("unroll") for (int u = 0; u < nu; ++u) { o = mfma(va[u], pa[u0 + u], o); o = mfma(vb[u], pb[u0 + u], o); } } while (0)
    wait4<15, 1>(L, L_FS, 33 - sk);
    PVN(0, 4); PIN(o);
    ST(8);
    if (hw >= 2) {
      if (hw == 2) vmcnt<1>(); else vmcnt<0>();
      v4u h0 = *(LAS v4u*)(L + L_K + hw * 2048 + lane * 16), h1 = *(LAS v4u*)(L + L_K + hw * 2048 + 1024 + lane * 16);
#pragma unroll 1
      for (int it = 0; it < SPIN_CAP && anybad<12>(neq(h0, t12) | neq(h1, t12)); ++it) { ld2w(h0, h1, ho, ho + 8 * 448 * 16, ws); CNT(14); }
      float v[4];
#pragma unroll
      for (int r = 0; r < 4; ++r) v[r] = sw32(untag<12>(h0[r]), untag<12>(h1[r]));
      const float a0 = sw16(v[0], v[1]), a1 = sw16(v[2], v[3]);
      const float b0 = a0 + dppf<ROR8>(a0), b1 = a1 + dppf<ROR8>(a1);
      const float gg = rbf((lane & 8) ? b1 : b0);
      const auto sw = __builtin_amdgcn_permlane32_swap(asu(gg), asu(gg), false, false);
      const float hv = rbf(gg * frcp(1.f + fexp(-gg))) * asf(sw[1]);
      if (lane < 32) *(LAS u16*)(L + L_HB + (hw & 1) * 64 + (4 * e + ((lane >> 2) & 2) + (lane >> 4)) * 2) = e < 7 ? (u16)(pk_bf16(hv, hv) & 0xFFFFu) : (u16)0;
      LGKM0(); lds_set(L, fh, 5);
      ST(5);
    }
    wait4<15, 0>(L, L_FS, 39 - sk);
    PVN(4, 3); PIN(o);
    const unsigned so = WS_ST + (((rg >> 1) * 2 + (hw & 1)) * 16 + 4 * (n & 3) + g) * 16;
    wait4<15, 0>(L, L_FS, C_V - sk);
    ST(9);
    PVN(7, 1);
    st_sc1<0x00FF00FF00FF00FFull>(ooff, v4u{tagt<12>(o[0], tv), tagt<12>(o[1], tv), tagt<12>(o[2], tv), tagt<12>(o[3], tv)}, ws);
    ST(10);
    if (hw < 2) {
      unsigned oo = WS_O + (rg * 2 + hw) * 4096 + lane * 16, oba = L_OB + hw * 128 + (4 * n + (g & 1) * 2 + (g >> 1)) * 2;
      DMA_S(" sc1", so, ws, lbase + L_K + hw * 1024);
      const float gate = *(LAS float*)(L + L_GATE + hw * 8);
      PIN(oo); PIN(oba);
      spin_until(tabs + (TO >> 4));
      ST(11);
      v4u a0, a1, a2, a3;
      asm volatile("global_load_dwordx4 %0, %4, %5 sc1\n\tglobal_load_dwordx4 %1, %4, %5 offset:1024 sc1\n\tglobal_load_dwordx4 %2, %4, %5 offset:2048 sc1\n\t"
                   "global_load_dwordx4 %3, %4, %5 offset:3072 sc1\n\ts_waitcnt vmcnt(4)" : "=&v"(a0), "=&v"(a1), "=&v"(a2), "=&v"(a3) : "v"(oo), "s"(ws) : "memory");
      v4u st = *(LAS v4u*)(L + L_K + hw * 1024 + lane * 16);
      if (anybad<12>(neq(st, t12))) {
#pragma unroll 1
        for (int it = 0; it < SPIN_CAP; ++it) { CNT(14); ld1w(st, so, ws); if (!anybad<12>(neq(st, t12))) break; }
      }
      const float ms = untag<12>(st.x), ls = untag<12>(st.y);
      float M = ms; M = fmaxf(M, dppf<XOR1>(M)); M = fmaxf(M, dppf<XOR2>(M)); M = x16max(M); M = x32max(M);
      const float ex = fexp(ms - M);
      float Ls = ex * ls; Ls += dppf<XOR1>(Ls); Ls += dppf<XOR2>(Ls); Ls = sw16(Ls, Ls); Ls = sw32(Ls, Ls);
      const float wv_ = gate * ex * frcp(Ls);
      const float wt0 = dppf<0x00>(wv_), wt1 = dppf<0x55>(wv_), wt2 = dppf<0xAA>(wv_), wt3 = dppf<0xFF>(wv_);
      f32x2 w0 = {wt0, wt0}, w1 = {wt1, wt1}, w2 = {wt2, wt2}, w3 = {wt3, wt3};
      PIN(w0); PIN(w1); PIN(w2); PIN(w3);
      asm volatile("s_waitcnt vmcnt(0)" : "+v"(a0), "+v"(a1), "+v"(a2), "+v"(a3)::"memory");
      ST(12);
#pragma unroll 1
      for (int it = 0; it < SPIN_CAP; ++it) {
#define UT2(a, i) f32x2{untag<12>(a[i]), untag<12>(a[i + 1])}
        const f32x2 lo = __builtin_elementwise_fma(w0, UT2(a0, 0), w1 * UT2(a1, 0)) + __builtin_elementwise_fma(w2, UT2(a2, 0), w3 * UT2(a3, 0));
        const f32x2 hi = __builtin_elementwise_fma(w0, UT2(a0, 2), w1 * UT2(a1, 2)) + __builtin_elementwise_fma(w2, UT2(a2, 2), w3 * UT2(a3, 2));
        const float t = sw16(sw32(lo[0], lo[1]), sw32(hi[0], hi[1]));
        *(volatile LAS u16*)(L + oba) = (u16)(pk_bf16(t, t) & 0xFFFFu);
        SB();
        if (!anybad<12>(neq(a0, t12) | neq(a1, t12) | neq(a2, t12) | neq(a3, t12))) break;
        CNT(15);
        ld4w(a0, a1, a2, a3, oo, ws);
      }
      LGKM0();
      ST(13);
      __builtin_amdgcn_s_barrier();
    } else if (hw == 2) {
      __builtin_amdgcn_s_barrier();
    } else {
      unsigned yo = WS_Y + (((xc >> 1) * 64 + lane) * 2 + (xc & 1)) * 512 + j * 16;
      unsigned op = ((xc & 1) * 512 + (xc >> 1) * 128 + 4 * j + (g & 1) * 2 + (g >> 1)) * 2;
      PIN(yo); PIN(op);
      __builtin_amdgcn_s_barrier();
      __builtin_amdgcn_s_sleep(4);
      spin_until(tabs + ((TO + TY) >> 4));
      ST(11);
      float t;
#pragma unroll 1
      for (int it = 0; it < SPIN_CAP; ++it) {
        v4u y;
        ld1w(y, yo, ws);
        ST(12);
        t = sw16(sw32(untag<12>(y.x), untag<12>(y.y)), sw32(untag<12>(y.z), untag<12>(y.w)));
        t += dppf<XOR1>(t); t += dppf<XOR2>(t); t += dppf<ROR4>(t); t += dppf<ROR8>(t);
        if (!anybad<12>(neq(y, t12))) break;
        CNT(15);
      }
      asm volatile("s_mov_b64 exec, %3\n\tglobal_store_short %0, %1, %2\n\ts_mov_b64 exec, -1" ::"v"(op), "v"(pk_bf16(t, t)), "s"(out), "s"(0x0001000100010001ull) : "memory");
      if (c == 0) asm volatile("s_mov_b64 exec, 1\n\tglobal_store_dword %0, %1, %2 sc1\n\ts_mov_b64 exec, -1" ::"v"(0), "v"(t32), "s"(ws) : "memory");
      ST(13);
    }
  } else {
    const int w = wv;
    const unsigned fs = L_FS + 4 * w;
    int issued = 5, landed = 0, due = TK0 + w * (TAU / 4), seen = 0, dt = 0;
#define PUBLISH(vm_) do { if (issued - (vm_) != landed) { landed = issued - (vm_); lds_set(L, fs, landed); } } while (0)
#define PACE() do { \
      const int v0_ = vm_inflight(); PUBLISH(v0_); \
      if (seen < due || v0_ >= WIN) { \
        const int d_ = *(volatile LAS int*)(L + L_DT); \
        _Pragma("unroll 1") for (int it_ = 0; it_ < SPIN_CAP; ++it_) { \
          seen = now16(t0) - dt; \
          const int vm_ = vm_inflight(); PUBLISH(vm_); \
          if (seen >= due && vm_ < WIN) break; } \
        const int dn_ = __builtin_amdgcn_readfirstlane(d_); seen += dt - dn_; dt = dn_; } \
      due += TAU; ++issued; } while (0)
    {
      const char* sb = w == 0 ? (const char*)x : w == 1 ? (const char*)x + 1024 : w == 2 ? (const char*)gw : (const char*)ws;
      DMA_S("", w == 3 ? 0 : lane * 16, sb, lbase + (w < 2 ? L_X : L_P - 2048) + w * 1024);
    }
    {
      const char* base = (const char*)wq + (size_t)(128 * qp + 8 * w) * 8192 + qc * 128;
      const unsigned voff = (lane >> 3) * 8192 + (lane & 7) * 16;
#pragma unroll
      for (int i = 0; i < 4; ++i) DMA_S(" nt", voff, base + (size_t)i * 32 * 8192, lbase + L_WQ + (4 * i + w) * 1024);
    }
    if (w == 0 && lane < 9) *(volatile LAS int*)(L + L_FS + 4 * lane) = lane < 8 ? -64 : 0;
    if (w == 1) *(volatile LAS v2u*)(L + L_Z + 8 * lane) = v2u{0, 0};
    ST(7);
    LGKM0();
    __builtin_amdgcn_s_barrier();
    if (has_f) {
      const unsigned voff = (lane >> 3) * 3584 + (lane & 7) * 16;
      const size_t off = (size_t)(64 * xc + 8 * w) * 3584 + j * 128;
#pragma unroll 1
      for (int i = 0; i < 4; ++i) { PACE(); DMA_S(" nt", voff, (const char*)(i < 2 ? wg : wu) + off + (i & 1) * (32 * 3584), lbase + L_GU + (4 * i + w) * 1024); }
    } else {
      due += 4 * TAU;
    }
    {
      const char* kb = (const char*)kc + kvoff + w * 1024;
      const unsigned voff = g * 256 + ((n ^ (4 * w + g)) * 16);
#pragma unroll 1
      for (int i = 0; i < 16; ++i) { PACE(); DMA_S(" nt", voff, kb + (((i + rot) & 15) << 12), lbase + L_K + i * 4096 + w * 1024); }
    }
    ST(1);
    {
      const unsigned voff = g * 256 + ((n ^ (2 * (g | ((w >> 1) << 2)))) * 16);
      const char* vb = (const char*)vc + kvoff + w * 1024;
#pragma unroll 1
      for (int i = 0; i < 16; ++i) {
        if (i == 8) wait4<3, 0>(L, L_FH, 1);
        if (i >= 16 - NL) due += TAUL - TAU;
        PACE(); DMA_S(" nt", voff, vb + (((i + rot) & 15) << 12), lbase + L_V + i * 4096 + w * 1024);
      }
    }
    ST(2);
    wait4<12, 0>(L, L_FH, 1);
    {
      const unsigned vo = g * 1024 + ((n ^ (2 * (g | ((w >> 1) << 2)))) * 16);
      const char* ob = (const char*)wo + (size_t)(64 * rg + 4 * w) * 1024 + cg * 256;
      const char* db = (const char*)wd + (size_t)(28 * rg) * 1024 + cg * 256;
#pragma unroll 1
      for (int i = 0; i < 6; ++i) {
        const char* src = i >= 2 ? ob + (i - 2) * 16384 : db + (i == 0 ? 4 * w : w < 3 ? 16 + 4 * w : 0) * 1024;
        due += TAU2 - TAU; PACE();
        DMA_S(" nt", vo, src, lbase + L_WO + (i >= 2 ? i - 2 : i + 4) * 4096 + w * 1024);
      }
    }
    ST(3);
#pragma unroll 1
    for (int it = 0; it < SPIN_CAP && landed < C_WD - sk; ++it) { const int vm = vm_inflight(); PUBLISH(vm); }
    ST(4);
    unsigned t12 = *(volatile LAS unsigned*)(L + L_TAG) & 0xFFFu;
    TR_ADDR(w);
    unsigned yoff = WS_Y + ((cg * 64 + rg) * 2 + (n & 1)) * 512 + w * 128 + ((n >> 1) & 1) * 64 + g * 16;
    unsigned oa = lbase + (n < 2 ? L_OB + n * 128 + g * 16 : L_Z), ob = lbase + ((n >> 1) == 1 ? L_OB + (n & 1) * 128 + g * 16 : L_Z);
    const float g1 = *(LAS float*)(L + L_GATE + (n & 1) * 8 + 4);
    wait4<12, 0>(L, L_FH, 5);
    const v4u ha = *(LAS v4u*)(L + (n < 2 ? L_HB + n * 64 + g * 16 : L_Z)), hb = *(LAS v4u*)(L + ((n >> 1) == 1 ? L_HB + (n & 1) * 64 + g * 16 : L_Z));
    wait4<15, 0>(L, L_FS, C_WD - sk);
    f32x4 y = mfma(tr2(L + L_WD + ta1, 1024), hb, mfma(tr2(L + L_WD + ta0, 1024), ha, f32x4{0, 0, 0, 0})) * g1;
    PIN(y);
    vmcnt<0>(); lds_set(L, fs, C_WO - sk);
    ST(5);
    wait4<15, 0>(L, L_FS, C_WO - sk);
    v4u wf[4];
    wf[0] = tr2(L + L_WO + ta0, 1024); wf[1] = tr2(L + L_WO + ta1, 1024); wf[2] = tr2(L + L_WO + ta0 + 8192, 1024); wf[3] = tr2(L + L_WO + ta1 + 8192, 1024);
    PIN(wf[0]); PIN(wf[1]); PIN(wf[2]); PIN(wf[3]); PIN(oa); PIN(ob); PIN(yoff); PIN(t12);
    __builtin_amdgcn_s_setprio(3);
    ST(6);
    __builtin_amdgcn_s_barrier();
    asm volatile("" ::: "memory");
    ST(10);
    {
      const v4u a0 = *(LAS v4u*)(uintptr_t)oa, b0 = *(LAS v4u*)(uintptr_t)ob, a1 = *(LAS v4u*)(uintptr_t)(oa + 64), b1 = *(LAS v4u*)(uintptr_t)(ob + 64);
      SB();
      y = mfma(wf[0], a0, y); y = mfma(wf[1], b0, y); y = mfma(wf[2], a1, y); y = mfma(wf[3], b1, y);
      st_sc1<0x000F000F000F000Full>(yoff, v4u{tagt<12>(y[0], t12), tagt<12>(y[1], t12), tagt<12>(y[2], t12), tagt<12>(y[3], t12)}, ws);
    }
    ST(11);
  }
  if (wv == PWAVE) {
    const unsigned tg = __builtin_amdgcn_readfirstlane(*(volatile LAS unsigned*)(L + L_TAG));
    st_sc1<1>(WS_STAMP + c * 128, v4u{stamp[PROBE], tg, stamp[0], tg}, ws);
    if (c == 0) {
      for (int it = 0; it < 1000000; ++it) { if ((unsigned)__builtin_amdgcn_s_memrealtime() - stamp[0] >= 2500u) break; }
      int mx = -100000, mn = 100000;
      for (int k = 0; k < 4; ++k) { v4u e; ld1w(e, WS_STAMP + (lane + 64 * k) * 128, ws);
        if (e.y == tg) { mx = max(mx, PMODE ? (int)(e.x - e.z) : (int)(e.x - stamp[0])); mn = PMODE ? 0 : min(mn, (int)(e.z - stamp[0])); } }
      for (int o = 32; o > 0; o >>= 1) { mx = max(mx, __shfl_xor(mx, o)); mn = min(mn, __shfl_xor(mn, o)); }
      const unsigned tgt = 4000u + AMP * (unsigned)(__builtin_amdgcn_readfirstlane(mx) - __builtin_amdgcn_readfirstlane(mn));
      for (int it = 0; it < 1000000; ++it) { if ((unsigned)__builtin_amdgcn_s_memrealtime() - stamp[0] >= tgt) break; }
    }
  }
}

extern "C" void kernel_launch(void* const* d_in, const int* in_sizes, int n_in, void* d_out, int out_size, void* d_ws, size_t ws_size, hipStream_t stream) {
  (void)hipFuncSetAttribute((const void*)fused_decode, hipFuncAttributeMaxDynamicSharedMemorySize, L_END);
  fused_decode<<<256, 512, L_END, stream>>>((const u16*)d_in[0], (const u16*)d_in[9], (char*)d_ws, (const u16*)d_in[3], (const u16*)d_in[6], (const u16*)d_in[7],
                                            (const u16*)d_in[1], (const u16*)d_in[2], (const u16*)d_in[4], (const u16*)d_in[8], (u16*)d_out);
}
```

```cpp
#include <hip/hip_runtime.h>
#include <hip/hip_bf16.h>
#include <stdint.h>

#define STAMP 0
#define TAU 256
#define TK0 280
#define TAUL TAU
#define NL 0
#define TAU2 256
#define TT 2900
#define TQ 4300
#define TO 2950
#define TY 1650
#define ORD 1
#define ROT 0
#define GAP 0
#define PF 0
#define TVE (TK0 + (35 - NL) * TAU + NL * TAUL)
#define WIN 12
#define CODE_PF 23

typedef unsigned short u16;
typedef unsigned long long u64;
typedef unsigned v4u __attribute__((ext_vector_type(4)));
typedef unsigned v16u __attribute__((ext_vector_type(16)));
typedef int v4i __attribute__((ext_vector_type(4)));
typedef unsigned v2u __attribute__((ext_vector_type(2)));
typedef short v4s __attribute__((ext_vector_type(4)));
typedef float f32x4 __attribute__((ext_vector_type(4)));
typedef float f32x2 __attribute__((ext_vector_type(2)));
typedef __bf16 v8bf __attribute__((ext_vector_type(8)));
typedef __bf16 v2bf __attribute__((ext_vector_type(2)));
#define GAS __attribute__((address_space(1)))
#define LAS __attribute__((address_space(3)))
#define SB() __builtin_amdgcn_sched_barrier(0)

constexpr unsigned WS_T = 1024;
constexpr unsigned WS_Q = 4096;
constexpr unsigned WS_H = WS_Q + 131072;
constexpr unsigned WS_ST = WS_H + 229376;
constexpr unsigned WS_O = WS_ST + 32768;
constexpr unsigned WS_Y = WS_O + 524288;
constexpr unsigned WS_STAMP = 4u << 20;
constexpr unsigned L_K = 0, L_V = 65536, L_WQ = L_V + 8 * 4096, L_WO = 131072, L_GU = L_WO, L_WD = L_WO + 16384, L_X = L_WD + 8192,
                   L_Q = L_X + 2048, QSTR = 320, L_P = L_Q + 4 * QSTR, PSTR = 576, L_OB = L_P + 4 * PSTR, L_HB = L_OB + 256, L_MX = L_HB + 128,
                   L_SUM = L_MX + 64, L_GATE = L_SUM + 64, L_FS = L_GATE + 16, L_FH = L_FS + 16, L_DT = L_FH + 16, L_TAG = L_DT + 4, L_T0 = L_DT + 8,
                   L_Z = L_DT + 16, L_END = L_Z + 512;
static_assert(L_END <= 163840, "LDS");
constexpr int SPIN_CAP = 1 << 17;

__device__ __forceinline__ float asf(unsigned u) { return __builtin_bit_cast(float, u); }
__device__ __forceinline__ unsigned asu(float f) { return __builtin_bit_cast(unsigned, f); }
__device__ __forceinline__ unsigned pk_bf16(float lo, float hi) { f32x2 x = {lo, hi}; return __builtin_bit_cast(unsigned, __builtin_convertvector(x, v2bf)); }
__device__ __forceinline__ float rbf(float f) { return asf(pk_bf16(f, f) << 16); }
template <int CTRL> __device__ __forceinline__ float dppf(float x) { return asf(__builtin_amdgcn_mov_dpp(asu(x), CTRL, 0xf, 0xf, true)); }
constexpr int XOR1 = 0xB1, XOR2 = 0x4E, HMIR = 0x141, ROR4 = 0x124, ROR8 = 0x128;
__device__ __forceinline__ float sw16(float a, float b) { auto s = __builtin_amdgcn_permlane16_swap(asu(a), asu(b), false, false); return asf(s[0]) + asf(s[1]); }
__device__ __forceinline__ float sw32(float a, float b) { auto s = __builtin_amdgcn_permlane32_swap(asu(a), asu(b), false, false); return asf(s[0]) + asf(s[1]); }
__device__ __forceinline__ float x16max(float x) { auto s = __builtin_amdgcn_permlane16_swap(asu(x), asu(x), false, false); return fmaxf(asf(s[0]), asf(s[1])); }
__device__ __forceinline__ float x32max(float x) { auto s = __builtin_amdgcn_permlane32_swap(asu(x), asu(x), false, false); return fmaxf(asf(s[0]), asf(s[1])); }
__device__ __forceinline__ float frcp(float x) { return __builtin_amdgcn_rcpf(x); }
__device__ __forceinline__ float fexp(float x) { return __builtin_amdgcn_exp2f(x * 1.4426950408889634f); }
__device__ __forceinline__ f32x4 mfma(v4u a, v4u b, f32x4 c) { return __builtin_amdgcn_mfma_f32_16x16x32_bf16(__builtin_bit_cast(v8bf, a), __builtin_bit_cast(v8bf, b), c, 0, 0, 0); }
__device__ __forceinline__ v2u trread(LAS char* p) { return __builtin_bit_cast(v2u, __builtin_amdgcn_ds_read_tr16_b64_v4i16((LAS v4s*)p)); }
__device__ __forceinline__ v4u tr2(LAS char* p, int d) { v2u a = trread(p), b = trread(p + d); return v4u{a.x, a.y, b.x, b.y}; }
template <int B> __device__ __forceinline__ unsigned tagt(float v, unsigned t) { return (asu(v) & ~((1u << B) - 1)) | t; }
template <int B> __device__ __forceinline__ unsigned tagf(float v, unsigned t) { return ((asu(v) + (1u << (B - 1))) & ~((1u << B) - 1)) | t; }
template <int B> __device__ __forceinline__ float untag(unsigned w) { return asf(w & ~((1u << B) - 1)); }
__device__ __forceinline__ unsigned neq(v4u g, unsigned t) { return __builtin_amdgcn_bitop3_b32(g.x, g.y, t, 0x7E) | __builtin_amdgcn_bitop3_b32(g.z, g.w, t, 0x7E); }
template <int B> __device__ __forceinline__ bool anybad(unsigned x) { return __builtin_amdgcn_ballot_w64((x & ((1u << B) - 1)) != 0) != 0; }
#define P64(lo, hi) ((char*)(((u64)(hi) << 32) | (lo)))
#define PINS(x) asm volatile("" : "+s"(x))
#define PIN(x) asm volatile("" : "+v"(x))
#define CNT(i)
#define CNT0(i)

#define DMA_S(MOD, voff, sbase, ldst) asm volatile("s_mov_b32 m0, %2\n\ts_nop 0\n\tglobal_load_lds_dwordx4 %0, %1" MOD :: "v"(voff), "s"(sbase), "s"(ldst) : "memory")
template <int N> __device__ __forceinline__ void vmcnt() { asm volatile("s_waitcnt vmcnt(%0)" ::"i"(N) : "memory"); }
#define LGKM0() asm volatile("s_waitcnt lgkmcnt(0)" ::: "memory")
__device__ __forceinline__ void ld1w(v4u& a, unsigned o0, const char* sb) {
  asm volatile("global_load_dwordx4 %0, %1, %2 sc1\n\ts_waitcnt vmcnt(0)" : "=&v"(a) : "v"(o0), "s"(sb) : "memory"); }
__device__ __forceinline__ void ld2w(v4u& a, v4u& b, unsigned o0, unsigned o1, const char* sb) {
  asm volatile("global_load_dwordx4 %0, %2, %4 sc1\n\tglobal_load_dwordx4 %1, %3, %4 sc1\n\ts_waitcnt vmcnt(0)" : "=&v"(a), "=&v"(b) : "v"(o0), "v"(o1), "s"(sb) : "memory"); }
__device__ __forceinline__ void ld4w(v4u& a, v4u& b, v4u& c, v4u& d, unsigned o0, const char* sb) {
  asm volatile("global_load_dwordx4 %0, %4, %5 sc1\n\tglobal_load_dwordx4 %1, %4, %5 offset:1024 sc1\n\tglobal_load_dwordx4 %2, %4, %5 offset:2048 sc1\n\t"
               "global_load_dwordx4 %3, %4, %5 offset:3072 sc1\n\ts_waitcnt vmcnt(0)" : "=&v"(a), "=&v"(b), "=&v"(c), "=&v"(d) : "v"(o0), "s"(sb) : "memory"); }
template <u64 MASK> __device__ __forceinline__ void st_sc1(unsigned voff, v4u d, char* sbase) {
  asm volatile("s_mov_b64 exec, %3\n\tglobal_store_dwordx4 %0, %1, %2 sc1\n\ts_mov_b64 exec, -1\n\ts_nop 0" ::"v"(voff), "v"(d), "s"(sbase), "s"(MASK) : "memory"); }

#define ST(i)
#define STAMP0()

__device__ __forceinline__ int now16(unsigned t0) { return (int)(((unsigned)__builtin_amdgcn_s_memrealtime() - t0) * 16u); }
__device__ __forceinline__ int vm_inflight() { unsigned r; asm volatile("s_getreg_b32 %0, hwreg(HW_REG_IB_STS, 0, 4)" : "=s"(r)::"memory"); return (int)r; }
__device__ __forceinline__ int clock_offset(LAS char* L) { return __builtin_amdgcn_readfirstlane(*(volatile LAS int*)(L + L_DT)); }
__device__ __forceinline__ void wait_until(LAS char* L, unsigned t0, int t) {
#pragma unroll 1
  for (int it = 0; it < SPIN_CAP && now16(t0) - clock_offset(L) < t; ++it) __builtin_amdgcn_s_sleep(1);
}
__device__ __forceinline__ void spin_until(unsigned tick) {
#pragma unroll 1
  for (int it = 0; it < SPIN_CAP && (int)((unsigned)__builtin_amdgcn_s_memrealtime() - tick) < 0; ++it) {}
}
__device__ __forceinline__ void lds_set(LAS char* L, unsigned off, unsigned v) {
  asm volatile("s_mov_b64 exec, 1\n\tds_write_b32 %0, %1\n\ts_mov_b64 exec, -1" ::"v"((unsigned)(uintptr_t)(L + off)), "v"(v) : "memory");
}
template <int MASK, int SLEEP> __device__ __forceinline__ int wait4(LAS char* L, unsigned off, int n) {
  int r = 0;
#pragma unroll 1
  for (int it = 0; it < SPIN_CAP; ++it) {
    const v4i f = *(volatile LAS v4i*)(L + off);
    int m = 0x7fffffff;
    if (MASK & 1) m = min(m, f.x);
    if (MASK & 2) m = min(m, f.y);
    if (MASK & 4) m = min(m, f.z);
    if (MASK & 8) m = min(m, f.w);
    r = __builtin_amdgcn_readfirstlane(m);
    if (r >= n) break;
    if (SLEEP) __builtin_amdgcn_s_sleep(SLEEP);
  }
  asm volatile("" ::: "memory");
  return r;
}
#define TR_ADDR(w_) const int fr = (n >> 2) | ((g & 1) << 2); \
    unsigned ta0 = (8 * g + (n >> 2)) * 256 + ((((2 * (w_)) ^ fr) * 2 + ((n & 3) >> 1)) * 16) + (n & 1) * 8; \
    unsigned ta1 = (8 * g + (n >> 2)) * 256 + ((((2 * (w_) + 1) ^ fr) * 2 + ((n & 3) >> 1)) * 16) + (n & 1) * 8; PIN(ta0); PIN(ta1)

constexpr int C_V = 41, C_WD = 43, C_WO = 47;

__global__ void __launch_bounds__(512) __attribute__((amdgpu_waves_per_eu(2, 2)))
fused_decode(const u16* x_, const u16* gw_, char* ws_, const u16* wq_, const u16* wg_, const u16* wu_, const u16* kc_, const u16* vc_, const u16* wo_,
             const u16* wd_, u16* out_) {
  asm volatile("s_load_dwordx16 s[76:91], %0, 0x0\n\ts_load_dwordx4 s[92:95], %0, 0x40\n\ts_load_dwordx2 s[96:97], %0, 0x50 ; ARGS_LOAD"
               :: "s"(__builtin_amdgcn_kernarg_segment_ptr()) : "s76", "s77", "s78", "s79", "s80", "s81", "s82", "s83", "s84", "s85", "s86", "s87", "s88", "s89", "s90", "s91", "s92", "s93", "s94", "s95", "s96", "s97");
#define ARGS(EXTRA) v16u ka; v4u kb; v2u kd; asm volatile("s_waitcnt lgkmcnt(0) ; ARGS_WAIT" EXTRA : "={s[76:91]}"(ka), "={s[92:95]}"(kb), "={s[96:97]}"(kd) :: "s98", "s99"); \
  const char* x = P64(ka[0], ka[1]); const char* gw = P64(ka[2], ka[3]); char* ws = P64(ka[4], ka[5]); const char* wq = P64(ka[6], ka[7]); \
  const char* wg = P64(ka[8], ka[9]); const char* wu = P64(ka[10], ka[11]); const char* kc = P64(ka[12], ka[13]); const char* vc = P64(ka[14], ka[15]); \
  const char* wo = P64(kb[0], kb[1]); const char* wd = P64(kb[2], kb[3]); char* out = P64(kd[0], kd[1])
#define T0() v2u tt; asm volatile("s_waitcnt lgkmcnt(0) ; T0_WAIT" : "={s[98:99]}"(tt)::"memory"); const unsigned t0 = tt[0]; STAMP0()
  extern __shared__ __attribute__((aligned(16))) char smem[];
  LAS char* L = (LAS char*)smem;
  const unsigned lbase = (unsigned)(uintptr_t)L;
  const int c = blockIdx.x, lane = threadIdx.x & 63, wv = __builtin_amdgcn_readfirstlane(threadIdx.x >> 6);
  const int xc = c & 7, j = c >> 3;
  const int kvh = xc, b = j & 1, sp = j >> 1;
  const int rg = 8 * xc + (j >> 2), cg = j & 3;
  const int qp = xc >> 1, qc = 32 * (xc & 1) + j;
  const bool has_f = j < 28;
  const int sk = has_f ? 0 : 4;
  const int g = lane >> 4, n = lane & 15;
  const int rot = ROT == 0 ? (j >> 2) * 2 + (j & 1) : ROT == 1 ? sp : ROT == 2 ? sp + 8 * b + xc : (sp >> 1) + 8 * (sp & 1) + 4 * b;
  const int stp = ORD == 0 ? 1 : ORD == 1 ? 2 * xc + 1 : ORD == 2 ? 2 * ((xc + (j >> 2)) & 7) + 1 : 2 * ((xc + j) & 7) + 1;
  const size_t kvoff = ((size_t)(b * 8 + kvh) * 16384 + 12288 + sp * 256) * 256;
  if (wv == 4) {
    const char* pc;
    asm volatile("s_getpc_b64 %0" : "=s"(pc));
    asm volatile("s_mov_b32 m0, %2\n\ts_nop 0\n\tglobal_load_lds_dword %0, %1" ::"v"(min(j, CODE_PF) * 512 + (lane & 7) * 64), "s"((const char*)((uintptr_t)pc & ~(uintptr_t)255)), "s"(lbase + L_OB) : "memory");
  }

  if (wv >= 4) {
    const int hw = wv - 4;
    const unsigned fh = L_FH + 4 * hw;
    __builtin_amdgcn_s_setprio(2);
    ARGS("");
    if (hw == 1) {
      const char* a = lane == 0 ? (const char*)vc + kvoff : lane == 1 ? (const char*)wo + (size_t)rg * 65536 : lane == 2 ? (const char*)wd + (size_t)rg * 28672
                    : lane == 3 ? (const char*)out : (const char*)kc + kvoff;
      asm volatile("s_mov_b32 m0, %1\n\ts_nop 0\n\tglobal_load_lds_dword %0, off" ::"v"(a), "s"(lbase + L_OB) : "memory");
    }
    __builtin_amdgcn_s_barrier();
    const unsigned t0 = __builtin_amdgcn_readfirstlane(*(volatile LAS unsigned*)(L + L_T0));
    STAMP0();
    wait4<15, 0>(L, L_FS, 1);
    const unsigned t32 = __builtin_amdgcn_readfirstlane(*(LAS unsigned*)(L + L_P + 1024)) + 1, t12 = t32 & 0xFFFu, t8 = t32 & 0xFFu;
    ST(1);
    if (hw < 2) {
      v4u xq[4];
#pragma unroll
      for (int s = 0; s < 4; ++s) xq[s] = *(LAS v4u*)(L + L_X + (n & 1) * 1024 + qp * 256 + s * 64 + g * 16);
      const int col = 64 * qc + 32 * hw + 16 * ((n >> 1) & 1) + 4 * g;
      const unsigned qoff = WS_Q + (((((col >> 9) * 2 + (n & 1)) * 4 + ((col >> 7) & 3)) * 4 + qp) * 128 + (col & 127)) * 4;
      LAS char* a = L + L_WQ + (8 * g + (n >> 2)) * 128 + hw * 64 + (n & 3) * 8;
      wait4<15, 0>(L, L_FS, 5);
      ST(2);
      v4u af[8];
#pragma unroll
      for (int s = 0; s < 4; ++s) { af[2 * s] = tr2(a + s * 4096, 512); af[2 * s + 1] = tr2(a + s * 4096 + 32, 512); }
      SB();
      f32x4 d0 = {0, 0, 0, 0}, d1 = {0, 0, 0, 0};
#pragma unroll
      for (int s = 0; s < 4; ++s) { d0 = mfma(af[2 * s], xq[s], d0); d1 = mfma(af[2 * s + 1], xq[s], d1); }
      v4u gr;
#pragma unroll
      for (int r = 0; r < 4; ++r) gr[r] = tagf<8>((n & 2) ? d1[r] : d0[r], t8);
      st_sc1<0x000F000F000F000Full>(qoff, gr, ws);
    } else {
      if (hw == 2 && c < 8) st_sc1<0xFF>(WS_T + c * 128 + n * 16, v4u{t0, t32, t0, t32}, ws);
      {
        const v4u gwv = *(LAS v4u*)(L + L_P + lane * 16), xv = *(LAS v4u*)(L + L_X + (hw & 1) * 1024 + lane * 16);
        float s = 0.f;
#pragma unroll
        for (int k = 0; k < 4; ++k) {
          const unsigned pr = pk_bf16(asf(xv[k] << 16) * asf(gwv[k] << 16), asf(xv[k] & 0xFFFF0000u) * asf(gwv[k] & 0xFFFF0000u));
          s += asf(pr << 16) + asf(pr & 0xFFFF0000u);
        }
        s += dppf<XOR1>(s); s += dppf<XOR2>(s); s += dppf<ROR4>(s); s += dppf<ROR8>(s); s = sw16(s, s); s = sw32(s, s);
        const float gs = rbf(frcp(1.f + fexp(-rbf(s))));
        if (lane == 0) *(LAS f32x2*)(L + L_GATE + (hw & 1) * 8) = f32x2{gs, rbf(1.f - gs)};
        lds_set(L, L_TAG, t32);
      }
      v4u xg[2];
#pragma unroll
      for (int s = 0; s < 2; ++s) xg[s] = *(LAS v4u*)(L + L_X + (n & 1) * 1024 + xc * 128 + s * 64 + g * 16);
      const unsigned hoff = WS_H + ((((n & 1) * 2 + (hw & 1)) * 8 + xc) * 448 + 16 * j + 4 * ((n >> 1) & 3) + g) * 16;
      LAS char* a = L + L_GU + (hw & 1) * 8192 + (8 * g + (n >> 2)) * 128 + (n & 3) * 8;
      wait4<15, 0>(L, L_FS, 9 - sk);
      ST(2);
      v4u af[8];
#pragma unroll
      for (int s = 0; s < 2; ++s)
#pragma unroll
        for (int t = 0; t < 4; ++t) af[4 * s + t] = tr2(a + s * 4096 + t * 32, 512);
      SB();
      f32x4 d[4];
#pragma unroll
      for (int t = 0; t < 4; ++t) d[t] = mfma(af[4 + t], xg[1], mfma(af[t], xg[0], f32x4{0, 0, 0, 0}));
      v4u gr;
#pragma unroll
      for (int r = 0; r < 4; ++r) { const float lo = (n & 2) ? d[1][r] : d[0][r], hi = (n & 2) ? d[3][r] : d[2][r]; gr[r] = tagf<12>((n & 4) ? hi : lo, t12); }
      PIN(gr);
      if (has_f) st_sc1<0x00FF00FF00FF00FFull>(hoff, gr, ws);
    }
    lds_set(L, fh, 1);
    ST(3);
    if (hw == 1) {
      wait_until(L, t0, TT);
      v4u tv;
      ld1w(tv, WS_T + (lane & 7) * 128, ws);
      const bool ok = tv.y == t32 && tv.w == t32;
      float d = ok ? (float)(int)(tv.x - t0) : 0.f, k = ok ? 1.f : 0.f;
      d += dppf<XOR1>(d); k += dppf<XOR1>(k); d += dppf<XOR2>(d); k += dppf<XOR2>(k); d += dppf<HMIR>(d); k += dppf<HMIR>(k);
      lds_set(L, L_DT, (int)fminf(fmaxf(d * 16.f * frcp(fmaxf(k, 1.f)), -1600.f), 1600.f));
    }
    {
      const unsigned qo = WS_Q + ((kvh * 2 + b) * 4 + hw) * 2048 + lane * 16;
      wait_until(L, t0, TQ);
      v4u q0, q1;
      CNT0(14); CNT0(15);
#pragma unroll 1
      for (int it = 0; it < SPIN_CAP; ++it) { ld2w(q0, q1, qo, qo + 1024, ws); if (!anybad<8>(neq(q0, t8) | neq(q1, t8))) break; CNT(14); }
      float v[4];
#pragma unroll
      for (int r = 0; r < 4; ++r) { v[r] = untag<8>(q0[r]) + untag<8>(q1[r]); v[r] = sw32(v[r], v[r]); }
      if (lane < 32) *(LAS v2u*)(L + L_Q + hw * QSTR + lane * 8) = v2u{pk_bf16(v[0], v[1]), pk_bf16(v[2], v[3])};
      LGKM0(); lds_set(L, fh, 2);
      ST(4);
    }
    wait4<15, 1>(L, L_FH, 2);
    v4u qf[4], kf[16];
#pragma unroll
    for (int s = 0; s < 4; ++s) qf[s] = *(LAS v4u*)(L + L_Q + (n & 3) * QSTR + s * 64 + g * 16);
    wait4<15, 0>(L, L_FS, 13 + 4 * hw - sk);
    ST(6);
#pragma unroll
    for (int s = 0; s < 4; ++s)
#pragma unroll
      for (int t = 0; t < 4; ++t) kf[s * 4 + t] = *(LAS v4u*)(L + L_K + (4 * hw + t) * 4096 + n * 256 + (((4 * s + g) ^ n) * 16));
    SB();
    float sc[4];
    {
      f32x4 d[4];
#pragma unroll
      for (int t = 0; t < 4; ++t) d[t] = f32x4{0, 0, 0, 0};
#pragma unroll
      for (int s = 0; s < 4; ++s)
#pragma unroll
        for (int t = 0; t < 4; ++t) d[t] = mfma(kf[s * 4 + t], qf[s], d[t]);
      constexpr float SC = 0.08838834764831845f;
      float v[4];
#pragma unroll
      for (int r = 0; r < 4; ++r) { const float lo = (n & 4) ? d[1][r] : d[0][r], hi = (n & 4) ? d[3][r] : d[2][r]; v[r] = (n & 8) ? hi : lo; }
      const unsigned a = pk_bf16(v[0], v[1]), bq = pk_bf16(v[2], v[3]);
      const unsigned a2 = pk_bf16(asf(a << 16) * SC, asf(a & 0xFFFF0000u) * SC), b2 = pk_bf16(asf(bq << 16) * SC, asf(bq & 0xFFFF0000u) * SC);
      sc[0] = asf(a2 << 16); sc[1] = asf(a2 & 0xFFFF0000u); sc[2] = asf(b2 << 16); sc[3] = asf(b2 & 0xFFFF0000u);
    }
    float m = fmaxf(fmaxf(sc[0], sc[1]), fmaxf(sc[2], sc[3]));
    m = fmaxf(m, dppf<ROR4>(m)); m = fmaxf(m, dppf<ROR8>(m)); m = x16max(m); m = x32max(m);
    if (lane < 4) *(LAS float*)(L + L_MX + lane * 16 + hw * 4) = m;
    LGKM0(); lds_set(L, fh, 3);
    wait4<15, 0>(L, L_FH, 3);
    const int e = lane & 7;
    const unsigned ho = WS_H + (((hw & 1) * 16 + (lane >> 3)) * 448 + 7 * rg + (e < 7 ? e : 6)) * 16;
    if (hw >= 2) { DMA_S(" sc1", ho, ws, lbase + L_K + hw * 2048); DMA_S(" sc1", ho + 8 * 448 * 16, ws, lbase + L_K + hw * 2048 + 1024); }
    { const f32x4 mm = *(LAS f32x4*)(L + L_MX + (n & 3) * 16); m = fmaxf(fmaxf(mm[0], mm[1]), fmaxf(mm[2], mm[3])); }
    {
      float p[4];
#pragma unroll
      for (int r = 0; r < 4; ++r) p[r] = fexp(sc[r] - m);
      *(LAS v2u*)(L + L_P + (n & 3) * PSTR + (64 * hw + 16 * (n >> 2) + 4 * g) * 2) = v2u{pk_bf16(p[0], p[1]), pk_bf16(p[2], p[3])};
      float l = (p[0] + p[1]) + (p[2] + p[3]);
      l += dppf<ROR4>(l); l += dppf<ROR8>(l); l = sw16(l, l); l = sw32(l, l);
      if (lane < 4) *(LAS float*)(L + L_SUM + lane * 16 + hw * 4) = l;
    }
    LGKM0(); lds_set(L, fh, 4);
    ST(7);
    TR_ADDR(hw);
    unsigned ooff = WS_O + (((2 * (4 * kvh + (n & 3)) + (hw >> 1)) * 2 + b) * 16 + sp) * 256 + (hw & 1) * 128 + ((n >> 2) & 1) * 64 + g * 16;
    unsigned tv = t12;
    PIN(ooff); PIN(tv);
    const int tabs = t0 + ((clock_offset(L) + TVE) >> 4);
    wait4<15, 0>(L, L_FH, 4);
    v4u pa[8], pb[8];
    {
      const unsigned pp = L_P + (n & 3) * PSTR + g * 16, aa = n < 4 ? pp : L_Z, ab = (n >> 2) == 1 ? pp : L_Z;
#pragma unroll
      for (int u = 0; u < 8; ++u) { pa[u] = *(LAS v4u*)(L + aa + u * 64); pb[u] = *(LAS v4u*)(L + ab + u * 64); }
    }
    if (hw == 2) {
      const f32x4 ls = *(LAS f32x4*)(L + L_SUM + (n & 3) * 16);
      st_sc1<0xFF>(WS_ST + ((kvh * 2 + b) * 16 + sp) * 128 + n * 16, v4u{tagt<12>(m, t12), tagf<12>((ls[0] + ls[1]) + (ls[2] + ls[3]), t12), t12, t12}, ws);
    }
    f32x4 o = {0, 0, 0, 0};
#define PVN(u0, nu) do { v4u va[nu], vb[nu]; _Pragma("unroll") for (int u = 0; u < nu; ++u) { va[u] = tr2(L + L_V + ta0 + (u0 + u) * 8192, 1024); vb[u] = tr2(L + L_V + ta1 + (u0 + u) * 8192, 1024); } SB(); \
      _Pragma("unroll") for (int u = 0; u < nu; ++u) { o = mfma(va[u], pa[u0 + u], o); o = mfma(vb[u], pb[u0 + u], o); } } while (0)
    const unsigned so = WS_ST + ((xc * 2 + (hw & 1)) * 16 + 4 * (n & 3) + g) * 128 + (j >> 3) * 16;
    if (wait4<15, 1>(L, L_FS, 33 - sk) >= C_V - sk) {
      PVN(0, 8);
    } else {
      PVN(0, 4); PIN(o);
      ST(8);
      if (wait4<15, 0>(L, L_FS, 39 - sk) >= C_V - sk) {
        PVN(4, 4);
      } else {
        PVN(4, 3); PIN(o);
        wait4<15, 0>(L, L_FS, C_V - sk);
        ST(9);
        PVN(7, 1);
      }
    }
    st_sc1<0x00FF00FF00FF00FFull>(ooff, v4u{tagt<12>(o[0], tv), tagt<12>(o[1], tv), tagt<12>(o[2], tv), tagt<12>(o[3], tv)}, ws);
    ST(10);
    if (hw >= 2) {
      vmcnt<1>();
      v4u h0 = *(LAS v4u*)(L + L_K + hw * 2048 + lane * 16), h1 = *(LAS v4u*)(L + L_K + hw * 2048 + 1024 + lane * 16);
#pragma unroll 1
      for (int it = 0; it < SPIN_CAP && anybad<12>(neq(h0, t12) | neq(h1, t12)); ++it) { ld2w(h0, h1, ho, ho + 8 * 448 * 16, ws); CNT(14); }
      float v[4];
#pragma unroll
      for (int r = 0; r < 4; ++r) v[r] = sw32(untag<12>(h0[r]), untag<12>(h1[r]));
      const float a0 = sw16(v[0], v[1]), a1 = sw16(v[2], v[3]);
      const float b0 = a0 + dppf<ROR8>(a0), b1 = a1 + dppf<ROR8>(a1);
      const float gg = rbf((lane & 8) ? b1 : b0);
      const auto sw = __builtin_amdgcn_permlane32_swap(asu(gg), asu(gg), false, false);
      const float hv = rbf(gg * frcp(1.f + fexp(-gg))) * asf(sw[1]);
      if (lane < 32) *(LAS u16*)(L + L_HB + (hw & 1) * 64 + (4 * e + ((lane >> 2) & 2) + (lane >> 4)) * 2) = e < 7 ? (u16)(pk_bf16(hv, hv) & 0xFFFFu) : (u16)0;
      LGKM0(); lds_set(L, fh, 5);
      ST(5);
    }
    if (hw < 2) {
      unsigned oo = WS_O + (rg * 2 + hw) * 4096 + lane * 16, oba = L_OB + hw * 128 + (4 * n + (g & 1) * 2 + (g >> 1)) * 2;
      DMA_S(" sc1", so, ws, lbase + L_K + hw * 1024);
      const float gate = *(LAS float*)(L + L_GATE + hw * 8);
      PIN(oo); PIN(oba);
      spin_until(tabs + (TO >> 4));
      ST(11);
      v4u a0, a1, a2, a3;
      asm volatile("global_load_dwordx4 %0, %4, %5 sc1\n\tglobal_load_dwordx4 %1, %4, %5 offset:1024 sc1\n\tglobal_load_dwordx4 %2, %4, %5 offset:2048 sc1\n\t"
                   "global_load_dwordx4 %3, %4, %5 offset:3072 sc1\n\ts_waitcnt vmcnt(4)" : "=&v"(a0), "=&v"(a1), "=&v"(a2), "=&v"(a3) : "v"(oo), "s"(ws) : "memory");
      v4u st = *(LAS v4u*)(L + L_K + hw * 1024 + lane * 16);
      if (anybad<12>(neq(st, t12))) {
#pragma unroll 1
        for (int it = 0; it < SPIN_CAP; ++it) { CNT(14); ld1w(st, so, ws); if (!anybad<12>(neq(st, t12))) break; }
      }
      const float ms = untag<12>(st.x), ls = untag<12>(st.y);
      float M = ms; M = fmaxf(M, dppf<XOR1>(M)); M = fmaxf(M, dppf<XOR2>(M)); M = x16max(M); M = x32max(M);
      const float ex = fexp(ms - M);
      float Ls = ex * ls; Ls += dppf<XOR1>(Ls); Ls += dppf<XOR2>(Ls); Ls = sw16(Ls, Ls); Ls = sw32(Ls, Ls);
      const float wv_ = gate * ex * frcp(Ls);
      const float wt0 = dppf<0x00>(wv_), wt1 = dppf<0x55>(wv_), wt2 = dppf<0xAA>(wv_), wt3 = dppf<0xFF>(wv_);
      f32x2 w0 = {wt0, wt0}, w1 = {wt1, wt1}, w2 = {wt2, wt2}, w3 = {wt3, wt3};
      PIN(w0); PIN(w1); PIN(w2); PIN(w3);
      asm volatile("s_waitcnt vmcnt(0)" : "+v"(a0), "+v"(a1), "+v"(a2), "+v"(a3)::"memory");
      ST(12);
#pragma unroll 1
      for (int it = 0; it < SPIN_CAP; ++it) {
#define UT2(a, i) f32x2{untag<12>(a[i]), untag<12>(a[i + 1])}
        const f32x2 lo = __builtin_elementwise_fma(w0, UT2(a0, 0), w1 * UT2(a1, 0)) + __builtin_elementwise_fma(w2, UT2(a2, 0), w3 * UT2(a3, 0));
        const f32x2 hi = __builtin_elementwise_fma(w0, UT2(a0, 2), w1 * UT2(a1, 2)) + __builtin_elementwise_fma(w2, UT2(a2, 2), w3 * UT2(a3, 2));
        const float t = sw16(sw32(lo[0], lo[1]), sw32(hi[0], hi[1]));
        *(volatile LAS u16*)(L + oba) = (u16)(pk_bf16(t, t) & 0xFFFFu);
        SB();
        if (!anybad<12>(neq(a0, t12) | neq(a1, t12) | neq(a2, t12) | neq(a3, t12))) break;
        CNT(15);
        ld4w(a0, a1, a2, a3, oo, ws);
      }
      LGKM0();
      ST(13);
      __builtin_amdgcn_s_barrier();
    } else if (hw == 2) {
      __builtin_amdgcn_s_barrier();
    } else {
      unsigned yo = WS_Y + (((xc >> 1) * 64 + lane) * 2 + (xc & 1)) * 512 + j * 16;
      unsigned op = ((xc & 1) * 512 + (xc >> 1) * 128 + 4 * j + (g & 1) * 2 + (g >> 1)) * 2;
      PIN(yo); PIN(op);
      __builtin_amdgcn_s_barrier();
      __builtin_amdgcn_s_sleep(4);
      spin_until(tabs + ((TO + TY) >> 4));
      ST(11);
      float t;
#pragma unroll 1
      for (int it = 0; it < SPIN_CAP; ++it) {
        v4u y;
        ld1w(y, yo, ws);
        ST(12);
        t = sw16(sw32(untag<12>(y.x), untag<12>(y.y)), sw32(untag<12>(y.z), untag<12>(y.w)));
        t += dppf<XOR1>(t); t += dppf<XOR2>(t); t += dppf<ROR4>(t); t += dppf<ROR8>(t);
        if (!anybad<12>(neq(y, t12))) break;
        CNT(15);
      }
      asm volatile("s_mov_b64 exec, %3\n\tglobal_store_short %0, %1, %2\n\ts_mov_b64 exec, -1" ::"v"(op), "v"(pk_bf16(t, t)), "s"(out), "s"(0x0001000100010001ull) : "memory");
      if (c == 0) asm volatile("s_mov_b64 exec, 1\n\tglobal_store_dword %0, %1, %2 sc1\n\ts_mov_b64 exec, -1" ::"v"(0), "v"(t32), "s"(ws) : "memory");
      ST(13);
    }
  } else {
    const int w = wv;
    const unsigned fs = L_FS + 4 * w;
    int issued = 5, landed = 0, due = TK0 + w * (TAU / 4), seen = 0, dt = 0;
#define PUBLISH(vm_) do { if (issued - (vm_) != landed) { landed = issued - (vm_); lds_set(L, fs, landed); } } while (0)
#define PACE() do { \
      const int v0_ = vm_inflight(); PUBLISH(v0_); \
      if (seen < due || v0_ >= WIN) { \
        const int d_ = *(volatile LAS int*)(L + L_DT); \
        _Pragma("unroll 1") for (int it_ = 0; it_ < SPIN_CAP; ++it_) { \
          seen = now16(t0) - dt; \
          const int vm_ = vm_inflight(); PUBLISH(vm_); \
          if (seen >= due && vm_ < WIN) break; } \
        const int dn_ = __builtin_amdgcn_readfirstlane(d_); seen += dt - dn_; dt = dn_; } \
      due += TAU; ++issued; } while (0)
    unsigned v0 = w == 3 ? 0 : lane * 16 + (w == 1 ? 1024 : 0), vq = (lane >> 3) * 8192 + (lane & 7) * 16;
    unsigned l0 = lbase + (w < 2 ? L_X : L_P - 2048) + w * 1024, lq = lbase + L_WQ + w * 1024, oq = (128 * qp + 8 * w) * 8192 + qc * 128;
    PIN(v0); PIN(vq); PINS(l0); PINS(lq); PINS(oq);
    ARGS("\n\ts_memrealtime s[98:99] ; T0_LOAD");
    DMA_S("", v0, P64(w < 2 ? ka[0] : w == 2 ? ka[2] : ka[4], w < 2 ? ka[1] : w == 2 ? ka[3] : ka[5]), l0);
#pragma unroll
    for (int i = 0; i < 4; ++i) DMA_S(" nt", vq, wq + oq + i * (32 * 8192), lq + i * 4096);
    if (w == 0 && lane < 9) *(volatile LAS int*)(L + L_FS + 4 * lane) = lane < 8 ? -64 : 0;
    if (w == 1) *(volatile LAS v2u*)(L + L_Z + 8 * lane) = v2u{0, 0};
    T0();
    if (w == 0) { lds_set(L, L_T0, t0); LGKM0(); }
    ST(7);
    __builtin_amdgcn_s_barrier();
    if (has_f) {
      const unsigned voff = (lane >> 3) * 3584 + (lane & 7) * 16;
      const size_t off = (size_t)(64 * xc + 8 * w) * 3584 + j * 128;
#pragma unroll 1
      for (int i = 0; i < 4; ++i) { PACE(); DMA_S(" nt", voff, (const char*)(i < 2 ? wg : wu) + off + (i & 1) * (32 * 3584), lbase + L_GU + (4 * i + w) * 1024); }
    } else {
      due += 4 * TAU;
    }
    {
      const char* kb = (const char*)kc + kvoff + w * 1024;
      const unsigned voff = g * 256 + ((n ^ (4 * w + g)) * 16);
#pragma unroll 1
      for (int i = 0; i < 16; ++i) { PACE(); DMA_S(" nt", voff, kb + (((i * stp + rot) & 15) << 12), lbase + L_K + i * 4096 + w * 1024); }
    }
    ST(1);
    {
      const unsigned voff = g * 256 + ((n ^ (2 * (g | ((w >> 1) << 2)))) * 16);
      const char* vb = (const char*)vc + kvoff + w * 1024;
#pragma unroll 1
      for (int i = 0; i < 16; ++i) {
        if (i == 8) wait4<3, 0>(L, L_FH, 1);
        if (i >= 16 - NL) due += TAUL - TAU;
        PACE(); DMA_S(" nt", voff, vb + (((i * stp + rot) & 15) << 12), lbase + L_V + i * 4096 + w * 1024);
      }
    }
    ST(2);
    due += GAP;
    wait4<12, 0>(L, L_FH, 1);
    {
      const unsigned vo = g * 1024 + ((n ^ (2 * (g | ((w >> 1) << 2)))) * 16);
      const char* ob = (const char*)wo + (size_t)(64 * rg + 4 * w) * 1024 + cg * 256;
      const char* db = (const char*)wd + (size_t)(28 * rg) * 1024 + cg * 256;
#pragma unroll 1
      for (int i = 0; i < 6; ++i) {
        const char* src = i >= 2 ? ob + (i - 2) * 16384 : db + (i == 0 ? 4 * w : w < 3 ? 16 + 4 * w : 0) * 1024;
        due += TAU2 - TAU; PACE();
        DMA_S(" nt", vo, src, lbase + L_WO + (i >= 2 ? i - 2 : i + 4) * 4096 + w * 1024);
      }
    }
    ST(3);
#pragma unroll 1
    for (int it = 0; it < SPIN_CAP && landed < C_WD - sk; ++it) { const int vm = vm_inflight(); PUBLISH(vm); }
    ST(4);
    unsigned t12 = *(volatile LAS unsigned*)(L + L_TAG) & 0xFFFu;
    TR_ADDR(w);
    unsigned yoff = WS_Y + ((cg * 64 + rg) * 2 + (n & 1)) * 512 + w * 128 + ((n >> 1) & 1) * 64 + g * 16;
    unsigned oa = lbase + (n < 2 ? L_OB + n * 128 + g * 16 : L_Z), ob = lbase + ((n >> 1) == 1 ? L_OB + (n & 1) * 128 + g * 16 : L_Z);
    const float g1 = *(LAS float*)(L + L_GATE + (n & 1) * 8 + 4);
    wait4<12, 0>(L, L_FH, 5);
    const v4u ha = *(LAS v4u*)(L + (n < 2 ? L_HB + n * 64 + g * 16 : L_Z)), hb = *(LAS v4u*)(L + ((n >> 1) == 1 ? L_HB + (n & 1) * 64 + g * 16 : L_Z));
    wait4<15, 0>(L, L_FS, C_WD - sk);
    f32x4 y = mfma(tr2(L + L_WD + ta1, 1024), hb, mfma(tr2(L + L_WD + ta0, 1024), ha, f32x4{0, 0, 0, 0})) * g1;
    PIN(y);
    vmcnt<0>(); lds_set(L, fs, C_WO - sk);
    ST(5);
    wait4<15, 0>(L, L_FS, C_WO - sk);
    v4u wf[4];
    wf[0] = tr2(L + L_WO + ta0, 1024); wf[1] = tr2(L + L_WO + ta1, 1024); wf[2] = tr2(L + L_WO + ta0 + 8192, 1024); wf[3] = tr2(L + L_WO + ta1 + 8192, 1024);
    PIN(wf[0]); PIN(wf[1]); PIN(wf[2]); PIN(wf[3]); PIN(oa); PIN(ob); PIN(yoff); PIN(t12);
    __builtin_amdgcn_s_setprio(3);
    ST(6);
    __builtin_amdgcn_s_barrier();
    asm volatile("" ::: "memory");
    ST(10);
    {
      const v4u a0 = *(LAS v4u*)(uintptr_t)oa, b0 = *(LAS v4u*)(uintptr_t)ob, a1 = *(LAS v4u*)(uintptr_t)(oa + 64), b1 = *(LAS v4u*)(uintptr_t)(ob + 64);
      SB();
      y = mfma(wf[0], a0, y); y = mfma(wf[1], b0, y); y = mfma(wf[2], a1, y); y = mfma(wf[3], b1, y);
      st_sc1<0x000F000F000F000Full>(yoff, v4u{tagt<12>(y[0], t12), tagt<12>(y[1], t12), tagt<12>(y[2], t12), tagt<12>(y[3], t12)}, ws);
    }
    ST(11);
  }
}

extern "C" void kernel_launch(void* const* d_in, const int* in_sizes, int n_in, void* d_out, int out_size, void* d_ws, size_t ws_size, hipStream_t stream) {
  (void)hipFuncSetAttribute((const void*)fused_decode, hipFuncAttributeMaxDynamicSharedMemorySize, L_END);
  fused_decode<<<256, 512, L_END, stream>>>((const u16*)d_in[0], (const u16*)d_in[9], (char*)d_ws, (const u16*)d_in[3], (const u16*)d_in[6], (const u16*)d_in[7],
                                            (const u16*)d_in[1], (const u16*)d_in[2], (const u16*)d_in[4], (const u16*)d_in[8], (u16*)d_out);
}
```

```cpp
#define KMODE 0
#define PROBE 15
#define PMODE 1
#define AMP 8u
#define PWAVE 7
#define SMASK 1
#define TO 3000
#define TY 1750
#include <hip/hip_runtime.h>
#include <hip/hip_bf16.h>
#include <stdint.h>

#define STAMP 0
#define TAU 256
#define TK0 440
#define TAUL TAU
#define NL 0
#define TAU2 256
#define TT 2900
#define TQ 4300
#define TVE (TK0 + (35 - NL) * TAU + NL * TAUL)
#define WIN 12
#define CODE_PF 25

typedef unsigned short u16;
typedef unsigned long long u64;
typedef unsigned v4u __attribute__((ext_vector_type(4)));
typedef int v4i __attribute__((ext_vector_type(4)));
typedef unsigned v2u __attribute__((ext_vector_type(2)));
typedef short v4s __attribute__((ext_vector_type(4)));
typedef float f32x4 __attribute__((ext_vector_type(4)));
typedef float f32x2 __attribute__((ext_vector_type(2)));
typedef __bf16 v8bf __attribute__((ext_vector_type(8)));
typedef __bf16 v2bf __attribute__((ext_vector_type(2)));
#define GAS __attribute__((address_space(1)))
#define LAS __attribute__((address_space(3)))
#define SB() __builtin_amdgcn_sched_barrier(0)

constexpr unsigned WS_T = 1024;
constexpr unsigned WS_Q = 4096;
constexpr unsigned WS_H = WS_Q + 131072;
constexpr unsigned WS_ST = WS_H + 229376;
constexpr unsigned WS_O = WS_ST + 16384;
constexpr unsigned WS_Y = WS_O + 524288;
constexpr unsigned WS_STAMP = 4u << 20;
constexpr unsigned L_K = 0, L_V = 65536, L_WQ = L_V + 8 * 4096, L_WO = 131072, L_GU = L_WO, L_WD = L_WO + 16384, L_X = L_WD + 8192,
                   L_Q = L_X + 2048, QSTR = 320, L_P = L_Q + 4 * QSTR, PSTR = 576, L_OB = L_P + 4 * PSTR, L_HB = L_OB + 256, L_MX = L_HB + 128,
                   L_SUM = L_MX + 64, L_GATE = L_SUM + 64, L_FS = L_GATE + 16, L_FH = L_FS + 16, L_DT = L_FH + 16, L_TAG = L_DT + 4,
                   L_Z = L_DT + 16, L_END = L_Z + 512;
static_assert(L_END <= 163840, "LDS");
constexpr int SPIN_CAP = 1 << 17;

__device__ __forceinline__ float asf(unsigned u) { return __builtin_bit_cast(float, u); }
__device__ __forceinline__ unsigned asu(float f) { return __builtin_bit_cast(unsigned, f); }
__device__ __forceinline__ unsigned pk_bf16(float lo, float hi) { f32x2 x = {lo, hi}; return __builtin_bit_cast(unsigned, __builtin_convertvector(x, v2bf)); }
__device__ __forceinline__ float rbf(float f) { return asf(pk_bf16(f, f) << 16); }
template <int CTRL> __device__ __forceinline__ float dppf(float x) { return asf(__builtin_amdgcn_mov_dpp(asu(x), CTRL, 0xf, 0xf, true)); }
constexpr int XOR1 = 0xB1, XOR2 = 0x4E, HMIR = 0x141, ROR4 = 0x124, ROR8 = 0x128;
__device__ __forceinline__ float sw16(float a, float b) { auto s = __builtin_amdgcn_permlane16_swap(asu(a), asu(b), false, false); return asf(s[0]) + asf(s[1]); }
__device__ __forceinline__ float sw32(float a, float b) { auto s = __builtin_amdgcn_permlane32_swap(asu(a), asu(b), false, false); return asf(s[0]) + asf(s[1]); }
__device__ __forceinline__ float x16max(float x) { auto s = __builtin_amdgcn_permlane16_swap(asu(x), asu(x), false, false); return fmaxf(asf(s[0]), asf(s[1])); }
__device__ __forceinline__ float x32max(float x) { auto s = __builtin_amdgcn_permlane32_swap(asu(x), asu(x), false, false); return fmaxf(asf(s[0]), asf(s[1])); }
__device__ __forceinline__ float frcp(float x) { return __builtin_amdgcn_rcpf(x); }
__device__ __forceinline__ float fexp(float x) { return __builtin_amdgcn_exp2f(x * 1.4426950408889634f); }
__device__ __forceinline__ f32x4 mfma(v4u a, v4u b, f32x4 c) { return __builtin_amdgcn_mfma_f32_16x16x32_bf16(__builtin_bit_cast(v8bf, a), __builtin_bit_cast(v8bf, b), c, 0, 0, 0); }
__device__ __forceinline__ v2u trread(LAS char* p) { return __builtin_bit_cast(v2u, __builtin_amdgcn_ds_read_tr16_b64_v4i16((LAS v4s*)p)); }
__device__ __forceinline__ v4u tr2(LAS char* p, int d) { v2u a = trread(p), b = trread(p + d); return v4u{a.x, a.y, b.x, b.y}; }
template <int B> __device__ __forceinline__ unsigned tagt(float v, unsigned t) { return (asu(v) & ~((1u << B) - 1)) | t; }
template <int B> __device__ __forceinline__ unsigned tagf(float v, unsigned t) { return ((asu(v) + (1u << (B - 1))) & ~((1u << B) - 1)) | t; }
template <int B> __device__ __forceinline__ float untag(unsigned w) { return asf(w & ~((1u << B) - 1)); }
__device__ __forceinline__ unsigned neq(v4u g, unsigned t) { return __builtin_amdgcn_bitop3_b32(g.x, g.y, t, 0x7E) | __builtin_amdgcn_bitop3_b32(g.z, g.w, t, 0x7E); }
template <int B> __device__ __forceinline__ bool anybad(unsigned x) { return __builtin_amdgcn_ballot_w64((x & ((1u << B) - 1)) != 0) != 0; }
#define PIN(x) asm volatile("" : "+v"(x))
#define CNT(i)
#define CNT0(i)

#define DMA_S(MOD, voff, sbase, ldst) asm volatile("s_mov_b32 m0, %2\n\ts_nop 0\n\tglobal_load_lds_dwordx4 %0, %1" MOD :: "v"(voff), "s"(sbase), "s"(ldst) : "memory")
template <int N> __device__ __forceinline__ void vmcnt() { asm volatile("s_waitcnt vmcnt(%0)" ::"i"(N) : "memory"); }
#define LGKM0() asm volatile("s_waitcnt lgkmcnt(0)" ::: "memory")
__device__ __forceinline__ void ld1w(v4u& a, unsigned o0, const char* sb) {
  asm volatile("global_load_dwordx4 %0, %1, %2 sc1\n\ts_waitcnt vmcnt(0)" : "=&v"(a) : "v"(o0), "s"(sb) : "memory"); }
__device__ __forceinline__ void ld2w(v4u& a, v4u& b, unsigned o0, unsigned o1, const char* sb) {
  asm volatile("global_load_dwordx4 %0, %2, %4 sc1\n\tglobal_load_dwordx4 %1, %3, %4 sc1\n\ts_waitcnt vmcnt(0)" : "=&v"(a), "=&v"(b) : "v"(o0), "v"(o1), "s"(sb) : "memory"); }
__device__ __forceinline__ void ld4w(v4u& a, v4u& b, v4u& c, v4u& d, unsigned o0, const char* sb) {
  asm volatile("global_load_dwordx4 %0, %4, %5 sc1\n\tglobal_load_dwordx4 %1, %4, %5 offset:1024 sc1\n\tglobal_load_dwordx4 %2, %4, %5 offset:2048 sc1\n\t"
               "global_load_dwordx4 %3, %4, %5 offset:3072 sc1\n\ts_waitcnt vmcnt(0)" : "=&v"(a), "=&v"(b), "=&v"(c), "=&v"(d) : "v"(o0), "s"(sb) : "memory"); }
template <u64 MASK> __device__ __forceinline__ void st_sc1(unsigned voff, v4u d, char* sbase) {
  asm volatile("s_mov_b64 exec, %3\n\tglobal_store_dwordx4 %0, %1, %2 sc1\n\ts_mov_b64 exec, -1\n\ts_nop 0" ::"v"(voff), "v"(d), "s"(sbase), "s"(MASK) : "memory"); }

#define ST(i) do { if ((SMASK >> (i)) & 1) { SB(); stamp[i] = (unsigned)__builtin_amdgcn_s_memrealtime(); SB(); } } while (0)

__device__ __forceinline__ int now16(unsigned t0) { return (int)(((unsigned)__builtin_amdgcn_s_memrealtime() - t0) * 16u); }
__device__ __forceinline__ int vm_inflight() { unsigned r; asm volatile("s_getreg_b32 %0, hwreg(HW_REG_IB_STS, 0, 4)" : "=s"(r)::"memory"); return (int)r; }
__device__ __forceinline__ int clock_offset(LAS char* L) { return __builtin_amdgcn_readfirstlane(*(volatile LAS int*)(L + L_DT)); }
__device__ __forceinline__ void wait_until(LAS char* L, unsigned t0, int t) {
#pragma unroll 1
  for (int it = 0; it < SPIN_CAP && now16(t0) - clock_offset(L) < t; ++it) __builtin_amdgcn_s_sleep(1);
}
__device__ __forceinline__ void spin_until(unsigned tick) {
#pragma unroll 1
  for (int it = 0; it < SPIN_CAP && (int)((unsigned)__builtin_amdgcn_s_memrealtime() - tick) < 0; ++it) {}
}
__device__ __forceinline__ void lds_set(LAS char* L, unsigned off, unsigned v) {
  asm volatile("s_mov_b64 exec, 1\n\tds_write_b32 %0, %1\n\ts_mov_b64 exec, -1" ::"v"((unsigned)(uintptr_t)(L + off)), "v"(v) : "memory");
}
template <int MASK, int SLEEP> __device__ __forceinline__ void wait4(LAS char* L, unsigned off, int n) {
#pragma unroll 1
  for (int it = 0; it < SPIN_CAP; ++it) {
    const v4i f = *(volatile LAS v4i*)(L + off);
    int m = 0x7fffffff;
    if (MASK & 1) m = min(m, f.x);
    if (MASK & 2) m = min(m, f.y);
    if (MASK & 4) m = min(m, f.z);
    if (MASK & 8) m = min(m, f.w);
    if (__builtin_amdgcn_readfirstlane(m) >= n) break;
    if (SLEEP) __builtin_amdgcn_s_sleep(SLEEP);
  }
  asm volatile("" ::: "memory");
}
#define TR_ADDR(w_) const int fr = (n >> 2) | ((g & 1) << 2); \
    unsigned ta0 = (8 * g + (n >> 2)) * 256 + ((((2 * (w_)) ^ fr) * 2 + ((n & 3) >> 1)) * 16) + (n & 1) * 8; \
    unsigned ta1 = (8 * g + (n >> 2)) * 256 + ((((2 * (w_) + 1) ^ fr) * 2 + ((n & 3) >> 1)) * 16) + (n & 1) * 8; PIN(ta0); PIN(ta1)

constexpr int C_V = 41, C_WD = 43, C_WO = 47;

__global__ void __launch_bounds__(512) __attribute__((amdgpu_waves_per_eu(2, 2)))
fused_decode(const u16* a0_, const u16* a1_, char* a2_, const u16* a3_, const u16* a4_, const u16* a5_, const u16* a6_, const u16* a7_, const u16* a8_, const u16* a9_, u16* a10_) {
  u64 ta_;
  typedef const __attribute__((address_space(4))) u64* kptr;
  kptr ka = (kptr)__builtin_amdgcn_kernarg_segment_ptr();
  u64 tb_ = 0;
  asm volatile("s_memrealtime %0\n\ts_waitcnt lgkmcnt(0)" : "=s"(ta_), "+s"(ka));
  const u16* x = (const u16*)ka[0]; const u16* gw = (const u16*)ka[1]; char* ws = (char*)ka[2]; const u16* wq = (const u16*)ka[3]; const u16* wg = (const u16*)ka[4];
  const u16* wu = (const u16*)ka[5]; const u16* kc = (const u16*)ka[6]; const u16* vc = (const u16*)ka[7]; const u16* wo = (const u16*)ka[8]; const u16* wd = (const u16*)ka[9]; u16* out = (u16*)ka[10];
  extern __shared__ __attribute__((aligned(16))) char smem[];
  LAS char* L = (LAS char*)smem;
  const unsigned lbase = (unsigned)(uintptr_t)L;
  const int c = blockIdx.x, lane = threadIdx.x & 63, wv = __builtin_amdgcn_readfirstlane(threadIdx.x >> 6);
  const int xc = c & 7, j = c >> 3;
  const int kvh = xc, b = j & 1, sp = j >> 1;
  const int rg = 8 * xc + (j >> 2), cg = j & 3;
  const int qp = xc >> 1, qc = 32 * (xc & 1) + j;
  const bool has_f = j < 28;
  const int sk = has_f ? 0 : 4;
  const int g = lane >> 4, n = lane & 15;
  const int rot = (j >> 2) * 2 + (j & 1);
  const size_t kvoff = ((size_t)(b * 8 + kvh) * 16384 + 12288 + sp * 256) * 256;
  unsigned stamp[16];
  for (int i = 0; i < 16; ++i) stamp[i] = 0;
  asm volatile("" ::"s"(x), "s"(kc), "s"(vc), "s"(wq), "s"(wo), "s"(wg), "s"(wu), "s"(wd), "s"(gw), "s"(out), "s"(ws));
  const unsigned t0 = (unsigned)__builtin_amdgcn_s_memrealtime();
  stamp[0] = (unsigned)ta_; stamp[15] = KMODE ? (unsigned)tb_ : t0;
  if (wv == 4) {
    const char* pc;
    asm volatile("s_getpc_b64 %0" : "=s"(pc));
    asm volatile("s_mov_b32 m0, %2\n\ts_nop 0\n\tglobal_load_lds_dword %0, %1" ::"v"(min(j, CODE_PF) * 512 + (lane & 7) * 64), "s"((const char*)((uintptr_t)pc & ~(uintptr_t)255)), "s"(lbase + L_OB) : "memory");
  }

  if (wv >= 4) {
    const int hw = wv - 4;
    const unsigned fh = L_FH + 4 * hw;
    __builtin_amdgcn_s_setprio(2);
    if (hw == 1) {
      const char* a = lane == 0 ? (const char*)vc + kvoff : lane == 1 ? (const char*)wo + (size_t)rg * 65536 : lane == 2 ? (const char*)wd + (size_t)rg * 28672
                    : lane == 3 ? (const char*)out : (const char*)kc + kvoff;
      asm volatile("s_mov_b32 m0, %1\n\ts_nop 0\n\tglobal_load_lds_dword %0, off" ::"v"(a), "s"(lbase + L_OB) : "memory");
    }
    __builtin_amdgcn_s_barrier();
    wait4<15, 0>(L, L_FS, 1);
    const unsigned t32 = __builtin_amdgcn_readfirstlane(*(LAS unsigned*)(L + L_P + 1024)) + 1, t12 = t32 & 0xFFFu, t8 = t32 & 0xFFu;
    ST(1);
    if (hw < 2) {
      v4u xq[4];
#pragma unroll
      for (int s = 0; s < 4; ++s) xq[s] = *(LAS v4u*)(L + L_X + (n & 1) * 1024 + qp * 256 + s * 64 + g * 16);
      const int col = 64 * qc + 32 * hw + 16 * ((n >> 1) & 1) + 4 * g;
      const unsigned qoff = WS_Q + (((((col >> 9) * 2 + (n & 1)) * 4 + ((col >> 7) & 3)) * 4 + qp) * 128 + (col & 127)) * 4;
      LAS char* a = L + L_WQ + (8 * g + (n >> 2)) * 128 + hw * 64 + (n & 3) * 8;
      wait4<15, 0>(L, L_FS, 5);
      ST(2);
      v4u af[8];
#pragma unroll
      for (int s = 0; s < 4; ++s) { af[2 * s] = tr2(a + s * 4096, 512); af[2 * s + 1] = tr2(a + s * 4096 + 32, 512); }
      SB();
      f32x4 d0 = {0, 0, 0, 0}, d1 = {0, 0, 0, 0};
#pragma unroll
      for (int s = 0; s < 4; ++s) { d0 = mfma(af[2 * s], xq[s], d0); d1 = mfma(af[2 * s + 1], xq[s], d1); }
      v4u gr;
#pragma unroll
      for (int r = 0; r < 4; ++r) gr[r] = tagf<8>((n & 2) ? d1[r] : d0[r], t8);
      st_sc1<0x000F000F000F000Full>(qoff, gr, ws);
    } else {
      if (hw == 2 && c < 8) st_sc1<1>(WS_T + c * 128, v4u{t0, t32, t0, t32}, ws);
      {
        const v4u gwv = *(LAS v4u*)(L + L_P + lane * 16), xv = *(LAS v4u*)(L + L_X + (hw & 1) * 1024 + lane * 16);
        float s = 0.f;
#pragma unroll
        for (int k = 0; k < 4; ++k) {
          const unsigned pr = pk_bf16(asf(xv[k] << 16) * asf(gwv[k] << 16), asf(xv[k] & 0xFFFF0000u) * asf(gwv[k] & 0xFFFF0000u));
          s += asf(pr << 16) + asf(pr & 0xFFFF0000u);
        }
        s += dppf<XOR1>(s); s += dppf<XOR2>(s); s += dppf<ROR4>(s); s += dppf<ROR8>(s); s = sw16(s, s); s = sw32(s, s);
        const float gs = rbf(frcp(1.f + fexp(-rbf(s))));
        if (lane == 0) *(LAS f32x2*)(L + L_GATE + (hw & 1) * 8) = f32x2{gs, rbf(1.f - gs)};
        lds_set(L, L_TAG, t32);
      }
      v4u xg[2];
#pragma unroll
      for (int s = 0; s < 2; ++s) xg[s] = *(LAS v4u*)(L + L_X + (n & 1) * 1024 + xc * 128 + s * 64 + g * 16);
      const unsigned hoff = WS_H + ((((n & 1) * 2 + (hw & 1)) * 8 + xc) * 448 + 16 * j + 4 * ((n >> 1) & 3) + g) * 16;
      LAS char* a = L + L_GU + (hw & 1) * 8192 + (8 * g + (n >> 2)) * 128 + (n & 3) * 8;
      wait4<15, 0>(L, L_FS, 9 - sk);
      ST(2);
      v4u af[8];
#pragma unroll
      for (int s = 0; s < 2; ++s)
#pragma unroll
        for (int t = 0; t < 4; ++t) af[4 * s + t] = tr2(a + s * 4096 + t * 32, 512);
      SB();
      f32x4 d[4];
#pragma unroll
      for (int t = 0; t < 4; ++t) d[t] = mfma(af[4 + t], xg[1], mfma(af[t], xg[0], f32x4{0, 0, 0, 0}));
      v4u gr;
#pragma unroll
      for (int r = 0; r < 4; ++r) { const float lo = (n & 2) ? d[1][r] : d[0][r], hi = (n & 2) ? d[3][r] : d[2][r]; gr[r] = tagf<12>((n & 4) ? hi : lo, t12); }
      PIN(gr);
      if (has_f) st_sc1<0x00FF00FF00FF00FFull>(hoff, gr, ws);
    }
    lds_set(L, fh, 1);
    ST(3);
    if (hw == 1) {
      wait_until(L, t0, TT);
      v4u tv;
      ld1w(tv, WS_T + (lane & 7) * 128, ws);
      const bool ok = tv.y == t32 && tv.w == t32;
      float d = ok ? (float)(int)(tv.x - t0) : 0.f, k = ok ? 1.f : 0.f;
      d += dppf<XOR1>(d); k += dppf<XOR1>(k); d += dppf<XOR2>(d); k += dppf<XOR2>(k); d += dppf<HMIR>(d); k += dppf<HMIR>(k);
      lds_set(L, L_DT, (int)fminf(fmaxf(d * 16.f * frcp(fmaxf(k, 1.f)), -1600.f), 1600.f));
    }
    {
      const unsigned qo = WS_Q + ((kvh * 2 + b) * 4 + hw) * 2048 + lane * 16;
      wait_until(L, t0, TQ);
      v4u q0, q1;
      CNT0(14); CNT0(15);
#pragma unroll 1
      for (int it = 0; it < SPIN_CAP; ++it) { ld2w(q0, q1, qo, qo + 1024, ws); if (!anybad<8>(neq(q0, t8) | neq(q1, t8))) break; CNT(14); }
      float v[4];
#pragma unroll
      for (int r = 0; r < 4; ++r) { v[r] = untag<8>(q0[r]) + untag<8>(q1[r]); v[r] = sw32(v[r], v[r]); }
      if (lane < 32) *(LAS v2u*)(L + L_Q + hw * QSTR + lane * 8) = v2u{pk_bf16(v[0], v[1]), pk_bf16(v[2], v[3])};
      LGKM0(); lds_set(L, fh, 2);
      ST(4);
    }
    wait4<15, 1>(L, L_FH, 2);
    v4u qf[4], kf[16];
#pragma unroll
    for (int s = 0; s < 4; ++s) qf[s] = *(LAS v4u*)(L + L_Q + (n & 3) * QSTR + s * 64 + g * 16);
    wait4<15, 0>(L, L_FS, 13 + 4 * hw - sk);
    ST(6);
#pragma unroll
    for (int s = 0; s < 4; ++s)
#pragma unroll
      for (int t = 0; t < 4; ++t) kf[s * 4 + t] = *(LAS v4u*)(L + L_K + (4 * hw + t) * 4096 + n * 256 + (((4 * s + g) ^ n) * 16));
    SB();
    float sc[4];
    {
      f32x4 d[4];
#pragma unroll
      for (int t = 0; t < 4; ++t) d[t] = f32x4{0, 0, 0, 0};
#pragma unroll
      for (int s = 0; s < 4; ++s)
#pragma unroll
        for (int t = 0; t < 4; ++t) d[t] = mfma(kf[s * 4 + t], qf[s], d[t]);
      constexpr float SC = 0.08838834764831845f;
      float v[4];
#pragma unroll
      for (int r = 0; r < 4; ++r) { const float lo = (n & 4) ? d[1][r] : d[0][r], hi = (n & 4) ? d[3][r] : d[2][r]; v[r] = (n & 8) ? hi : lo; }
      const unsigned a = pk_bf16(v[0], v[1]), bq = pk_bf16(v[2], v[3]);
      const unsigned a2 = pk_bf16(asf(a << 16) * SC, asf(a & 0xFFFF0000u) * SC), b2 = pk_bf16(asf(bq << 16) * SC, asf(bq & 0xFFFF0000u) * SC);
      sc[0] = asf(a2 << 16); sc[1] = asf(a2 & 0xFFFF0000u); sc[2] = asf(b2 << 16); sc[3] = asf(b2 & 0xFFFF0000u);
    }
    float m = fmaxf(fmaxf(sc[0], sc[1]), fmaxf(sc[2], sc[3]));
    m = fmaxf(m, dppf<ROR4>(m)); m = fmaxf(m, dppf<ROR8>(m)); m = x16max(m); m = x32max(m);
    if (lane < 4) *(LAS float*)(L + L_MX + lane * 16 + hw * 4) = m;
    LGKM0(); lds_set(L, fh, 3);
    wait4<15, 0>(L, L_FH, 3);
    const int e = lane & 7;
    const unsigned ho = WS_H + (((hw & 1) * 16 + (lane >> 3)) * 448 + 7 * rg + (e < 7 ? e : 6)) * 16;
    if (hw >= 2) { DMA_S(" sc1", ho, ws, lbase + L_K + hw * 2048); DMA_S(" sc1", ho + 8 * 448 * 16, ws, lbase + L_K + hw * 2048 + 1024); }
    { const f32x4 mm = *(LAS f32x4*)(L + L_MX + (n & 3) * 16); m = fmaxf(fmaxf(mm[0], mm[1]), fmaxf(mm[2], mm[3])); }
    {
      float p[4];
#pragma unroll
      for (int r = 0; r < 4; ++r) p[r] = fexp(sc[r] - m);
      *(LAS v2u*)(L + L_P + (n & 3) * PSTR + (64 * hw + 16 * (n >> 2) + 4 * g) * 2) = v2u{pk_bf16(p[0], p[1]), pk_bf16(p[2], p[3])};
      float l = (p[0] + p[1]) + (p[2] + p[3]);
      l += dppf<ROR4>(l); l += dppf<ROR8>(l); l = sw16(l, l); l = sw32(l, l);
      if (lane < 4) *(LAS float*)(L + L_SUM + lane * 16 + hw * 4) = l;
    }
    LGKM0(); lds_set(L, fh, 4);
    ST(7);
    TR_ADDR(hw);
    unsigned ooff = WS_O + (((2 * (4 * kvh + (n & 3)) + (hw >> 1)) * 2 + b) * 16 + sp) * 256 + (hw & 1) * 128 + ((n >> 2) & 1) * 64 + g * 16;
    unsigned tv = t12;
    PIN(ooff); PIN(tv);
    const int tabs = t0 + ((clock_offset(L) + TVE) >> 4);
    wait4<15, 0>(L, L_FH, 4);
    v4u pa[8], pb[8];
    {
      const unsigned pp = L_P + (n & 3) * PSTR + g * 16, aa = n < 4 ? pp : L_Z, ab = (n >> 2) == 1 ? pp : L_Z;
#pragma unroll
      for (int u = 0; u < 8; ++u) { pa[u] = *(LAS v4u*)(L + aa + u * 64); pb[u] = *(LAS v4u*)(L + ab + u * 64); }
    }
    if (hw == 2) {
      const f32x4 ls = *(LAS f32x4*)(L + L_SUM + (n & 3) * 16);
      st_sc1<0xF>(WS_ST + (((kvh * 4 + (n & 3)) * 2 + b) * 16 + sp) * 16, v4u{tagt<12>(m, t12), tagf<12>((ls[0] + ls[1]) + (ls[2] + ls[3]), t12), t12, t12}, ws);
    }
    f32x4 o = {0, 0, 0, 0};
#define PVN(u0, nu) do { v4u va[nu], vb[nu]; _Pragma("unroll") for (int u = 0; u < nu; ++u) { va[u] = tr2(L + L_V + ta0 + (u0 + u) * 8192, 1024); vb[u] = tr2(L + L_V + ta1 + (u0 + u) * 8192, 1024); } SB(); \
      _Pragma("unroll") for (int u = 0; u < nu; ++u) { o = mfma(va[u], pa[u0 + u], o); o = mfma(vb[u], pb[u0 + u], o); } } while (0)
    wait4<15, 1>(L, L_FS, 33 - sk);
    PVN(0, 4); PIN(o);
    ST(8);
    if (hw >= 2) {
      if (hw == 2) vmcnt<1>(); else vmcnt<0>();
      v4u h0 = *(LAS v4u*)(L + L_K + hw * 2048 + lane * 16), h1 = *(LAS v4u*)(L + L_K + hw * 2048 + 1024 + lane * 16);
#pragma unroll 1
      for (int it = 0; it < SPIN_CAP && anybad<12>(neq(h0, t12) | neq(h1, t12)); ++it) { ld2w(h0, h1, ho, ho + 8 * 448 * 16, ws); CNT(14); }
      float v[4];
#pragma unroll
      for (int r = 0; r < 4; ++r) v[r] = sw32(untag<12>(h0[r]), untag<12>(h1[r]));
      const float a0 = sw16(v[0], v[1]), a1 = sw16(v[2], v[3]);
      const float b0 = a0 + dppf<ROR8>(a0), b1 = a1 + dppf<ROR8>(a1);
      const float gg = rbf((lane & 8) ? b1 : b0);
      const auto sw = __builtin_amdgcn_permlane32_swap(asu(gg), asu(gg), false, false);
      const float hv = rbf(gg * frcp(1.f + fexp(-gg))) * asf(sw[1]);
      if (lane < 32) *(LAS u16*)(L + L_HB + (hw & 1) * 64 + (4 * e + ((lane >> 2) & 2) + (lane >> 4)) * 2) = e < 7 ? (u16)(pk_bf16(hv, hv) & 0xFFFFu) : (u16)0;
      LGKM0(); lds_set(L, fh, 5);
      ST(5);
    }
    wait4<15, 0>(L, L_FS, 39 - sk);
    PVN(4, 3); PIN(o);
    const unsigned so = WS_ST + (((rg >> 1) * 2 + (hw & 1)) * 16 + 4 * (n & 3) + g) * 16;
    wait4<15, 0>(L, L_FS, C_V - sk);
    ST(9);
    PVN(7, 1);
    st_sc1<0x00FF00FF00FF00FFull>(ooff, v4u{tagt<12>(o[0], tv), tagt<12>(o[1], tv), tagt<12>(o[2], tv), tagt<12>(o[3], tv)}, ws);
    ST(10);
    if (hw < 2) {
      unsigned oo = WS_O + (rg * 2 + hw) * 4096 + lane * 16, oba = L_OB + hw * 128 + (4 * n + (g & 1) * 2 + (g >> 1)) * 2;
      DMA_S(" sc1", so, ws, lbase + L_K + hw * 1024);
      const float gate = *(LAS float*)(L + L_GATE + hw * 8);
      PIN(oo); PIN(oba);
      spin_until(tabs + (TO >> 4));
      ST(11);
      v4u a0, a1, a2, a3;
      asm volatile("global_load_dwordx4 %0, %4, %5 sc1\n\tglobal_load_dwordx4 %1, %4, %5 offset:1024 sc1\n\tglobal_load_dwordx4 %2, %4, %5 offset:2048 sc1\n\t"
                   "global_load_dwordx4 %3, %4, %5 offset:3072 sc1\n\ts_waitcnt vmcnt(4)" : "=&v"(a0), "=&v"(a1), "=&v"(a2), "=&v"(a3) : "v"(oo), "s"(ws) : "memory");
      v4u st = *(LAS v4u*)(L + L_K + hw * 1024 + lane * 16);
      if (anybad<12>(neq(st, t12))) {
#pragma unroll 1
        for (int it = 0; it < SPIN_CAP; ++it) { CNT(14); ld1w(st, so, ws); if (!anybad<12>(neq(st, t12))) break; }
      }
      const float ms = untag<12>(st.x), ls = untag<12>(st.y);
      float M = ms; M = fmaxf(M, dppf<XOR1>(M)); M = fmaxf(M, dppf<XOR2>(M)); M = x16max(M); M = x32max(M);
      const float ex = fexp(ms - M);
      float Ls = ex * ls; Ls += dppf<XOR1>(Ls); Ls += dppf<XOR2>(Ls); Ls = sw16(Ls, Ls); Ls = sw32(Ls, Ls);
      const float wv_ = gate * ex * frcp(Ls);
      const float wt0 = dppf<0x00>(wv_), wt1 = dppf<0x55>(wv_), wt2 = dppf<0xAA>(wv_), wt3 = dppf<0xFF>(wv_);
      f32x2 w0 = {wt0, wt0}, w1 = {wt1, wt1}, w2 = {wt2, wt2}, w3 = {wt3, wt3};
      PIN(w0); PIN(w1); PIN(w2); PIN(w3);
      asm volatile("s_waitcnt vmcnt(0)" : "+v"(a0), "+v"(a1), "+v"(a2), "+v"(a3)::"memory");
      ST(12);
#pragma unroll 1
      for (int it = 0; it < SPIN_CAP; ++it) {
#define UT2(a, i) f32x2{untag<12>(a[i]), untag<12>(a[i + 1])}
        const f32x2 lo = __builtin_elementwise_fma(w0, UT2(a0, 0), w1 * UT2(a1, 0)) + __builtin_elementwise_fma(w2, UT2(a2, 0), w3 * UT2(a3, 0));
        const f32x2 hi = __builtin_elementwise_fma(w0, UT2(a0, 2), w1 * UT2(a1, 2)) + __builtin_elementwise_fma(w2, UT2(a2, 2), w3 * UT2(a3, 2));
        const float t = sw16(sw32(lo[0], lo[1]), sw32(hi[0], hi[1]));
        *(volatile LAS u16*)(L + oba) = (u16)(pk_bf16(t, t) & 0xFFFFu);
        SB();
        if (!anybad<12>(neq(a0, t12) | neq(a1, t12) | neq(a2, t12) | neq(a3, t12))) break;
        CNT(15);
        ld4w(a0, a1, a2, a3, oo, ws);
      }
      LGKM0();
      ST(13);
      __builtin_amdgcn_s_barrier();
    } else if (hw == 2) {
      __builtin_amdgcn_s_barrier();
    } else {
      unsigned yo = WS_Y + (((xc >> 1) * 64 + lane) * 2 + (xc & 1)) * 512 + j * 16;
      unsigned op = ((xc & 1) * 512 + (xc >> 1) * 128 + 4 * j + (g & 1) * 2 + (g >> 1)) * 2;
      PIN(yo); PIN(op);
      __builtin_amdgcn_s_barrier();
      __builtin_amdgcn_s_sleep(4);
      spin_until(tabs + ((TO + TY) >> 4));
      ST(11);
      float t;
#pragma unroll 1
      for (int it = 0; it < SPIN_CAP; ++it) {
        v4u y;
        ld1w(y, yo, ws);
        ST(12);
        t = sw16(sw32(untag<12>(y.x), untag<12>(y.y)), sw32(untag<12>(y.z), untag<12>(y.w)));
        t += dppf<XOR1>(t); t += dppf<XOR2>(t); t += dppf<ROR4>(t); t += dppf<ROR8>(t);
        if (!anybad<12>(neq(y, t12))) break;
        CNT(15);
      }
      asm volatile("s_mov_b64 exec, %3\n\tglobal_store_short %0, %1, %2\n\ts_mov_b64 exec, -1" ::"v"(op), "v"(pk_bf16(t, t)), "s"(out), "s"(0x0001000100010001ull) : "memory");
      if (c == 0) asm volatile("s_mov_b64 exec, 1\n\tglobal_store_dword %0, %1, %2 sc1\n\ts_mov_b64 exec, -1" ::"v"(0), "v"(t32), "s"(ws) : "memory");
      ST(13);
    }
  } else {
    const int w = wv;
    const unsigned fs = L_FS + 4 * w;
    int issued = 5, landed = 0, due = TK0 + w * (TAU / 4), seen = 0, dt = 0;
#define PUBLISH(vm_) do { if (issued - (vm_) != landed) { landed = issued - (vm_); lds_set(L, fs, landed); } } while (0)
#define PACE() do { \
      const int v0_ = vm_inflight(); PUBLISH(v0_); \
      if (seen < due || v0_ >= WIN) { \
        const int d_ = *(volatile LAS int*)(L + L_DT); \
        _Pragma("unroll 1") for (int it_ = 0; it_ < SPIN_CAP; ++it_) { \
          seen = now16(t0) - dt; \
          const int vm_ = vm_inflight(); PUBLISH(vm_); \
          if (seen >= due && vm_ < WIN) break; } \
        const int dn_ = __builtin_amdgcn_readfirstlane(d_); seen += dt - dn_; dt = dn_; } \
      due += TAU; ++issued; } while (0)
    {
      const char* sb = w == 0 ? (const char*)x : w == 1 ? (const char*)x + 1024 : w == 2 ? (const char*)gw : (const char*)ws;
      DMA_S("", w == 3 ? 0 : lane * 16, sb, lbase + (w < 2 ? L_X : L_P - 2048) + w * 1024);
    }
    {
      const char* base = (const char*)wq + (size_t)(128 * qp + 8 * w) * 8192 + qc * 128;
      const unsigned voff = (lane >> 3) * 8192 + (lane & 7) * 16;
#pragma unroll
      for (int i = 0; i < 4; ++i) DMA_S(" nt", voff, base + (size_t)i * 32 * 8192, lbase + L_WQ + (4 * i + w) * 1024);
    }
    if (w == 0 && lane < 9) *(volatile LAS int*)(L + L_FS + 4 * lane) = lane < 8 ? -64 : 0;
    if (w == 1) *(volatile LAS v2u*)(L + L_Z + 8 * lane) = v2u{0, 0};
    ST(7);
    LGKM0();
    __builtin_amdgcn_s_barrier();
    if (has_f) {
      const unsigned voff = (lane >> 3) * 3584 + (lane & 7) * 16;
      const size_t off = (size_t)(64 * xc + 8 * w) * 3584 + j * 128;
#pragma unroll 1
      for (int i = 0; i < 4; ++i) { PACE(); DMA_S(" nt", voff, (const char*)(i < 2 ? wg : wu) + off + (i & 1) * (32 * 3584), lbase + L_GU + (4 * i + w) * 1024); }
    } else {
      due += 4 * TAU;
    }
    {
      const char* kb = (const char*)kc + kvoff + w * 1024;
      const unsigned voff = g * 256 + ((n ^ (4 * w + g)) * 16);
#pragma unroll 1
      for (int i = 0; i < 16; ++i) { PACE(); DMA_S(" nt", voff, kb + (((i + rot) & 15) << 12), lbase + L_K + i * 4096 + w * 1024); }
    }
    ST(1);
    {
      const unsigned voff = g * 256 + ((n ^ (2 * (g | ((w >> 1) << 2)))) * 16);
      const char* vb = (const char*)vc + kvoff + w * 1024;
#pragma unroll 1
      for (int i = 0; i < 16; ++i) {
        if (i == 8) wait4<3, 0>(L, L_FH, 1);
        if (i >= 16 - NL) due += TAUL - TAU;
        PACE(); DMA_S(" nt", voff, vb + (((i + rot) & 15) << 12), lbase + L_V + i * 4096 + w * 1024);
      }
    }
    ST(2);
    wait4<12, 0>(L, L_FH, 1);
    {
      const unsigned vo = g * 1024 + ((n ^ (2 * (g | ((w >> 1) << 2)))) * 16);
      const char* ob = (const char*)wo + (size_t)(64 * rg + 4 * w) * 1024 + cg * 256;
      const char* db = (const char*)wd + (size_t)(28 * rg) * 1024 + cg * 256;
#pragma unroll 1
      for (int i = 0; i < 6; ++i) {
        const char* src = i >= 2 ? ob + (i - 2) * 16384 : db + (i == 0 ? 4 * w : w < 3 ? 16 + 4 * w : 0) * 1024;
        due += TAU2 - TAU; PACE();
        DMA_S(" nt", vo, src, lbase + L_WO + (i >= 2 ? i - 2 : i + 4) * 4096 + w * 1024);
      }
    }
    ST(3);
#pragma unroll 1
    for (int it = 0; it < SPIN_CAP && landed < C_WD - sk; ++it) { const int vm = vm_inflight(); PUBLISH(vm); }
    ST(4);
    unsigned t12 = *(volatile LAS unsigned*)(L + L_TAG) & 0xFFFu;
    TR_ADDR(w);
    unsigned yoff = WS_Y + ((cg * 64 + rg) * 2 + (n & 1)) * 512 + w * 128 + ((n >> 1) & 1) * 64 + g * 16;
    unsigned oa = lbase + (n < 2 ? L_OB + n * 128 + g * 16 : L_Z), ob = lbase + ((n >> 1) == 1 ? L_OB + (n & 1) * 128 + g * 16 : L_Z);
    const float g1 = *(LAS float*)(L + L_GATE + (n & 1) * 8 + 4);
    wait4<12, 0>(L, L_FH, 5);
    const v4u ha = *(LAS v4u*)(L + (n < 2 ? L_HB + n * 64 + g * 16 : L_Z)), hb = *(LAS v4u*)(L + ((n >> 1) == 1 ? L_HB + (n & 1) * 64 + g * 16 : L_Z));
    wait4<15, 0>(L, L_FS, C_WD - sk);
    f32x4 y = mfma(tr2(L + L_WD + ta1, 1024), hb, mfma(tr2(L + L_WD + ta0, 1024), ha, f32x4{0, 0, 0, 0})) * g1;
    PIN(y);
    vmcnt<0>(); lds_set(L, fs, C_WO - sk);
    ST(5);
    wait4<15, 0>(L, L_FS, C_WO - sk);
    v4u wf[4];
    wf[0] = tr2(L + L_WO + ta0, 1024); wf[1] = tr2(L + L_WO + ta1, 1024); wf[2] = tr2(L + L_WO + ta0 + 8192, 1024); wf[3] = tr2(L + L_WO + ta1 + 8192, 1024);
    PIN(wf[0]); PIN(wf[1]); PIN(wf[2]); PIN(wf[3]); PIN(oa); PIN(ob); PIN(yoff); PIN(t12);
    __builtin_amdgcn_s_setprio(3);
    ST(6);
    __builtin_amdgcn_s_barrier();
    asm volatile("" ::: "memory");
    ST(10);
    {
      const v4u a0 = *(LAS v4u*)(uintptr_t)oa, b0 = *(LAS v4u*)(uintptr_t)ob, a1 = *(LAS v4u*)(uintptr_t)(oa + 64), b1 = *(LAS v4u*)(uintptr_t)(ob + 64);
      SB();
      y = mfma(wf[0], a0, y); y = mfma(wf[1], b0, y); y = mfma(wf[2], a1, y); y = mfma(wf[3], b1, y);
      st_sc1<0x000F000F000F000Full>(yoff, v4u{tagt<12>(y[0], t12), tagt<12>(y[1], t12), tagt<12>(y[2], t12), tagt<12>(y[3], t12)}, ws);
    }
    ST(11);
  }
  if (wv == PWAVE) {
    const unsigned tg = __builtin_amdgcn_readfirstlane(*(volatile LAS unsigned*)(L + L_TAG));
    st_sc1<1>(WS_STAMP + c * 128, v4u{stamp[PROBE], tg, stamp[0], tg}, ws);
    if (c == 0) {
      for (int it = 0; it < 1000000; ++it) { if ((unsigned)__builtin_amdgcn_s_memrealtime() - stamp[0] >= 2500u) break; }
      int mx = -100000, mn = 100000;
      for (int k = 0; k < 4; ++k) { v4u e; ld1w(e, WS_STAMP + (lane + 64 * k) * 128, ws);
        if (e.y == tg) { mx = max(mx, PMODE ? (int)(e.x - e.z) : (int)(e.x - stamp[0])); mn = PMODE ? 0 : min(mn, (int)(e.z - stamp[0])); } }
      for (int o = 32; o > 0; o >>= 1) { mx = max(mx, __shfl_xor(mx, o)); mn = min(mn, __shfl_xor(mn, o)); }
      const unsigned tgt = 4000u + AMP * (unsigned)(__builtin_amdgcn_readfirstlane(mx) - __builtin_amdgcn_readfirstlane(mn));
      for (int it = 0; it < 1000000; ++it) { if ((unsigned)__builtin_amdgcn_s_memrealtime() - stamp[0] >= tgt) break; }
    }
  }
}

extern "C" void kernel_launch(void* const* d_in, const int* in_sizes, int n_in, void* d_out, int out_size, void* d_ws, size_t ws_size, hipStream_t stream) {
  (void)hipFuncSetAttribute((const void*)fused_decode, hipFuncAttributeMaxDynamicSharedMemorySize, L_END);
  fused_decode<<<256, 512, L_END, stream>>>((const u16*)d_in[0], (const u16*)d_in[9], (char*)d_ws, (const u16*)d_in[3], (const u16*)d_in[6], (const u16*)d_in[7],
                                            (const u16*)d_in[1], (const u16*)d_in[2], (const u16*)d_in[4], (const u16*)d_in[8], (u16*)d_out);
}
```

```cpp
#define TO 3100
#define TY 1650
#include <hip/hip_runtime.h>
#include <hip/hip_bf16.h>
#include <stdint.h>

#define STAMP 0
#define TAU 256
#define TK0 280
#define TAUL TAU
#define NL 0
#define TAU2 256
#define TT 2900
#define TQ 4300
#define ORD 1
#define ROT 0
#define GAP 0
#define PF 0
#define TVE (TK0 + (35 - NL) * TAU + NL * TAUL)
#define WIN 12
#define CODE_PF 23

typedef unsigned short u16;
typedef unsigned long long u64;
typedef unsigned v4u __attribute__((ext_vector_type(4)));
typedef unsigned v16u __attribute__((ext_vector_type(16)));
typedef int v4i __attribute__((ext_vector_type(4)));
typedef unsigned v2u __attribute__((ext_vector_type(2)));
typedef short v4s __attribute__((ext_vector_type(4)));
typedef float f32x4 __attribute__((ext_vector_type(4)));
typedef float f32x2 __attribute__((ext_vector_type(2)));
typedef __bf16 v8bf __attribute__((ext_vector_type(8)));
typedef __bf16 v2bf __attribute__((ext_vector_type(2)));
#define GAS __attribute__((address_space(1)))
#define LAS __attribute__((address_space(3)))
#define SB() __builtin_amdgcn_sched_barrier(0)

constexpr unsigned WS_T = 1024;
constexpr unsigned WS_Q = 4096;
constexpr unsigned WS_H = WS_Q + 131072;
constexpr unsigned WS_ST = WS_H + 229376;
constexpr unsigned WS_O = WS_ST + 32768;
constexpr unsigned WS_Y = WS_O + 524288;
constexpr unsigned WS_STAMP = 4u << 20;
constexpr unsigned L_K = 0, L_V = 65536, L_WQ = L_V + 8 * 4096, L_WO = 131072, L_GU = L_WO, L_WD = L_WO + 16384, L_X = L_WD + 8192,
                   L_Q = L_X + 2048, QSTR = 320, L_P = L_Q + 4 * QSTR, PSTR = 576, L_OB = L_P + 4 * PSTR, L_HB = L_OB + 256, L_MX = L_HB + 128,
                   L_SUM = L_MX + 64, L_GATE = L_SUM + 64, L_FS = L_GATE + 16, L_FH = L_FS + 16, L_DT = L_FH + 16, L_TAG = L_DT + 4, L_T0 = L_DT + 8,
                   L_Z = L_DT + 16, L_END = L_Z + 512;
static_assert(L_END <= 163840, "LDS");
constexpr int SPIN_CAP = 1 << 17;

__device__ __forceinline__ float asf(unsigned u) { return __builtin_bit_cast(float, u); }
__device__ __forceinline__ unsigned asu(float f) { return __builtin_bit_cast(unsigned, f); }
__device__ __forceinline__ unsigned pk_bf16(float lo, float hi) { f32x2 x = {lo, hi}; return __builtin_bit_cast(unsigned, __builtin_convertvector(x, v2bf)); }
__device__ __forceinline__ float rbf(float f) { return asf(pk_bf16(f, f) << 16); }
template <int CTRL> __device__ __forceinline__ float dppf(float x) { return asf(__builtin_amdgcn_mov_dpp(asu(x), CTRL, 0xf, 0xf, true)); }
constexpr int XOR1 = 0xB1, XOR2 = 0x4E, HMIR = 0x141, ROR4 = 0x124, ROR8 = 0x128;
__device__ __forceinline__ float sw16(float a, float b) { auto s = __builtin_amdgcn_permlane16_swap(asu(a), asu(b), false, false); return asf(s[0]) + asf(s[1]); }
__device__ __forceinline__ float sw32(float a, float b) { auto s = __builtin_amdgcn_permlane32_swap(asu(a), asu(b), false, false); return asf(s[0]) + asf(s[1]); }
__device__ __forceinline__ float x16max(float x) { auto s = __builtin_amdgcn_permlane16_swap(asu(x), asu(x), false, false); return fmaxf(asf(s[0]), asf(s[1])); }
__device__ __forceinline__ float x32max(float x) { auto s = __builtin_amdgcn_permlane32_swap(asu(x), asu(x), false, false); return fmaxf(asf(s[0]), asf(s[1])); }
__device__ __forceinline__ float frcp(float x) { return __builtin_amdgcn_rcpf(x); }
__device__ __forceinline__ float fexp(float x) { return __builtin_amdgcn_exp2f(x * 1.4426950408889634f); }
__device__ __forceinline__ f32x4 mfma(v4u a, v4u b, f32x4 c) { return __builtin_amdgcn_mfma_f32_16x16x32_bf16(__builtin_bit_cast(v8bf, a), __builtin_bit_cast(v8bf, b), c, 0, 0, 0); }
__device__ __forceinline__ v2u trread(LAS char* p) { return __builtin_bit_cast(v2u, __builtin_amdgcn_ds_read_tr16_b64_v4i16((LAS v4s*)p)); }
__device__ __forceinline__ v4u tr2(LAS char* p, int d) { v2u a = trread(p), b = trread(p + d); return v4u{a.x, a.y, b.x, b.y}; }
template <int B> __device__ __forceinline__ unsigned tagt(float v, unsigned t) { return (asu(v) & ~((1u << B) - 1)) | t; }
template <int B> __device__ __forceinline__ unsigned tagf(float v, unsigned t) { return ((asu(v) + (1u << (B - 1))) & ~((1u << B) - 1)) | t; }
template <int B> __device__ __forceinline__ float untag(unsigned w) { return asf(w & ~((1u << B) - 1)); }
__device__ __forceinline__ unsigned neq(v4u g, unsigned t) { return __builtin_amdgcn_bitop3_b32(g.x, g.y, t, 0x7E) | __builtin_amdgcn_bitop3_b32(g.z, g.w, t, 0x7E); }
template <int B> __device__ __forceinline__ bool anybad(unsigned x) { return __builtin_amdgcn_ballot_w64((x & ((1u << B) - 1)) != 0) != 0; }
#define P64(lo, hi) ((char*)(((u64)(hi) << 32) | (lo)))
#define PINS(x) asm volatile("" : "+s"(x))
#define PIN(x) asm volatile("" : "+v"(x))
#define CNT(i)
#define CNT0(i)

#define DMA_S(MOD, voff, sbase, ldst) asm volatile("s_mov_b32 m0, %2\n\ts_nop 0\n\tglobal_load_lds_dwordx4 %0, %1" MOD :: "v"(voff), "s"(sbase), "s"(ldst) : "memory")
template <int N> __device__ __forceinline__ void vmcnt() { asm volatile("s_waitcnt vmcnt(%0)" ::"i"(N) : "memory"); }
#define LGKM0() asm volatile("s_waitcnt lgkmcnt(0)" ::: "memory")
__device__ __forceinline__ void ld1w(v4u& a, unsigned o0, const char* sb) {
  asm volatile("global_load_dwordx4 %0, %1, %2 sc1\n\ts_waitcnt vmcnt(0)" : "=&v"(a) : "v"(o0), "s"(sb) : "memory"); }
__device__ __forceinline__ void ld2w(v4u& a, v4u& b, unsigned o0, unsigned o1, const char* sb) {
  asm volatile("global_load_dwordx4 %0, %2, %4 sc1\n\tglobal_load_dwordx4 %1, %3, %4 sc1\n\ts_waitcnt vmcnt(0)" : "=&v"(a), "=&v"(b) : "v"(o0), "v"(o1), "s"(sb) : "memory"); }
__device__ __forceinline__ void ld4w(v4u& a, v4u& b, v4u& c, v4u& d, unsigned o0, const char* sb) {
  asm volatile("global_load_dwordx4 %0, %4, %5 sc1\n\tglobal_load_dwordx4 %1, %4, %5 offset:1024 sc1\n\tglobal_load_dwordx4 %2, %4, %5 offset:2048 sc1\n\t"
               "global_load_dwordx4 %3, %4, %5 offset:3072 sc1\n\ts_waitcnt vmcnt(0)" : "=&v"(a), "=&v"(b), "=&v"(c), "=&v"(d) : "v"(o0), "s"(sb) : "memory"); }
template <u64 MASK> __device__ __forceinline__ void st_sc1(unsigned voff, v4u d, char* sbase) {
  asm volatile("s_mov_b64 exec, %3\n\tglobal_store_dwordx4 %0, %1, %2 sc1\n\ts_mov_b64 exec, -1\n\ts_nop 0" ::"v"(voff), "v"(d), "s"(sbase), "s"(MASK) : "memory"); }

#define ST(i)
#define STAMP0()

__device__ __forceinline__ int now16(unsigned t0) { return (int)(((unsigned)__builtin_amdgcn_s_memrealtime() - t0) * 16u); }
__device__ __forceinline__ int vm_inflight() { unsigned r; asm volatile("s_getreg_b32 %0, hwreg(HW_REG_IB_STS, 0, 4)" : "=s"(r)::"memory"); return (int)r; }
__device__ __forceinline__ int clock_offset(LAS char* L) { return __builtin_amdgcn_readfirstlane(*(volatile LAS int*)(L + L_DT)); }
__device__ __forceinline__ void wait_until(LAS char* L, unsigned t0, int t) {
#pragma unroll 1
  for (int it = 0; it < SPIN_CAP && now16(t0) - clock_offset(L) < t; ++it) __builtin_amdgcn_s_sleep(1);
}
__device__ __forceinline__ void spin_until(unsigned tick) {
#pragma unroll 1
  for (int it = 0; it < SPIN_CAP && (int)((unsigned)__builtin_amdgcn_s_memrealtime() - tick) < 0; ++it) {}
}
__device__ __forceinline__ void lds_set(LAS char* L, unsigned off, unsigned v) {
  asm volatile("s_mov_b64 exec, 1\n\tds_write_b32 %0, %1\n\ts_mov_b64 exec, -1" ::"v"((unsigned)(uintptr_t)(L + off)), "v"(v) : "memory");
}
template <int MASK, int SLEEP> __device__ __forceinline__ int wait4(LAS char* L, unsigned off, int n) {
  int r = 0;
#pragma unroll 1
  for (int it = 0; it < SPIN_CAP; ++it) {
    const v4i f = *(volatile LAS v4i*)(L + off);
    int m = 0x7fffffff;
    if (MASK & 1) m = min(m, f.x);
    if (MASK & 2) m = min(m, f.y);
    if (MASK & 4) m = min(m, f.z);
    if (MASK & 8) m = min(m, f.w);
    r = __builtin_amdgcn_readfirstlane(m);
    if (r >= n) break;
    if (SLEEP) __builtin_amdgcn_s_sleep(SLEEP);
  }
  asm volatile("" ::: "memory");
  return r;
}
#define TR_ADDR(w_) const int fr = (n >> 2) | ((g & 1) << 2); \
    unsigned ta0 = (8 * g + (n >> 2)) * 256 + ((((2 * (w_)) ^ fr) * 2 + ((n & 3) >> 1)) * 16) + (n & 1) * 8; \
    unsigned ta1 = (8 * g + (n >> 2)) * 256 + ((((2 * (w_) + 1) ^ fr) * 2 + ((n & 3) >> 1)) * 16) + (n & 1) * 8; PIN(ta0); PIN(ta1)

constexpr int C_V = 41, C_WD = 43, C_WO = 47;

__global__ void __launch_bounds__(512) __attribute__((amdgpu_waves_per_eu(2, 2)))
fused_decode(const u16* x_, const u16* gw_, char* ws_, const u16* wq_, const u16* wg_, const u16* wu_, const u16* kc_, const u16* vc_, const u16* wo_,
             const u16* wd_, u16* out_) {
  asm volatile("s_load_dwordx16 s[76:91], %0, 0x0\n\ts_load_dwordx4 s[92:95], %0, 0x40\n\ts_load_dwordx2 s[96:97], %0, 0x50 ; ARGS_LOAD"
               :: "s"(__builtin_amdgcn_kernarg_segment_ptr()) : "s76", "s77", "s78", "s79", "s80", "s81", "s82", "s83", "s84", "s85", "s86", "s87", "s88", "s89", "s90", "s91", "s92", "s93", "s94", "s95", "s96", "s97");
#define ARGS(EXTRA) v16u ka; v4u kb; v2u kd; asm volatile("s_waitcnt lgkmcnt(0) ; ARGS_WAIT" EXTRA : "={s[76:91]}"(ka), "={s[92:95]}"(kb), "={s[96:97]}"(kd) :: "s98", "s99"); \
  const char* x = P64(ka[0], ka[1]); const char* gw = P64(ka[2], ka[3]); char* ws = P64(ka[4], ka[5]); const char* wq = P64(ka[6], ka[7]); \
  const char* wg = P64(ka[8], ka[9]); const char* wu = P64(ka[10], ka[11]); const char* kc = P64(ka[12], ka[13]); const char* vc = P64(ka[14], ka[15]); \
  const char* wo = P64(kb[0], kb[1]); const char* wd = P64(kb[2], kb[3]); char* out = P64(kd[0], kd[1])
#define T0() v2u tt; asm volatile("s_waitcnt lgkmcnt(0) ; T0_WAIT" : "={s[98:99]}"(tt)::"memory"); const unsigned t0 = tt[0]; STAMP0()
  extern __shared__ __attribute__((aligned(16))) char smem[];
  LAS char* L = (LAS char*)smem;
  const unsigned lbase = (unsigned)(uintptr_t)L;
  const int c = blockIdx.x, lane = threadIdx.x & 63, wv = __builtin_amdgcn_readfirstlane(threadIdx.x >> 6);
  const int xc = c & 7, j = c >> 3;
  const int kvh = xc, b = j & 1, sp = j >> 1;
  const int rg = 8 * xc + (j >> 2), cg = j & 3;
  const int qp = xc >> 1, qc = 32 * (xc & 1) + j;
  const bool has_f = j < 28;
  const int sk = has_f ? 0 : 4;
  const int g = lane >> 4, n = lane & 15;
  const int rot = ROT == 0 ? (j >> 2) * 2 + (j & 1) : ROT == 1 ? sp : ROT == 2 ? sp + 8 * b + xc : (sp >> 1) + 8 * (sp & 1) + 4 * b;
  const int stp = ORD == 0 ? 1 : ORD == 1 ? 2 * xc + 1 : ORD == 2 ? 2 * ((xc + (j >> 2)) & 7) + 1 : 2 * ((xc + j) & 7) + 1;
  const size_t kvoff = ((size_t)(b * 8 + kvh) * 16384 + 12288 + sp * 256) * 256;
  if (wv == 4) {
    const char* pc;
    asm volatile("s_getpc_b64 %0" : "=s"(pc));
    asm volatile("s_mov_b32 m0, %2\n\ts_nop 0\n\tglobal_load_lds_dword %0, %1" ::"v"(min(j, CODE_PF) * 512 + (lane & 7) * 64), "s"((const char*)((uintptr_t)pc & ~(uintptr_t)255)), "s"(lbase + L_OB) : "memory");
  }

  if (wv >= 4) {
    const int hw = wv - 4;
    const unsigned fh = L_FH + 4 * hw;
    __builtin_amdgcn_s_setprio(2);
    ARGS("");
    if (hw == 1) {
      const char* a = lane == 0 ? (const char*)vc + kvoff : lane == 1 ? (const char*)wo + (size_t)rg * 65536 : lane == 2 ? (const char*)wd + (size_t)rg * 28672
                    : lane == 3 ? (const char*)out : (const char*)kc + kvoff;
      asm volatile("s_mov_b32 m0, %1\n\ts_nop 0\n\tglobal_load_lds_dword %0, off" ::"v"(a), "s"(lbase + L_OB) : "memory");
    }
    __builtin_amdgcn_s_barrier();
    const unsigned t0 = __builtin_amdgcn_readfirstlane(*(volatile LAS unsigned*)(L + L_T0));
    STAMP0();
    wait4<15, 0>(L, L_FS, 1);
    const unsigned t32 = __builtin_amdgcn_readfirstlane(*(LAS unsigned*)(L + L_P + 1024)) + 1, t12 = t32 & 0xFFFu, t8 = t32 & 0xFFu;
    ST(1);
    if (hw < 2) {
      v4u xq[4];
#pragma unroll
      for (int s = 0; s < 4; ++s) xq[s] = *(LAS v4u*)(L + L_X + (n & 1) * 1024 + qp * 256 + s * 64 + g * 16);
      const int col = 64 * qc + 32 * hw + 16 * ((n >> 1) & 1) + 4 * g;
      const unsigned qoff = WS_Q + (((((col >> 9) * 2 + (n & 1)) * 4 + ((col >> 7) & 3)) * 4 + qp) * 128 + (col & 127)) * 4;
      LAS char* a = L + L_WQ + (8 * g + (n >> 2)) * 128 + hw * 64 + (n & 3) * 8;
      wait4<15, 0>(L, L_FS, 5);
      ST(2);
      v4u af[8];
#pragma unroll
      for (int s = 0; s < 4; ++s) { af[2 * s] = tr2(a + s * 4096, 512); af[2 * s + 1] = tr2(a + s * 4096 + 32, 512); }
      SB();
      f32x4 d0 = {0, 0, 0, 0}, d1 = {0, 0, 0, 0};
#pragma unroll
      for (int s = 0; s < 4; ++s) { d0 = mfma(af[2 * s], xq[s], d0); d1 = mfma(af[2 * s + 1], xq[s], d1); }
      v4u gr;
#pragma unroll
      for (int r = 0; r < 4; ++r) gr[r] = tagf<8>((n & 2) ? d1[r] : d0[r], t8);
      st_sc1<0x000F000F000F000Full>(qoff, gr, ws);
    } else {
      if (hw == 2 && c < 8) st_sc1<0xFF>(WS_T + c * 128 + n * 16, v4u{t0, t32, t0, t32}, ws);
      {
        const v4u gwv = *(LAS v4u*)(L + L_P + lane * 16), xv = *(LAS v4u*)(L + L_X + (hw & 1) * 1024 + lane * 16);
        float s = 0.f;
#pragma unroll
        for (int k = 0; k < 4; ++k) {
          const unsigned pr = pk_bf16(asf(xv[k] << 16) * asf(gwv[k] << 16), asf(xv[k] & 0xFFFF0000u) * asf(gwv[k] & 0xFFFF0000u));
          s += asf(pr << 16) + asf(pr & 0xFFFF0000u);
        }
        s += dppf<XOR1>(s); s += dppf<XOR2>(s); s += dppf<ROR4>(s); s += dppf<ROR8>(s); s = sw16(s, s); s = sw32(s, s);
        const float gs = rbf(frcp(1.f + fexp(-rbf(s))));
        if (lane == 0) *(LAS f32x2*)(L + L_GATE + (hw & 1) * 8) = f32x2{gs, rbf(1.f - gs)};
        lds_set(L, L_TAG, t32);
      }
      v4u xg[2];
#pragma unroll
      for (int s = 0; s < 2; ++s) xg[s] = *(LAS v4u*)(L + L_X + (n & 1) * 1024 + xc * 128 + s * 64 + g * 16);
      const unsigned hoff = WS_H + ((((n & 1) * 2 + (hw & 1)) * 8 + xc) * 448 + 16 * j + 4 * ((n >> 1) & 3) + g) * 16;
      LAS char* a = L + L_GU + (hw & 1) * 8192 + (8 * g + (n >> 2)) * 128 + (n & 3) * 8;
      wait4<15, 0>(L, L_FS, 9 - sk);
      ST(2);
      v4u af[8];
#pragma unroll
      for (int s = 0; s < 2; ++s)
#pragma unroll
        for (int t = 0; t < 4; ++t) af[4 * s + t] = tr2(a + s * 4096 + t * 32, 512);
      SB();
      f32x4 d[4];
#pragma unroll
      for (int t = 0; t < 4; ++t) d[t] = mfma(af[4 + t], xg[1], mfma(af[t], xg[0], f32x4{0, 0, 0, 0}));
      v4u gr;
#pragma unroll
      for (int r = 0; r < 4; ++r) { const float lo = (n & 2) ? d[1][r] : d[0][r], hi = (n & 2) ? d[3][r] : d[2][r]; gr[r] = tagf<12>((n & 4) ? hi : lo, t12); }
      PIN(gr);
      if (has_f) st_sc1<0x00FF00FF00FF00FFull>(hoff, gr, ws);
    }
    lds_set(L, fh, 1);
    ST(3);
    if (hw == 1) {
      wait_until(L, t0, TT);
      v4u tv;
      ld1w(tv, WS_T + (lane & 7) * 128, ws);
      const bool ok = tv.y == t32 && tv.w == t32;
      float d = ok ? (float)(int)(tv.x - t0) : 0.f, k = ok ? 1.f : 0.f;
      d += dppf<XOR1>(d); k += dppf<XOR1>(k); d += dppf<XOR2>(d); k += dppf<XOR2>(k); d += dppf<HMIR>(d); k += dppf<HMIR>(k);
      lds_set(L, L_DT, (int)fminf(fmaxf(d * 16.f * frcp(fmaxf(k, 1.f)), -1600.f), 1600.f));
    }
    {
      const unsigned qo = WS_Q + ((kvh * 2 + b) * 4 + hw) * 2048 + lane * 16;
      wait_until(L, t0, TQ);
      v4u q0, q1;
      CNT0(14); CNT0(15);
#pragma unroll 1
      for (int it = 0; it < SPIN_CAP; ++it) { ld2w(q0, q1, qo, qo + 1024, ws); if (!anybad<8>(neq(q0, t8) | neq(q1, t8))) break; CNT(14); }
      float v[4];
#pragma unroll
      for (int r = 0; r < 4; ++r) { v[r] = untag<8>(q0[r]) + untag<8>(q1[r]); v[r] = sw32(v[r], v[r]); }
      if (lane < 32) *(LAS v2u*)(L + L_Q + hw * QSTR + lane * 8) = v2u{pk_bf16(v[0], v[1]), pk_bf16(v[2], v[3])};
      LGKM0(); lds_set(L, fh, 2);
      ST(4);
    }
    wait4<15, 1>(L, L_FH, 2);
    v4u qf[4], kf[16];
#pragma unroll
    for (int s = 0; s < 4; ++s) qf[s] = *(LAS v4u*)(L + L_Q + (n & 3) * QSTR + s * 64 + g * 16);
    wait4<15, 0>(L, L_FS, 13 + 4 * hw - sk);
    ST(6);
#pragma unroll
    for (int s = 0; s < 4; ++s)
#pragma unroll
      for (int t = 0; t < 4; ++t) kf[s * 4 + t] = *(LAS v4u*)(L + L_K + (4 * hw + t) * 4096 + n * 256 + (((4 * s + g) ^ n) * 16));
    SB();
    float sc[4];
    {
      f32x4 d[4];
#pragma unroll
      for (int t = 0; t < 4; ++t) d[t] = f32x4{0, 0, 0, 0};
#pragma unroll
      for (int s = 0; s < 4; ++s)
#pragma unroll
        for (int t = 0; t < 4; ++t) d[t] = mfma(kf[s * 4 + t], qf[s], d[t]);
      constexpr float SC = 0.08838834764831845f;
      float v[4];
#pragma unroll
      for (int r = 0; r < 4; ++r) { const float lo = (n & 4) ? d[1][r] : d[0][r], hi = (n & 4) ? d[3][r] : d[2][r]; v[r] = (n & 8) ? hi : lo; }
      const unsigned a = pk_bf16(v[0], v[1]), bq = pk_bf16(v[2], v[3]);
      const unsigned a2 = pk_bf16(asf(a << 16) * SC, asf(a & 0xFFFF0000u) * SC), b2 = pk_bf16(asf(bq << 16) * SC, asf(bq & 0xFFFF0000u) * SC);
      sc[0] = asf(a2 << 16); sc[1] = asf(a2 & 0xFFFF0000u); sc[2] = asf(b2 << 16); sc[3] = asf(b2 & 0xFFFF0000u);
    }
    float m = fmaxf(fmaxf(sc[0], sc[1]), fmaxf(sc[2], sc[3]));
    m = fmaxf(m, dppf<ROR4>(m)); m = fmaxf(m, dppf<ROR8>(m)); m = x16max(m); m = x32max(m);
    if (lane < 4) *(LAS float*)(L + L_MX + lane * 16 + hw * 4) = m;
    LGKM0(); lds_set(L, fh, 3);
    wait4<15, 0>(L, L_FH, 3);
    const int e = lane & 7;
    const unsigned ho = WS_H + (((hw & 1) * 16 + (lane >> 3)) * 448 + 7 * rg + (e < 7 ? e : 6)) * 16;
    if (hw >= 2) { DMA_S(" sc1", ho, ws, lbase + L_K + hw * 2048); DMA_S(" sc1", ho + 8 * 448 * 16, ws, lbase + L_K + hw * 2048 + 1024); }
    { const f32x4 mm = *(LAS f32x4*)(L + L_MX + (n & 3) * 16); m = fmaxf(fmaxf(mm[0], mm[1]), fmaxf(mm[2], mm[3])); }
    {
      float p[4];
#pragma unroll
      for (int r = 0; r < 4; ++r) p[r] = fexp(sc[r] - m);
      *(LAS v2u*)(L + L_P + (n & 3) * PSTR + (64 * hw + 16 * (n >> 2) + 4 * g) * 2) = v2u{pk_bf16(p[0], p[1]), pk_bf16(p[2], p[3])};
      float l = (p[0] + p[1]) + (p[2] + p[3]);
      l += dppf<ROR4>(l); l += dppf<ROR8>(l); l = sw16(l, l); l = sw32(l, l);
      if (lane < 4) *(LAS float*)(L + L_SUM + lane * 16 + hw * 4) = l;
    }
    LGKM0(); lds_set(L, fh, 4);
    ST(7);
    TR_ADDR(hw);
    unsigned ooff = WS_O + (((2 * (4 * kvh + (n & 3)) + (hw >> 1)) * 2 + b) * 16 + sp) * 256 + (hw & 1) * 128 + ((n >> 2) & 1) * 64 + g * 16;
    unsigned tv = t12;
    PIN(ooff); PIN(tv);
    const int tabs = t0 + ((clock_offset(L) + TVE) >> 4);
    wait4<15, 0>(L, L_FH, 4);
    v4u pa[8], pb[8];
    {
      const unsigned pp = L_P + (n & 3) * PSTR + g * 16, aa = n < 4 ? pp : L_Z, ab = (n >> 2) == 1 ? pp : L_Z;
#pragma unroll
      for (int u = 0; u < 8; ++u) { pa[u] = *(LAS v4u*)(L + aa + u * 64); pb[u] = *(LAS v4u*)(L + ab + u * 64); }
    }
    if (hw == 2) {
      const f32x4 ls = *(LAS f32x4*)(L + L_SUM + (n & 3) * 16);
      st_sc1<0xFF>(WS_ST + ((kvh * 2 + b) * 16 + sp) * 128 + n * 16, v4u{tagt<12>(m, t12), tagf<12>((ls[0] + ls[1]) + (ls[2] + ls[3]), t12), t12, t12}, ws);
    }
    f32x4 o = {0, 0, 0, 0};
#define PVN(u0, nu) do { v4u va[nu], vb[nu]; _Pragma("unroll") for (int u = 0; u < nu; ++u) { va[u] = tr2(L + L_V + ta0 + (u0 + u) * 8192, 1024); vb[u] = tr2(L + L_V + ta1 + (u0 + u) * 8192, 1024); } SB(); \
      _Pragma("unroll") for (int u = 0; u < nu; ++u) { o = mfma(va[u], pa[u0 + u], o); o = mfma(vb[u], pb[u0 + u], o); } } while (0)
    const unsigned so = WS_ST + ((xc * 2 + (hw & 1)) * 16 + 4 * (n & 3) + g) * 128 + (j >> 3) * 16;
    if (wait4<15, 1>(L, L_FS, 33 - sk) >= C_V - sk) {
      PVN(0, 8);
    } else {
      PVN(0, 4); PIN(o);
      ST(8);
      if (wait4<15, 0>(L, L_FS, 39 - sk) >= C_V - sk) {
        PVN(4, 4);
      } else {
        PVN(4, 3); PIN(o);
        wait4<15, 0>(L, L_FS, C_V - sk);
        ST(9);
        PVN(7, 1);
      }
    }
    st_sc1<0x00FF00FF00FF00FFull>(ooff, v4u{tagt<12>(o[0], tv), tagt<12>(o[1], tv), tagt<12>(o[2], tv), tagt<12>(o[3], tv)}, ws);
    ST(10);
    if (hw >= 2) {
      vmcnt<1>();
      v4u h0 = *(LAS v4u*)(L + L_K + hw * 2048 + lane * 16), h1 = *(LAS v4u*)(L + L_K + hw * 2048 + 1024 + lane * 16);
#pragma unroll 1
      for (int it = 0; it < SPIN_CAP && anybad<12>(neq(h0, t12) | neq(h1, t12)); ++it) { ld2w(h0, h1, ho, ho + 8 * 448 * 16, ws); CNT(14); }
      float v[4];
#pragma unroll
      for (int r = 0; r < 4; ++r) v[r] = sw32(untag<12>(h0[r]), untag<12>(h1[r]));
      const float a0 = sw16(v[0], v[1]), a1 = sw16(v[2], v[3]);
      const float b0 = a0 + dppf<ROR8>(a0), b1 = a1 + dppf<ROR8>(a1);
      const float gg = rbf((lane & 8) ? b1 : b0);
      const auto sw = __builtin_amdgcn_permlane32_swap(asu(gg), asu(gg), false, false);
      const float hv = rbf(gg * frcp(1.f + fexp(-gg))) * asf(sw[1]);
      if (lane < 32) *(LAS u16*)(L + L_HB + (hw & 1) * 64 + (4 * e + ((lane >> 2) & 2) + (lane >> 4)) * 2) = e < 7 ? (u16)(pk_bf16(hv, hv) & 0xFFFFu) : (u16)0;
      LGKM0(); lds_set(L, fh, 5);
      ST(5);
    }
    if (hw < 2) {
      unsigned oo = WS_O + (rg * 2 + hw) * 4096 + lane * 16, oba = L_OB + hw * 128 + (4 * n + (g & 1) * 2 + (g >> 1)) * 2;
      DMA_S(" sc1", so, ws, lbase + L_K + hw * 1024);
      const float gate = *(LAS float*)(L + L_GATE + hw * 8);
      PIN(oo); PIN(oba);
      spin_until(tabs + (TO >> 4));
      ST(11);
      v4u a0, a1, a2, a3;
      asm volatile("global_load_dwordx4 %0, %4, %5 sc1\n\tglobal_load_dwordx4 %1, %4, %5 offset:1024 sc1\n\tglobal_load_dwordx4 %2, %4, %5 offset:2048 sc1\n\t"
                   "global_load_dwordx4 %3, %4, %5 offset:3072 sc1\n\ts_waitcnt vmcnt(4)" : "=&v"(a0), "=&v"(a1), "=&v"(a2), "=&v"(a3) : "v"(oo), "s"(ws) : "memory");
      v4u st = *(LAS v4u*)(L + L_K + hw * 1024 + lane * 16);
      if (anybad<12>(neq(st, t12))) {
#pragma unroll 1
        for (int it = 0; it < SPIN_CAP; ++it) { CNT(14); ld1w(st, so, ws); if (!anybad<12>(neq(st, t12))) break; }
      }
      const float ms = untag<12>(st.x), ls = untag<12>(st.y);
      float M = ms; M = fmaxf(M, dppf<XOR1>(M)); M = fmaxf(M, dppf<XOR2>(M)); M = x16max(M); M = x32max(M);
      const float ex = fexp(ms - M);
      float Ls = ex * ls; Ls += dppf<XOR1>(Ls); Ls += dppf<XOR2>(Ls); Ls = sw16(Ls, Ls); Ls = sw32(Ls, Ls);
      const float wv_ = gate * ex * frcp(Ls);
      const float wt0 = dppf<0x00>(wv_), wt1 = dppf<0x55>(wv_), wt2 = dppf<0xAA>(wv_), wt3 = dppf<0xFF>(wv_);
      f32x2 w0 = {wt0, wt0}, w1 = {wt1, wt1}, w2 = {wt2, wt2}, w3 = {wt3, wt3};
      PIN(w0); PIN(w1); PIN(w2); PIN(w3);
      asm volatile("s_waitcnt vmcnt(0)" : "+v"(a0), "+v"(a1), "+v"(a2), "+v"(a3)::"memory");
      ST(12);
#pragma unroll 1
      for (int it = 0; it < SPIN_CAP; ++it) {
#define UT2(a, i) f32x2{untag<12>(a[i]), untag<12>(a[i + 1])}
        const f32x2 lo = __builtin_elementwise_fma(w0, UT2(a0, 0), w1 * UT2(a1, 0)) + __builtin_elementwise_fma(w2, UT2(a2, 0), w3 * UT2(a3, 0));
        const f32x2 hi = __builtin_elementwise_fma(w0, UT2(a0, 2), w1 * UT2(a1, 2)) + __builtin_elementwise_fma(w2, UT2(a2, 2), w3 * UT2(a3, 2));
        const float t = sw16(sw32(lo[0], lo[1]), sw32(hi[0], hi[1]));
        *(volatile LAS u16*)(L + oba) = (u16)(pk_bf16(t, t) & 0xFFFFu);
        SB();
        if (!anybad<12>(neq(a0, t12) | neq(a1, t12) | neq(a2, t12) | neq(a3, t12))) break;
        CNT(15);
        ld4w(a0, a1, a2, a3, oo, ws);
      }
      LGKM0();
      ST(13);
      __builtin_amdgcn_s_barrier();
    } else if (hw == 2) {
      __builtin_amdgcn_s_barrier();
    } else {
      unsigned yo = WS_Y + (((xc >> 1) * 64 + lane) * 2 + (xc & 1)) * 512 + j * 16;
      unsigned op = ((xc & 1) * 512 + (xc >> 1) * 128 + 4 * j + (g & 1) * 2 + (g >> 1)) * 2;
      PIN(yo); PIN(op);
      __builtin_amdgcn_s_barrier();
      __builtin_amdgcn_s_sleep(4);
      spin_until(tabs + ((TO + TY) >> 4));
      ST(11);
      float t;
#pragma unroll 1
      for (int it = 0; it < SPIN_CAP; ++it) {
        v4u y;
        ld1w(y, yo, ws);
        ST(12);
        t = sw16(sw32(untag<12>(y.x), untag<12>(y.y)), sw32(untag<12>(y.z), untag<12>(y.w)));
        t += dppf<XOR1>(t); t += dppf<XOR2>(t); t += dppf<ROR4>(t); t += dppf<ROR8>(t);
        if (!anybad<12>(neq(y, t12))) break;
        CNT(15);
      }
      asm volatile("s_mov_b64 exec, %3\n\tglobal_store_short %0, %1, %2\n\ts_mov_b64 exec, -1" ::"v"(op), "v"(pk_bf16(t, t)), "s"(out), "s"(0x0001000100010001ull) : "memory");
      if (c == 0) asm volatile("s_mov_b64 exec, 1\n\tglobal_store_dword %0, %1, %2 sc1\n\ts_mov_b64 exec, -1" ::"v"(0), "v"(t32), "s"(ws) : "memory");
      ST(13);
    }
  } else {
    const int w = wv;
    const unsigned fs = L_FS + 4 * w;
    int issued = 5, landed = 0, due = TK0 + w * (TAU / 4), seen = 0, dt = 0;
#define PUBLISH(vm_) do { if (issued - (vm_) != landed) { landed = issued - (vm_); lds_set(L, fs, landed); } } while (0)
#define PACE() do { \
      const int v0_ = vm_inflight(); PUBLISH(v0_); \
      if (seen < due || v0_ >= WIN) { \
        const int d_ = *(volatile LAS int*)(L + L_DT); \
        _Pragma("unroll 1") for (int it_ = 0; it_ < SPIN_CAP; ++it_) { \
          seen = now16(t0) - dt; \
          const int vm_ = vm_inflight(); PUBLISH(vm_); \
          if (seen >= due && vm_ < WIN) break; } \
        const int dn_ = __builtin_amdgcn_readfirstlane(d_); seen += dt - dn_; dt = dn_; } \
      due += TAU; ++issued; } while (0)
    unsigned v0 = w == 3 ? 0 : lane * 16 + (w == 1 ? 1024 : 0), vq = (lane >> 3) * 8192 + (lane & 7) * 16;
    unsigned l0 = lbase + (w < 2 ? L_X : L_P - 2048) + w * 1024, lq = lbase + L_WQ + w * 1024, oq = (128 * qp + 8 * w) * 8192 + qc * 128;
    PIN(v0); PIN(vq); PINS(l0); PINS(lq); PINS(oq);
    ARGS("\n\ts_memrealtime s[98:99] ; T0_LOAD");
    DMA_S("", v0, P64(w < 2 ? ka[0] : w == 2 ? ka[2] : ka[4], w < 2 ? ka[1] : w == 2 ? ka[3] : ka[5]), l0);
#pragma unroll
    for (int i = 0; i < 4; ++i) DMA_S(" nt", vq, wq + oq + i * (32 * 8192), lq + i * 4096);
    if (w == 0 && lane < 9) *(volatile LAS int*)(L + L_FS + 4 * lane) = lane < 8 ? -64 : 0;
    if (w == 1) *(volatile LAS v2u*)(L + L_Z + 8 * lane) = v2u{0, 0};
    T0();
    if (w == 0) { lds_set(L, L_T0, t0); LGKM0(); }
    ST(7);
    __builtin_amdgcn_s_barrier();
    if (has_f) {
      const unsigned voff = (lane >> 3) * 3584 + (lane & 7) * 16;
      const size_t off = (size_t)(64 * xc + 8 * w) * 3584 + j * 128;
#pragma unroll 1
      for (int i = 0; i < 4; ++i) { PACE(); DMA_S(" nt", voff, (const char*)(i < 2 ? wg : wu) + off + (i & 1) * (32 * 3584), lbase + L_GU + (4 * i + w) * 1024); }
    } else {
      due += 4 * TAU;
    }
    {
      const char* kb = (const char*)kc + kvoff + w * 1024;
      const unsigned voff = g * 256 + ((n ^ (4 * w + g)) * 16);
#pragma unroll 1
      for (int i = 0; i < 16; ++i) { PACE(); DMA_S(" nt", voff, kb + (((i * stp + rot) & 15) << 12), lbase + L_K + i * 4096 + w * 1024); }
    }
    ST(1);
    {
      const unsigned voff = g * 256 + ((n ^ (2 * (g | ((w >> 1) << 2)))) * 16);
      const char* vb = (const char*)vc + kvoff + w * 1024;
#pragma unroll 1
      for (int i = 0; i < 16; ++i) {
        if (i == 8) wait4<3, 0>(L, L_FH, 1);
        if (i >= 16 - NL) due += TAUL - TAU;
        PACE(); DMA_S(" nt", voff, vb + (((i * stp + rot) & 15) << 12), lbase + L_V + i * 4096 + w * 1024);
      }
    }
    ST(2);
    due += GAP;
    wait4<12, 0>(L, L_FH, 1);
    {
      const unsigned vo = g * 1024 + ((n ^ (2 * (g | ((w >> 1) << 2)))) * 16);
      const char* ob = (const char*)wo + (size_t)(64 * rg + 4 * w) * 1024 + cg * 256;
      const char* db = (const char*)wd + (size_t)(28 * rg) * 1024 + cg * 256;
#pragma unroll 1
      for (int i = 0; i < 6; ++i) {
        const char* src = i >= 2 ? ob + (i - 2) * 16384 : db + (i == 0 ? 4 * w : w < 3 ? 16 + 4 * w : 0) * 1024;
        due += TAU2 - TAU; PACE();
        DMA_S(" nt", vo, src, lbase + L_WO + (i >= 2 ? i - 2 : i + 4) * 4096 + w * 1024);
      }
    }
    ST(3);
#pragma unroll 1
    for (int it = 0; it < SPIN_CAP && landed < C_WD - sk; ++it) { const int vm = vm_inflight(); PUBLISH(vm); }
    ST(4);
    unsigned t12 = *(volatile LAS unsigned*)(L + L_TAG) & 0xFFFu;
    TR_ADDR(w);
    unsigned yoff = WS_Y + ((cg * 64 + rg) * 2 + (n & 1)) * 512 + w * 128 + ((n >> 1) & 1) * 64 + g * 16;
    unsigned oa = lbase + (n < 2 ? L_OB + n * 128 + g * 16 : L_Z), ob = lbase + ((n >> 1) == 1 ? L_OB + (n & 1) * 128 + g * 16 : L_Z);
    const float g1 = *(LAS float*)(L + L_GATE + (n & 1) * 8 + 4);
    wait4<12, 0>(L, L_FH, 5);
    const v4u ha = *(LAS v4u*)(L + (n < 2 ? L_HB + n * 64 + g * 16 : L_Z)), hb = *(LAS v4u*)(L + ((n >> 1) == 1 ? L_HB + (n & 1) * 64 + g * 16 : L_Z));
    wait4<15, 0>(L, L_FS, C_WD - sk);
    f32x4 y = mfma(tr2(L + L_WD + ta1, 1024), hb, mfma(tr2(L + L_WD + ta0, 1024), ha, f32x4{0, 0, 0, 0})) * g1;
    PIN(y);
    vmcnt<0>(); lds_set(L, fs, C_WO - sk);
    ST(5);
    wait4<15, 0>(L, L_FS, C_WO - sk);
    v4u wf[4];
    wf[0] = tr2(L + L_WO + ta0, 1024); wf[1] = tr2(L + L_WO + ta1, 1024); wf[2] = tr2(L + L_WO + ta0 + 8192, 1024); wf[3] = tr2(L + L_WO + ta1 + 8192, 1024);
    PIN(wf[0]); PIN(wf[1]); PIN(wf[2]); PIN(wf[3]); PIN(oa); PIN(ob); PIN(yoff); PIN(t12);
    __builtin_amdgcn_s_setprio(3);
    ST(6);
    __builtin_amdgcn_s_barrier();
    asm volatile("" ::: "memory");
    ST(10);
    {
      const v4u a0 = *(LAS v4u*)(uintptr_t)oa, b0 = *(LAS v4u*)(uintptr_t)ob, a1 = *(LAS v4u*)(uintptr_t)(oa + 64), b1 = *(LAS v4u*)(uintptr_t)(ob + 64);
      SB();
      y = mfma(wf[0], a0, y); y = mfma(wf[1], b0, y); y = mfma(wf[2], a1, y); y = mfma(wf[3], b1, y);
      st_sc1<0x000F000F000F000Full>(yoff, v4u{tagt<12>(y[0], t12), tagt<12>(y[1], t12), tagt<12>(y[2], t12), tagt<12>(y[3], t12)}, ws);
    }
    ST(11);
  }
}

extern "C" void kernel_launch(void* const* d_in, const int* in_sizes, int n_in, void* d_out, int out_size, void* d_ws, size_t ws_size, hipStream_t stream) {
  (void)hipFuncSetAttribute((const void*)fused_decode, hipFuncAttributeMaxDynamicSharedMemorySize, L_END);
  fused_decode<<<256, 512, L_END, stream>>>((const u16*)d_in[0], (const u16*)d_in[9], (char*)d_ws, (const u16*)d_in[3], (const u16*)d_in[6], (const u16*)d_in[7],
                                            (const u16*)d_in[1], (const u16*)d_in[2], (const u16*)d_in[4], (const u16*)d_in[8], (u16*)d_out);
}
```

```cpp
#define PROBE 10
#define PWAVE 7
#define SMASK 1025
#define TO 3000
#define TY 1750
#include <hip/hip_runtime.h>
#include <hip/hip_bf16.h>
#include <stdint.h>

#define STAMP 0
#define TAU 256
#define TK0 440
#define TAUL TAU
#define NL 0
#define TAU2 256
#define TT 2900
#define TQ 4300
#define TVE (TK0 + (35 - NL) * TAU + NL * TAUL)
#define WIN 12
#define CODE_PF 25

typedef unsigned short u16;
typedef unsigned long long u64;
typedef unsigned v4u __attribute__((ext_vector_type(4)));
typedef int v4i __attribute__((ext_vector_type(4)));
typedef unsigned v2u __attribute__((ext_vector_type(2)));
typedef short v4s __attribute__((ext_vector_type(4)));
typedef float f32x4 __attribute__((ext_vector_type(4)));
typedef float f32x2 __attribute__((ext_vector_type(2)));
typedef __bf16 v8bf __attribute__((ext_vector_type(8)));
typedef __bf16 v2bf __attribute__((ext_vector_type(2)));
#define GAS __attribute__((address_space(1)))
#define LAS __attribute__((address_space(3)))
#define SB() __builtin_amdgcn_sched_barrier(0)

constexpr unsigned WS_T = 1024;
constexpr unsigned WS_Q = 4096;
constexpr unsigned WS_H = WS_Q + 131072;
constexpr unsigned WS_ST = WS_H + 229376;
constexpr unsigned WS_O = WS_ST + 16384;
constexpr unsigned WS_Y = WS_O + 524288;
constexpr unsigned WS_STAMP = 4u << 20;
constexpr unsigned L_K = 0, L_V = 65536, L_WQ = L_V + 8 * 4096, L_WO = 131072, L_GU = L_WO, L_WD = L_WO + 16384, L_X = L_WD + 8192,
                   L_Q = L_X + 2048, QSTR = 320, L_P = L_Q + 4 * QSTR, PSTR = 576, L_OB = L_P + 4 * PSTR, L_HB = L_OB + 256, L_MX = L_HB + 128,
                   L_SUM = L_MX + 64, L_GATE = L_SUM + 64, L_FS = L_GATE + 16, L_FH = L_FS + 16, L_DT = L_FH + 16, L_TAG = L_DT + 4,
                   L_Z = L_DT + 16, L_END = L_Z + 512;
static_assert(L_END <= 163840, "LDS");
constexpr int SPIN_CAP = 1 << 17;

__device__ __forceinline__ float asf(unsigned u) { return __builtin_bit_cast(float, u); }
__device__ __forceinline__ unsigned asu(float f) { return __builtin_bit_cast(unsigned, f); }
__device__ __forceinline__ unsigned pk_bf16(float lo, float hi) { f32x2 x = {lo, hi}; return __builtin_bit_cast(unsigned, __builtin_convertvector(x, v2bf)); }
__device__ __forceinline__ float rbf(float f) { return asf(pk_bf16(f, f) << 16); }
template <int CTRL> __device__ __forceinline__ float dppf(float x) { return asf(__builtin_amdgcn_mov_dpp(asu(x), CTRL, 0xf, 0xf, true)); }
constexpr int XOR1 = 0xB1, XOR2 = 0x4E, HMIR = 0x141, ROR4 = 0x124, ROR8 = 0x128;
__device__ __forceinline__ float sw16(float a, float b) { auto s = __builtin_amdgcn_permlane16_swap(asu(a), asu(b), false, false); return asf(s[0]) + asf(s[1]); }
__device__ __forceinline__ float sw32(float a, float b) { auto s = __builtin_amdgcn_permlane32_swap(asu(a), asu(b), false, false); return asf(s[0]) + asf(s[1]); }
__device__ __forceinline__ float x16max(float x) { auto s = __builtin_amdgcn_permlane16_swap(asu(x), asu(x), false, false); return fmaxf(asf(s[0]), asf(s[1])); }
__device__ __forceinline__ float x32max(float x) { auto s = __builtin_amdgcn_permlane32_swap(asu(x), asu(x), false, false); return fmaxf(asf(s[0]), asf(s[1])); }
__device__ __forceinline__ float frcp(float x) { return __builtin_amdgcn_rcpf(x); }
__device__ __forceinline__ float fexp(float x) { return __builtin_amdgcn_exp2f(x * 1.4426950408889634f); }
__device__ __forceinline__ f32x4 mfma(v4u a, v4u b, f32x4 c) { return __builtin_amdgcn_mfma_f32_16x16x32_bf16(__builtin_bit_cast(v8bf, a), __builtin_bit_cast(v8bf, b), c, 0, 0, 0); }
__device__ __forceinline__ v2u trread(LAS char* p) { return __builtin_bit_cast(v2u, __builtin_amdgcn_ds_read_tr16_b64_v4i16((LAS v4s*)p)); }
__device__ __forceinline__ v4u tr2(LAS char* p, int d) { v2u a = trread(p), b = trread(p + d); return v4u{a.x, a.y, b.x, b.y}; }
template <int B> __device__ __forceinline__ unsigned tagt(float v, unsigned t) { return (asu(v) & ~((1u << B) - 1)) | t; }
template <int B> __device__ __forceinline__ unsigned tagf(float v, unsigned t) { return ((asu(v) + (1u << (B - 1))) & ~((1u << B) - 1)) | t; }
template <int B> __device__ __forceinline__ float untag(unsigned w) { return asf(w & ~((1u << B) - 1)); }
__device__ __forceinline__ unsigned neq(v4u g, unsigned t) { return __builtin_amdgcn_bitop3_b32(g.x, g.y, t, 0x7E) | __builtin_amdgcn_bitop3_b32(g.z, g.w, t, 0x7E); }
template <int B> __device__ __forceinline__ bool anybad(unsigned x) { return __builtin_amdgcn_ballot_w64((x & ((1u << B) - 1)) != 0) != 0; }
#define PIN(x) asm volatile("" : "+v"(x))
#define CNT(i)
#define CNT0(i)

#define DMA_S(MOD, voff, sbase, ldst) asm volatile("s_mov_b32 m0, %2\n\ts_nop 0\n\tglobal_load_lds_dwordx4 %0, %1" MOD :: "v"(voff), "s"(sbase), "s"(ldst) : "memory")
template <int N> __device__ __forceinline__ void vmcnt() { asm volatile("s_waitcnt vmcnt(%0)" ::"i"(N) : "memory"); }
#define LGKM0() asm volatile("s_waitcnt lgkmcnt(0)" ::: "memory")
__device__ __forceinline__ void ld1w(v4u& a, unsigned o0, const char* sb) {
  asm volatile("global_load_dwordx4 %0, %1, %2 sc1\n\ts_waitcnt vmcnt(0)" : "=&v"(a) : "v"(o0), "s"(sb) : "memory"); }
__device__ __forceinline__ void ld2w(v4u& a, v4u& b, unsigned o0, unsigned o1, const char* sb) {
  asm volatile("global_load_dwordx4 %0, %2, %4 sc1\n\tglobal_load_dwordx4 %1, %3, %4 sc1\n\ts_waitcnt vmcnt(0)" : "=&v"(a), "=&v"(b) : "v"(o0), "v"(o1), "s"(sb) : "memory"); }
__device__ __forceinline__ void ld4w(v4u& a, v4u& b, v4u& c, v4u& d, unsigned o0, const char* sb) {
  asm volatile("global_load_dwordx4 %0, %4, %5 sc1\n\tglobal_load_dwordx4 %1, %4, %5 offset:1024 sc1\n\tglobal_load_dwordx4 %2, %4, %5 offset:2048 sc1\n\t"
               "global_load_dwordx4 %3, %4, %5 offset:3072 sc1\n\ts_waitcnt vmcnt(0)" : "=&v"(a), "=&v"(b), "=&v"(c), "=&v"(d) : "v"(o0), "s"(sb) : "memory"); }
template <u64 MASK> __device__ __forceinline__ void st_sc1(unsigned voff, v4u d, char* sbase) {
  asm volatile("s_mov_b64 exec, %3\n\tglobal_store_dwordx4 %0, %1, %2 sc1\n\ts_mov_b64 exec, -1\n\ts_nop 0" ::"v"(voff), "v"(d), "s"(sbase), "s"(MASK) : "memory"); }

#define ST(i) do { if ((SMASK >> (i)) & 1) { SB(); stamp[i] = (unsigned)__builtin_amdgcn_s_memrealtime(); SB(); } } while (0)

__device__ __forceinline__ int now16(unsigned t0) { return (int)(((unsigned)__builtin_amdgcn_s_memrealtime() - t0) * 16u); }
__device__ __forceinline__ int vm_inflight() { unsigned r; asm volatile("s_getreg_b32 %0, hwreg(HW_REG_IB_STS, 0, 4)" : "=s"(r)::"memory"); return (int)r; }
__device__ __forceinline__ int clock_offset(LAS char* L) { return __builtin_amdgcn_readfirstlane(*(volatile LAS int*)(L + L_DT)); }
__device__ __forceinline__ void wait_until(LAS char* L, unsigned t0, int t) {
#pragma unroll 1
  for (int it = 0; it < SPIN_CAP && now16(t0) - clock_offset(L) < t; ++it) __builtin_amdgcn_s_sleep(1);
}
__device__ __forceinline__ void spin_until(unsigned tick) {
#pragma unroll 1
  for (int it = 0; it < SPIN_CAP && (int)((unsigned)__builtin_amdgcn_s_memrealtime() - tick) < 0; ++it) {}
}
__device__ __forceinline__ void lds_set(LAS char* L, unsigned off, unsigned v) {
  asm volatile("s_mov_b64 exec, 1\n\tds_write_b32 %0, %1\n\ts_mov_b64 exec, -1" ::"v"((unsigned)(uintptr_t)(L + off)), "v"(v) : "memory");
}
template <int MASK, int SLEEP> __device__ __forceinline__ void wait4(LAS char* L, unsigned off, int n) {
#pragma unroll 1
  for (int it = 0; it < SPIN_CAP; ++it) {
    const v4i f = *(volatile LAS v4i*)(L + off);
    int m = 0x7fffffff;
    if (MASK & 1) m = min(m, f.x);
    if (MASK & 2) m = min(m, f.y);
    if (MASK & 4) m = min(m, f.z);
    if (MASK & 8) m = min(m, f.w);
    if (__builtin_amdgcn_readfirstlane(m) >= n) break;
    if (SLEEP) __builtin_amdgcn_s_sleep(SLEEP);
  }
  asm volatile("" ::: "memory");
}
#define TR_ADDR(w_) const int fr = (n >> 2) | ((g & 1) << 2); \
    unsigned ta0 = (8 * g + (n >> 2)) * 256 + ((((2 * (w_)) ^ fr) * 2 + ((n & 3) >> 1)) * 16) + (n & 1) * 8; \
    unsigned ta1 = (8 * g + (n >> 2)) * 256 + ((((2 * (w_) + 1) ^ fr) * 2 + ((n & 3) >> 1)) * 16) + (n & 1) * 8; PIN(ta0); PIN(ta1)

constexpr int C_V = 41, C_WD = 43, C_WO = 47;

__global__ void __launch_bounds__(512) __attribute__((amdgpu_waves_per_eu(2, 2)))
fused_decode(const u16* __restrict__ x, const u16* __restrict__ gw, char* __restrict__ ws, const u16* __restrict__ wq, const u16* __restrict__ wg,
             const u16* __restrict__ wu, const u16* __restrict__ kc, const u16* __restrict__ vc, const u16* __restrict__ wo, const u16* __restrict__ wd,
             u16* __restrict__ out) {
  extern __shared__ __attribute__((aligned(16))) char smem[];
  LAS char* L = (LAS char*)smem;
  const unsigned lbase = (unsigned)(uintptr_t)L;
  const int c = blockIdx.x, lane = threadIdx.x & 63, wv = __builtin_amdgcn_readfirstlane(threadIdx.x >> 6);
  const int xc = c & 7, j = c >> 3;
  const int kvh = xc, b = j & 1, sp = j >> 1;
  const int rg = 8 * xc + (j >> 2), cg = j & 3;
  const int qp = xc >> 1, qc = 32 * (xc & 1) + j;
  const bool has_f = j < 28;
  const int sk = has_f ? 0 : 4;
  const int g = lane >> 4, n = lane & 15;
  const int rot = (j >> 2) * 2 + (j & 1);
  const size_t kvoff = ((size_t)(b * 8 + kvh) * 16384 + 12288 + sp * 256) * 256;
  unsigned stamp[16];
  for (int i = 0; i < 16; ++i) stamp[i] = 0;
  asm volatile("" ::"s"(x), "s"(kc), "s"(vc), "s"(wq), "s"(wo), "s"(wg), "s"(wu), "s"(wd), "s"(gw), "s"(out), "s"(ws));
  const unsigned t0 = (unsigned)__builtin_amdgcn_s_memrealtime();
  ST(0);
  if (wv == 4) {
    const char* pc;
    asm volatile("s_getpc_b64 %0" : "=s"(pc));
    asm volatile("s_mov_b32 m0, %2\n\ts_nop 0\n\tglobal_load_lds_dword %0, %1" ::"v"(min(j, CODE_PF) * 512 + (lane & 7) * 64), "s"((const char*)((uintptr_t)pc & ~(uintptr_t)255)), "s"(lbase + L_OB) : "memory");
  }

  if (wv >= 4) {
    const int hw = wv - 4;
    const unsigned fh = L_FH + 4 * hw;
    __builtin_amdgcn_s_setprio(2);
    if (hw == 1) {
      const char* a = lane == 0 ? (const char*)vc + kvoff : lane == 1 ? (const char*)wo + (size_t)rg * 65536 : lane == 2 ? (const char*)wd + (size_t)rg * 28672
                    : lane == 3 ? (const char*)out : (const char*)kc + kvoff;
      asm volatile("s_mov_b32 m0, %1\n\ts_nop 0\n\tglobal_load_lds_dword %0, off" ::"v"(a), "s"(lbase + L_OB) : "memory");
    }
    __builtin_amdgcn_s_barrier();
    wait4<15, 0>(L, L_FS, 1);
    const unsigned t32 = __builtin_amdgcn_readfirstlane(*(LAS unsigned*)(L + L_P + 1024)) + 1, t12 = t32 & 0xFFFu, t8 = t32 & 0xFFu;
    ST(1);
    if (hw < 2) {
      v4u xq[4];
#pragma unroll
      for (int s = 0; s < 4; ++s) xq[s] = *(LAS v4u*)(L + L_X + (n & 1) * 1024 + qp * 256 + s * 64 + g * 16);
      const int col = 64 * qc + 32 * hw + 16 * ((n >> 1) & 1) + 4 * g;
      const unsigned qoff = WS_Q + (((((col >> 9) * 2 + (n & 1)) * 4 + ((col >> 7) & 3)) * 4 + qp) * 128 + (col & 127)) * 4;
      LAS char* a = L + L_WQ + (8 * g + (n >> 2)) * 128 + hw * 64 + (n & 3) * 8;
      wait4<15, 0>(L, L_FS, 5);
      ST(2);
      v4u af[8];
#pragma unroll
      for (int s = 0; s < 4; ++s) { af[2 * s] = tr2(a + s * 4096, 512); af[2 * s + 1] = tr2(a + s * 4096 + 32, 512); }
      SB();
      f32x4 d0 = {0, 0, 0, 0}, d1 = {0, 0, 0, 0};
#pragma unroll
      for (int s = 0; s < 4; ++s) { d0 = mfma(af[2 * s], xq[s], d0); d1 = mfma(af[2 * s + 1], xq[s], d1); }
      v4u gr;
#pragma unroll
      for (int r = 0; r < 4; ++r) gr[r] = tagf<8>((n & 2) ? d1[r] : d0[r], t8);
      st_sc1<0x000F000F000F000Full>(qoff, gr, ws);
    } else {
      if (hw == 2 && c < 8) st_sc1<1>(WS_T + c * 128, v4u{t0, t32, t0, t32}, ws);
      {
        const v4u gwv = *(LAS v4u*)(L + L_P + lane * 16), xv = *(LAS v4u*)(L + L_X + (hw & 1) * 1024 + lane * 16);
        float s = 0.f;
#pragma unroll
        for (int k = 0; k < 4; ++k) {
          const unsigned pr = pk_bf16(asf(xv[k] << 16) * asf(gwv[k] << 16), asf(xv[k] & 0xFFFF0000u) * asf(gwv[k] & 0xFFFF0000u));
          s += asf(pr << 16) + asf(pr & 0xFFFF0000u);
        }
        s += dppf<XOR1>(s); s += dppf<XOR2>(s); s += dppf<ROR4>(s); s += dppf<ROR8>(s); s = sw16(s, s); s = sw32(s, s);
        const float gs = rbf(frcp(1.f + fexp(-rbf(s))));
        if (lane == 0) *(LAS f32x2*)(L + L_GATE + (hw & 1) * 8) = f32x2{gs, rbf(1.f - gs)};
        lds_set(L, L_TAG, t32);
      }
      v4u xg[2];
#pragma unroll
      for (int s = 0; s < 2; ++s) xg[s] = *(LAS v4u*)(L + L_X + (n & 1) * 1024 + xc * 128 + s * 64 + g * 16);
      const unsigned hoff = WS_H + ((((n & 1) * 2 + (hw & 1)) * 8 + xc) * 448 + 16 * j + 4 * ((n >> 1) & 3) + g) * 16;
      LAS char* a = L + L_GU + (hw & 1) * 8192 + (8 * g + (n >> 2)) * 128 + (n & 3) * 8;
      wait4<15, 0>(L, L_FS, 9 - sk);
      ST(2);
      v4u af[8];
#pragma unroll
      for (int s = 0; s < 2; ++s)
#pragma unroll
        for (int t = 0; t < 4; ++t) af[4 * s + t] = tr2(a + s * 4096 + t * 32, 512);
      SB();
      f32x4 d[4];
#pragma unroll
      for (int t = 0; t < 4; ++t) d[t] = mfma(af[4 + t], xg[1], mfma(af[t], xg[0], f32x4{0, 0, 0, 0}));
      v4u gr;
#pragma unroll
      for (int r = 0; r < 4; ++r) { const float lo = (n & 2) ? d[1][r] : d[0][r], hi = (n & 2) ? d[3][r] : d[2][r]; gr[r] = tagf<12>((n & 4) ? hi : lo, t12); }
      PIN(gr);
      if (has_f) st_sc1<0x00FF00FF00FF00FFull>(hoff, gr, ws);
    }
    lds_set(L, fh, 1);
    ST(3);
    if (hw == 1) {
      wait_until(L, t0, TT);
      v4u tv;
      ld1w(tv, WS_T + (lane & 7) * 128, ws);
      const bool ok = tv.y == t32 && tv.w == t32;
      float d = ok ? (float)(int)(tv.x - t0) : 0.f, k = ok ? 1.f : 0.f;
      d += dppf<XOR1>(d); k += dppf<XOR1>(k); d += dppf<XOR2>(d); k += dppf<XOR2>(k); d += dppf<HMIR>(d); k += dppf<HMIR>(k);
      lds_set(L, L_DT, (int)fminf(fmaxf(d * 16.f * frcp(fmaxf(k, 1.f)), -1600.f), 1600.f));
    }
    {
      const unsigned qo = WS_Q + ((kvh * 2 + b) * 4 + hw) * 2048 + lane * 16;
      wait_until(L, t0, TQ);
      v4u q0, q1;
      CNT0(14); CNT0(15);
#pragma unroll 1
      for (int it = 0; it < SPIN_CAP; ++it) { ld2w(q0, q1, qo, qo + 1024, ws); if (!anybad<8>(neq(q0, t8) | neq(q1, t8))) break; CNT(14); }
      float v[4];
#pragma unroll
      for (int r = 0; r < 4; ++r) { v[r] = untag<8>(q0[r]) + untag<8>(q1[r]); v[r] = sw32(v[r], v[r]); }
      if (lane < 32) *(LAS v2u*)(L + L_Q + hw * QSTR + lane * 8) = v2u{pk_bf16(v[0], v[1]), pk_bf16(v[2], v[3])};
      LGKM0(); lds_set(L, fh, 2);
      ST(4);
    }
    wait4<15, 1>(L, L_FH, 2);
    v4u qf[4], kf[16];
#pragma unroll
    for (int s = 0; s < 4; ++s) qf[s] = *(LAS v4u*)(L + L_Q + (n & 3) * QSTR + s * 64 + g * 16);
    wait4<15, 0>(L, L_FS, 13 + 4 * hw - sk);
    ST(6);
#pragma unroll
    for (int s = 0; s < 4; ++s)
#pragma unroll
      for (int t = 0; t < 4; ++t) kf[s * 4 + t] = *(LAS v4u*)(L + L_K + (4 * hw + t) * 4096 + n * 256 + (((4 * s + g) ^ n) * 16));
    SB();
    float sc[4];
    {
      f32x4 d[4];
#pragma unroll
      for (int t = 0; t < 4; ++t) d[t] = f32x4{0, 0, 0, 0};
#pragma unroll
      for (int s = 0; s < 4; ++s)
#pragma unroll
        for (int t = 0; t < 4; ++t) d[t] = mfma(kf[s * 4 + t], qf[s], d[t]);
      constexpr float SC = 0.08838834764831845f;
      float v[4];
#pragma unroll
      for (int r = 0; r < 4; ++r) { const float lo = (n & 4) ? d[1][r] : d[0][r], hi = (n & 4) ? d[3][r] : d[2][r]; v[r] = (n & 8) ? hi : lo; }
      const unsigned a = pk_bf16(v[0], v[1]), bq = pk_bf16(v[2], v[3]);
      const unsigned a2 = pk_bf16(asf(a << 16) * SC, asf(a & 0xFFFF0000u) * SC), b2 = pk_bf16(asf(bq << 16) * SC, asf(bq & 0xFFFF0000u) * SC);
      sc[0] = asf(a2 << 16); sc[1] = asf(a2 & 0xFFFF0000u); sc[2] = asf(b2 << 16); sc[3] = asf(b2 & 0xFFFF0000u);
    }
    float m = fmaxf(fmaxf(sc[0], sc[1]), fmaxf(sc[2], sc[3]));
    m = fmaxf(m, dppf<ROR4>(m)); m = fmaxf(m, dppf<ROR8>(m)); m = x16max(m); m = x32max(m);
    if (lane < 4) *(LAS float*)(L + L_MX + lane * 16 + hw * 4) = m;
    LGKM0(); lds_set(L, fh, 3);
    wait4<15, 0>(L, L_FH, 3);
    const int e = lane & 7;
    const unsigned ho = WS_H + (((hw & 1) * 16 + (lane >> 3)) * 448 + 7 * rg + (e < 7 ? e : 6)) * 16;
    if (hw >= 2) { DMA_S(" sc1", ho, ws, lbase + L_K + hw * 2048); DMA_S(" sc1", ho + 8 * 448 * 16, ws, lbase + L_K + hw * 2048 + 1024); }
    { const f32x4 mm = *(LAS f32x4*)(L + L_MX + (n & 3) * 16); m = fmaxf(fmaxf(mm[0], mm[1]), fmaxf(mm[2], mm[3])); }
    {
      float p[4];
#pragma unroll
      for (int r = 0; r < 4; ++r) p[r] = fexp(sc[r] - m);
      *(LAS v2u*)(L + L_P + (n & 3) * PSTR + (64 * hw + 16 * (n >> 2) + 4 * g) * 2) = v2u{pk_bf16(p[0], p[1]), pk_bf16(p[2], p[3])};
      float l = (p[0] + p[1]) + (p[2] + p[3]);
      l += dppf<ROR4>(l); l += dppf<ROR8>(l); l = sw16(l, l); l = sw32(l, l);
      if (lane < 4) *(LAS float*)(L + L_SUM + lane * 16 + hw * 4) = l;
    }
    LGKM0(); lds_set(L, fh, 4);
    ST(7);
    TR_ADDR(hw);
    unsigned ooff = WS_O + (((2 * (4 * kvh + (n & 3)) + (hw >> 1)) * 2 + b) * 16 + sp) * 256 + (hw & 1) * 128 + ((n >> 2) & 1) * 64 + g * 16;
    unsigned tv = t12;
    PIN(ooff); PIN(tv);
    const int tabs = t0 + ((clock_offset(L) + TVE) >> 4);
    wait4<15, 0>(L, L_FH, 4);
    v4u pa[8], pb[8];
    {
      const unsigned pp = L_P + (n & 3) * PSTR + g * 16, aa = n < 4 ? pp : L_Z, ab = (n >> 2) == 1 ? pp : L_Z;
#pragma unroll
      for (int u = 0; u < 8; ++u) { pa[u] = *(LAS v4u*)(L + aa + u * 64); pb[u] = *(LAS v4u*)(L + ab + u * 64); }
    }
    if (hw == 2) {
      const f32x4 ls = *(LAS f32x4*)(L + L_SUM + (n & 3) * 16);
      st_sc1<0xF>(WS_ST + (((kvh * 4 + (n & 3)) * 2 + b) * 16 + sp) * 16, v4u{tagt<12>(m, t12), tagf<12>((ls[0] + ls[1]) + (ls[2] + ls[3]), t12), t12, t12}, ws);
    }
    f32x4 o = {0, 0, 0, 0};
#define PVN(u0, nu) do { v4u va[nu], vb[nu]; _Pragma("unroll") for (int u = 0; u < nu; ++u) { va[u] = tr2(L + L_V + ta0 + (u0 + u) * 8192, 1024); vb[u] = tr2(L + L_V + ta1 + (u0 + u) * 8192, 1024); } SB(); \
      _Pragma("unroll") for (int u = 0; u < nu; ++u) { o = mfma(va[u], pa[u0 + u], o); o = mfma(vb[u], pb[u0 + u], o); } } while (0)
    wait4<15, 1>(L, L_FS, 33 - sk);
    PVN(0, 4); PIN(o);
    ST(8);
    if (hw >= 2) {
      if (hw == 2) vmcnt<1>(); else vmcnt<0>();
      v4u h0 = *(LAS v4u*)(L + L_K + hw * 2048 + lane * 16), h1 = *(LAS v4u*)(L + L_K + hw * 2048 + 1024 + lane * 16);
#pragma unroll 1
      for (int it = 0; it < SPIN_CAP && anybad<12>(neq(h0, t12) | neq(h1, t12)); ++it) { ld2w(h0, h1, ho, ho + 8 * 448 * 16, ws); CNT(14); }
      float v[4];
#pragma unroll
      for (int r = 0; r < 4; ++r) v[r] = sw32(untag<12>(h0[r]), untag<12>(h1[r]));
      const float a0 = sw16(v[0], v[1]), a1 = sw16(v[2], v[3]);
      const float b0 = a0 + dppf<ROR8>(a0), b1 = a1 + dppf<ROR8>(a1);
      const float gg = rbf((lane & 8) ? b1 : b0);
      const auto sw = __builtin_amdgcn_permlane32_swap(asu(gg), asu(gg), false, false);
      const float hv = rbf(gg * frcp(1.f + fexp(-gg))) * asf(sw[1]);
      if (lane < 32) *(LAS u16*)(L + L_HB + (hw & 1) * 64 + (4 * e + ((lane >> 2) & 2) + (lane >> 4)) * 2) = e < 7 ? (u16)(pk_bf16(hv, hv) & 0xFFFFu) : (u16)0;
      LGKM0(); lds_set(L, fh, 5);
      ST(5);
    }
    wait4<15, 0>(L, L_FS, 39 - sk);
    PVN(4, 3); PIN(o);
    const unsigned so = WS_ST + (((rg >> 1) * 2 + (hw & 1)) * 16 + 4 * (n & 3) + g) * 16;
    wait4<15, 0>(L, L_FS, C_V - sk);
    ST(9);
    PVN(7, 1);
    st_sc1<0x00FF00FF00FF00FFull>(ooff, v4u{tagt<12>(o[0], tv), tagt<12>(o[1], tv), tagt<12>(o[2], tv), tagt<12>(o[3], tv)}, ws);
    ST(10);
    if (hw < 2) {
      unsigned oo = WS_O + (rg * 2 + hw) * 4096 + lane * 16, oba = L_OB + hw * 128 + (4 * n + (g & 1) * 2 + (g >> 1)) * 2;
      DMA_S(" sc1", so, ws, lbase + L_K + hw * 1024);
      const float gate = *(LAS float*)(L + L_GATE + hw * 8);
      PIN(oo); PIN(oba);
      spin_until(tabs + (TO >> 4));
      ST(11);
      v4u a0, a1, a2, a3;
      asm volatile("global_load_dwordx4 %0, %4, %5 sc1\n\tglobal_load_dwordx4 %1, %4, %5 offset:1024 sc1\n\tglobal_load_dwordx4 %2, %4, %5 offset:2048 sc1\n\t"
                   "global_load_dwordx4 %3, %4, %5 offset:3072 sc1\n\ts_waitcnt vmcnt(4)" : "=&v"(a0), "=&v"(a1), "=&v"(a2), "=&v"(a3) : "v"(oo), "s"(ws) : "memory");
      v4u st = *(LAS v4u*)(L + L_K + hw * 1024 + lane * 16);
      if (anybad<12>(neq(st, t12))) {
#pragma unroll 1
        for (int it = 0; it < SPIN_CAP; ++it) { CNT(14); ld1w(st, so, ws); if (!anybad<12>(neq(st, t12))) break; }
      }
      const float ms = untag<12>(st.x), ls = untag<12>(st.y);
      float M = ms; M = fmaxf(M, dppf<XOR1>(M)); M = fmaxf(M, dppf<XOR2>(M)); M = x16max(M); M = x32max(M);
      const float ex = fexp(ms - M);
      float Ls = ex * ls; Ls += dppf<XOR1>(Ls); Ls += dppf<XOR2>(Ls); Ls = sw16(Ls, Ls); Ls = sw32(Ls, Ls);
      const float wv_ = gate * ex * frcp(Ls);
      const float wt0 = dppf<0x00>(wv_), wt1 = dppf<0x55>(wv_), wt2 = dppf<0xAA>(wv_), wt3 = dppf<0xFF>(wv_);
      f32x2 w0 = {wt0, wt0}, w1 = {wt1, wt1}, w2 = {wt2, wt2}, w3 = {wt3, wt3};
      PIN(w0); PIN(w1); PIN(w2); PIN(w3);
      asm volatile("s_waitcnt vmcnt(0)" : "+v"(a0), "+v"(a1), "+v"(a2), "+v"(a3)::"memory");
      ST(12);
#pragma unroll 1
      for (int it = 0; it < SPIN_CAP; ++it) {
#define UT2(a, i) f32x2{untag<12>(a[i]), untag<12>(a[i + 1])}
        const f32x2 lo = __builtin_elementwise_fma(w0, UT2(a0, 0), w1 * UT2(a1, 0)) + __builtin_elementwise_fma(w2, UT2(a2, 0), w3 * UT2(a3, 0));
        const f32x2 hi = __builtin_elementwise_fma(w0, UT2(a0, 2), w1 * UT2(a1, 2)) + __builtin_elementwise_fma(w2, UT2(a2, 2), w3 * UT2(a3, 2));
        const float t = sw16(sw32(lo[0], lo[1]), sw32(hi[0], hi[1]));
        *(volatile LAS u16*)(L + oba) = (u16)(pk_bf16(t, t) & 0xFFFFu);
        SB();
        if (!anybad<12>(neq(a0, t12) | neq(a1, t12) | neq(a2, t12) | neq(a3, t12))) break;
        CNT(15);
        ld4w(a0, a1, a2, a3, oo, ws);
      }
      LGKM0();
      ST(13);
      __builtin_amdgcn_s_barrier();
    } else if (hw == 2) {
      __builtin_amdgcn_s_barrier();
    } else {
      unsigned yo = WS_Y + (((xc >> 1) * 64 + lane) * 2 + (xc & 1)) * 512 + j * 16;
      unsigned op = ((xc & 1) * 512 + (xc >> 1) * 128 + 4 * j + (g & 1) * 2 + (g >> 1)) * 2;
      PIN(yo); PIN(op);
      __builtin_amdgcn_s_barrier();
      __builtin_amdgcn_s_sleep(4);
      spin_until(tabs + ((TO + TY) >> 4));
      ST(11);
      float t;
#pragma unroll 1
      for (int it = 0; it < SPIN_CAP; ++it) {
        v4u y;
        ld1w(y, yo, ws);
        ST(12);
        t = sw16(sw32(untag<12>(y.x), untag<12>(y.y)), sw32(untag<12>(y.z), untag<12>(y.w)));
        t += dppf<XOR1>(t); t += dppf<XOR2>(t); t += dppf<ROR4>(t); t += dppf<ROR8>(t);
        if (!anybad<12>(neq(y, t12))) break;
        CNT(15);
      }
      asm volatile("s_mov_b64 exec, %3\n\tglobal_store_short %0, %1, %2\n\ts_mov_b64 exec, -1" ::"v"(op), "v"(pk_bf16(t, t)), "s"(out), "s"(0x0001000100010001ull) : "memory");
      if (c == 0) asm volatile("s_mov_b64 exec, 1\n\tglobal_store_dword %0, %1, %2 sc1\n\ts_mov_b64 exec, -1" ::"v"(0), "v"(t32), "s"(ws) : "memory");
      ST(13);
    }
  } else {
    const int w = wv;
    const unsigned fs = L_FS + 4 * w;
    int issued = 5, landed = 0, due = TK0 + w * (TAU / 4), seen = 0, dt = 0;
#define PUBLISH(vm_) do { if (issued - (vm_) != landed) { landed = issued - (vm_); lds_set(L, fs, landed); } } while (0)
#define PACE() do { \
      const int v0_ = vm_inflight(); PUBLISH(v0_); \
      if (seen < due || v0_ >= WIN) { \
        const int d_ = *(volatile LAS int*)(L + L_DT); \
        _Pragma("unroll 1") for (int it_ = 0; it_ < SPIN_CAP; ++it_) { \
          seen = now16(t0) - dt; \
          const int vm_ = vm_inflight(); PUBLISH(vm_); \
          if (seen >= due && vm_ < WIN) break; } \
        const int dn_ = __builtin_amdgcn_readfirstlane(d_); seen += dt - dn_; dt = dn_; } \
      due += TAU; ++issued; } while (0)
    {
      const char* sb = w == 0 ? (const char*)x : w == 1 ? (const char*)x + 1024 : w == 2 ? (const char*)gw : (const char*)ws;
      DMA_S("", w == 3 ? 0 : lane * 16, sb, lbase + (w < 2 ? L_X : L_P - 2048) + w * 1024);
    }
    {
      const char* base = (const char*)wq + (size_t)(128 * qp + 8 * w) * 8192 + qc * 128;
      const unsigned voff = (lane >> 3) * 8192 + (lane & 7) * 16;
#pragma unroll
      for (int i = 0; i < 4; ++i) DMA_S(" nt", voff, base + (size_t)i * 32 * 8192, lbase + L_WQ + (4 * i + w) * 1024);
    }
    if (w == 0 && lane < 9) *(volatile LAS int*)(L + L_FS + 4 * lane) = lane < 8 ? -64 : 0;
    if (w == 1) *(volatile LAS v2u*)(L + L_Z + 8 * lane) = v2u{0, 0};
    ST(7);
    LGKM0();
    __builtin_amdgcn_s_barrier();
    if (has_f) {
      const unsigned voff = (lane >> 3) * 3584 + (lane & 7) * 16;
      const size_t off = (size_t)(64 * xc + 8 * w) * 3584 + j * 128;
#pragma unroll 1
      for (int i = 0; i < 4; ++i) { PACE(); DMA_S(" nt", voff, (const char*)(i < 2 ? wg : wu) + off + (i & 1) * (32 * 3584), lbase + L_GU + (4 * i + w) * 1024); }
    } else {
      due += 4 * TAU;
    }
    {
      const char* kb = (const char*)kc + kvoff + w * 1024;
      const unsigned voff = g * 256 + ((n ^ (4 * w + g)) * 16);
#pragma unroll 1
      for (int i = 0; i < 16; ++i) { PACE(); DMA_S(" nt", voff, kb + (((i + rot) & 15) << 12), lbase + L_K + i * 4096 + w * 1024); }
    }
    ST(1);
    {
      const unsigned voff = g * 256 + ((n ^ (2 * (g | ((w >> 1) << 2)))) * 16);
      const char* vb = (const char*)vc + kvoff + w * 1024;
#pragma unroll 1
      for (int i = 0; i < 16; ++i) {
        if (i == 8) wait4<3, 0>(L, L_FH, 1);
        if (i >= 16 - NL) due += TAUL - TAU;
        PACE(); DMA_S(" nt", voff, vb + (((i + rot) & 15) << 12), lbase + L_V + i * 4096 + w * 1024);
      }
    }
    ST(2);
    wait4<12, 0>(L, L_FH, 1);
    {
      const unsigned vo = g * 1024 + ((n ^ (2 * (g | ((w >> 1) << 2)))) * 16);
      const char* ob = (const char*)wo + (size_t)(64 * rg + 4 * w) * 1024 + cg * 256;
      const char* db = (const char*)wd + (size_t)(28 * rg) * 1024 + cg * 256;
#pragma unroll 1
      for (int i = 0; i < 6; ++i) {
        const char* src = i >= 2 ? ob + (i - 2) * 16384 : db + (i == 0 ? 4 * w : w < 3 ? 16 + 4 * w : 0) * 1024;
        due += TAU2 - TAU; PACE();
        DMA_S(" nt", vo, src, lbase + L_WO + (i >= 2 ? i - 2 : i + 4) * 4096 + w * 1024);
      }
    }
    ST(3);
#pragma unroll 1
    for (int it = 0; it < SPIN_CAP && landed < C_WD - sk; ++it) { const int vm = vm_inflight(); PUBLISH(vm); }
    ST(4);
    unsigned t12 = *(volatile LAS unsigned*)(L + L_TAG) & 0xFFFu;
    TR_ADDR(w);
    unsigned yoff = WS_Y + ((cg * 64 + rg) * 2 + (n & 1)) * 512 + w * 128 + ((n >> 1) & 1) * 64 + g * 16;
    unsigned oa = lbase + (n < 2 ? L_OB + n * 128 + g * 16 : L_Z), ob = lbase + ((n >> 1) == 1 ? L_OB + (n & 1) * 128 + g * 16 : L_Z);
    const float g1 = *(LAS float*)(L + L_GATE + (n & 1) * 8 + 4);
    wait4<12, 0>(L, L_FH, 5);
    const v4u ha = *(LAS v4u*)(L + (n < 2 ? L_HB + n * 64 + g * 16 : L_Z)), hb = *(LAS v4u*)(L + ((n >> 1) == 1 ? L_HB + (n & 1) * 64 + g * 16 : L_Z));
    wait4<15, 0>(L, L_FS, C_WD - sk);
    f32x4 y = mfma(tr2(L + L_WD + ta1, 1024), hb, mfma(tr2(L + L_WD + ta0, 1024), ha, f32x4{0, 0, 0, 0})) * g1;
    PIN(y);
    vmcnt<0>(); lds_set(L, fs, C_WO - sk);
    ST(5);
    wait4<15, 0>(L, L_FS, C_WO - sk);
    v4u wf[4];
    wf[0] = tr2(L + L_WO + ta0, 1024); wf[1] = tr2(L + L_WO + ta1, 1024); wf[2] = tr2(L + L_WO + ta0 + 8192, 1024); wf[3] = tr2(L + L_WO + ta1 + 8192, 1024);
    PIN(wf[0]); PIN(wf[1]); PIN(wf[2]); PIN(wf[3]); PIN(oa); PIN(ob); PIN(yoff); PIN(t12);
    __builtin_amdgcn_s_setprio(3);
    ST(6);
    __builtin_amdgcn_s_barrier();
    asm volatile("" ::: "memory");
    ST(10);
    {
      const v4u a0 = *(LAS v4u*)(uintptr_t)oa, b0 = *(LAS v4u*)(uintptr_t)ob, a1 = *(LAS v4u*)(uintptr_t)(oa + 64), b1 = *(LAS v4u*)(uintptr_t)(ob + 64);
      SB();
      y = mfma(wf[0], a0, y); y = mfma(wf[1], b0, y); y = mfma(wf[2], a1, y); y = mfma(wf[3], b1, y);
      st_sc1<0x000F000F000F000Full>(yoff, v4u{tagt<12>(y[0], t12), tagt<12>(y[1], t12), tagt<12>(y[2], t12), tagt<12>(y[3], t12)}, ws);
    }
    ST(11);
  }
  if (wv == PWAVE) {
    const unsigned tg = __builtin_amdgcn_readfirstlane(*(volatile LAS unsigned*)(L + L_TAG));
    st_sc1<1>(WS_STAMP + c * 128, v4u{stamp[PROBE], tg, stamp[0], tg}, ws);
    if (c == 0) {
      for (int it = 0; it < 1000000; ++it) { if ((unsigned)__builtin_amdgcn_s_memrealtime() - stamp[0] >= 2500u) break; }
      int mx = -100000, mn = 100000;
      for (int k = 0; k < 4; ++k) { v4u e; ld1w(e, WS_STAMP + (lane + 64 * k) * 128, ws);
        if (e.y == tg) { mx = max(mx, (int)(e.x - stamp[0])); mn = min(mn, (int)(e.z - stamp[0])); } }
      for (int o = 32; o > 0; o >>= 1) { mx = max(mx, __shfl_xor(mx, o)); mn = min(mn, __shfl_xor(mn, o)); }
      const unsigned tgt = 4000u + 8u * (unsigned)(__builtin_amdgcn_readfirstlane(mx) - __builtin_amdgcn_readfirstlane(mn));
      for (int it = 0; it < 1000000; ++it) { if ((unsigned)__builtin_amdgcn_s_memrealtime() - stamp[0] >= tgt) break; }
    }
  }
}

extern "C" void kernel_launch(void* const* d_in, const int* in_sizes, int n_in, void* d_out, int out_size, void* d_ws, size_t ws_size, hipStream_t stream) {
  (void)hipFuncSetAttribute((const void*)fused_decode, hipFuncAttributeMaxDynamicSharedMemorySize, L_END);
  fused_decode<<<256, 512, L_END, stream>>>((const u16*)d_in[0], (const u16*)d_in[9], (char*)d_ws, (const u16*)d_in[3], (const u16*)d_in[6], (const u16*)d_in[7],
                                            (const u16*)d_in[1], (const u16*)d_in[2], (const u16*)d_in[4], (const u16*)d_in[8], (u16*)d_out);
}
```

```cpp
#include <hip/hip_runtime.h>
#include <hip/hip_bf16.h>
#include <stdint.h>

constexpr int TK0 = 280;
constexpr int TAU = 228;
constexpr int TAU3 = 200;
constexpr int TVE = TK0 + 35 * TAU;
constexpr int TT = 2900;
constexpr int TQ = 4300;
constexpr int TO = TVE + 3160;
constexpr int TY = TO + 1720;
constexpr int WIN = 12;
constexpr int CODE_PF = 22;

typedef unsigned short u16;
typedef unsigned long long u64;
typedef unsigned v4u __attribute__((ext_vector_type(4)));
typedef unsigned v16u __attribute__((ext_vector_type(16)));
typedef int v4i __attribute__((ext_vector_type(4)));
typedef unsigned v2u __attribute__((ext_vector_type(2)));
typedef short v4s __attribute__((ext_vector_type(4)));
typedef float f32x4 __attribute__((ext_vector_type(4)));
typedef float f32x2 __attribute__((ext_vector_type(2)));
typedef __bf16 v8bf __attribute__((ext_vector_type(8)));
typedef __bf16 v2bf __attribute__((ext_vector_type(2)));
#define LAS __attribute__((address_space(3)))
#define SB() __builtin_amdgcn_sched_barrier(0)

constexpr unsigned WS_T = 1024;
constexpr unsigned WS_Q = 4096;
constexpr unsigned WS_H = WS_Q + 131072;
constexpr unsigned WS_ST = WS_H + 229376;
constexpr unsigned WS_O = WS_ST + 32768;
constexpr unsigned WS_Y = WS_O + 524288;
constexpr unsigned L_K = 0, L_V = 65536, L_WQ = L_V + 8 * 4096, L_WO = 131072, L_GU = L_WO, L_WD = L_WO + 16384, L_X = L_WD + 8192,
                   L_Q = L_X + 2048, QSTR = 320, L_P = L_Q + 4 * QSTR, PSTR = 576, L_OB = L_P + 4 * PSTR, L_HB = L_OB + 256, L_MX = L_HB + 128,
                   L_SUM = L_MX + 64, L_GATE = L_SUM + 64, L_FS = L_GATE + 16, L_FH = L_FS + 16, L_DT = L_FH + 16, L_TAG = L_DT + 4, L_T0 = L_DT + 8,
                   L_Z = L_DT + 16, L_END = L_Z + 512;
static_assert(L_END <= 163840, "LDS");
constexpr int SPIN_CAP = 1 << 21, LDS_CAP = 1 << 25;

__device__ __forceinline__ float asf(unsigned u) { return __builtin_bit_cast(float, u); }
__device__ __forceinline__ unsigned asu(float f) { return __builtin_bit_cast(unsigned, f); }
__device__ __forceinline__ unsigned pk_bf16(float lo, float hi) { f32x2 x = {lo, hi}; return __builtin_bit_cast(unsigned, __builtin_convertvector(x, v2bf)); }
__device__ __forceinline__ float rbf(float f) { return asf(pk_bf16(f, f) << 16); }
template <int CTRL> __device__ __forceinline__ float dppf(float x) { return asf(__builtin_amdgcn_mov_dpp(asu(x), CTRL, 0xf, 0xf, true)); }
constexpr int XOR1 = 0xB1, XOR2 = 0x4E, HMIR = 0x141, ROR4 = 0x124, ROR8 = 0x128;
__device__ __forceinline__ float sw16(float a, float b) { auto s = __builtin_amdgcn_permlane16_swap(asu(a), asu(b), false, false); return asf(s[0]) + asf(s[1]); }
__device__ __forceinline__ float sw32(float a, float b) { auto s = __builtin_amdgcn_permlane32_swap(asu(a), asu(b), false, false); return asf(s[0]) + asf(s[1]); }
__device__ __forceinline__ float vmax(float a, float b) { float r; asm("v_max_f32 %0, %1, %2" : "=v"(r) : "v"(a), "v"(b)); return r; }
__device__ __forceinline__ float x16max(float x) { auto s = __builtin_amdgcn_permlane16_swap(asu(x), asu(x), false, false); return vmax(asf(s[0]), asf(s[1])); }
__device__ __forceinline__ float x32max(float x) { auto s = __builtin_amdgcn_permlane32_swap(asu(x), asu(x), false, false); return vmax(asf(s[0]), asf(s[1])); }
__device__ __forceinline__ float frcp(float x) { return __builtin_amdgcn_rcpf(x); }
__device__ __forceinline__ float fexp(float x) { return __builtin_amdgcn_exp2f(x * 1.4426950408889634f); }
__device__ __forceinline__ f32x4 mfma(v4u a, v4u b, f32x4 c) { return __builtin_amdgcn_mfma_f32_16x16x32_bf16(__builtin_bit_cast(v8bf, a), __builtin_bit_cast(v8bf, b), c, 0, 0, 0); }
__device__ __forceinline__ v2u trread(LAS char* p) { return __builtin_bit_cast(v2u, __builtin_amdgcn_ds_read_tr16_b64_v4i16((LAS v4s*)p)); }
__device__ __forceinline__ v4u tr2(LAS char* p, int d) { v2u a = trread(p), b = trread(p + d); return v4u{a.x, a.y, b.x, b.y}; }
template <int B> __device__ __forceinline__ unsigned tagt(float v, unsigned t) { return (asu(v) & ~((1u << B) - 1)) | t; }
template <int B> __device__ __forceinline__ unsigned tagf(float v, unsigned t) { return ((asu(v) + (1u << (B - 1))) & ~((1u << B) - 1)) | t; }
template <int B> __device__ __forceinline__ float untag(unsigned w) { return asf(w & ~((1u << B) - 1)); }
__device__ __forceinline__ unsigned neq(v4u g, unsigned t) { return __builtin_amdgcn_bitop3_b32(g.x, g.y, t, 0x7E) | __builtin_amdgcn_bitop3_b32(g.z, g.w, t, 0x7E); }
template <int B> __device__ __forceinline__ bool anybad(unsigned x) { return __builtin_amdgcn_ballot_w64((x & ((1u << B) - 1)) != 0) != 0; }
#define P64(lo, hi) ((char*)(((u64)(hi) << 32) | (lo)))
#define PINS(x) asm volatile("" : "+s"(x))
#define PIN(x) asm volatile("" : "+v"(x))

#define DMA_S(MOD, voff, sbase, ldst) asm volatile("s_mov_b32 m0, %2\n\ts_nop 0\n\tglobal_load_lds_dwordx4 %0, %1" MOD :: "v"(voff), "s"(sbase), "s"(ldst) : "memory")
template <int N> __device__ __forceinline__ void vmcnt() { asm volatile("s_waitcnt vmcnt(%0)" ::"i"(N) : "memory"); }
#define LGKM0() asm volatile("s_waitcnt lgkmcnt(0)" ::: "memory")
__device__ __forceinline__ void ld1w(v4u& a, unsigned o0, const char* sb) {
  asm volatile("global_load_dwordx4 %0, %1, %2 sc1\n\ts_waitcnt vmcnt(0)" : "=&v"(a) : "v"(o0), "s"(sb) : "memory"); }
__device__ __forceinline__ void ld2w(v4u& a, v4u& b, unsigned o0, unsigned o1, const char* sb) {
  asm volatile("global_load_dwordx4 %0, %2, %4 sc1\n\tglobal_load_dwordx4 %1, %3, %4 sc1\n\ts_waitcnt vmcnt(0)" : "=&v"(a), "=&v"(b) : "v"(o0), "v"(o1), "s"(sb) : "memory"); }
template <u64 MASK> __device__ __forceinline__ void st_sc1(unsigned voff, v4u d, char* sbase) {
  asm volatile("s_mov_b64 exec, %3\n\tglobal_store_dwordx4 %0, %1, %2 sc1\n\ts_mov_b64 exec, -1\n\ts_nop 0" ::"v"(voff), "v"(d), "s"(sbase), "s"(MASK) : "memory"); }

__device__ __forceinline__ int now16(unsigned t0) { return (int)(((unsigned)__builtin_amdgcn_s_memrealtime() - t0) * 16u); }
__device__ __forceinline__ int vm_inflight() { unsigned r; asm volatile("s_getreg_b32 %0, hwreg(HW_REG_IB_STS, 0, 4)" : "=s"(r)::"memory"); return (int)r; }
__device__ __forceinline__ int clock_offset(LAS char* L) { return __builtin_amdgcn_readfirstlane(*(volatile LAS int*)(L + L_DT)); }
__device__ __forceinline__ void wait_until(LAS char* L, unsigned t0, int t) {
#pragma unroll 1
  for (int it = 0; it < SPIN_CAP && now16(t0) - clock_offset(L) < t; ++it) __builtin_amdgcn_s_sleep(1);
}
__device__ __forceinline__ void spin_until(unsigned tick) {
#pragma unroll 1
  for (int it = 0; it < SPIN_CAP && (int)((unsigned)__builtin_amdgcn_s_memrealtime() - tick) < 0; ++it) {}
}
__device__ __forceinline__ void lds_set(LAS char* L, unsigned off, unsigned v) {
  asm volatile("s_mov_b64 exec, 1\n\tds_write_b32 %0, %1\n\ts_mov_b64 exec, -1" ::"v"((unsigned)(uintptr_t)(L + off)), "v"(v) : "memory");
}
template <int MASK, int SLEEP> __device__ __forceinline__ int wait4(LAS char* L, unsigned off, int n) {
  int r = 0;
#pragma unroll 1
  for (int it = 0; it < LDS_CAP; ++it) {
    const v4i f = *(volatile LAS v4i*)(L + off);
    int m = 0x7fffffff;
    if (MASK & 1) m = min(m, f.x);
    if (MASK & 2) m = min(m, f.y);
    if (MASK & 4) m = min(m, f.z);
    if (MASK & 8) m = min(m, f.w);
    r = __builtin_amdgcn_readfirstlane(m);
    if (r >= n) break;
    if (SLEEP) __builtin_amdgcn_s_sleep(SLEEP);
  }
  asm volatile("" ::: "memory");
  return r;
}
#define TR_ADDR(w_) const int fr = (n >> 2) | ((g & 1) << 2); \
    unsigned ta0 = (8 * g + (n >> 2)) * 256 + ((((2 * (w_)) ^ fr) * 2 + ((n & 3) >> 1)) * 16) + (n & 1) * 8; \
    unsigned ta1 = (8 * g + (n >> 2)) * 256 + ((((2 * (w_) + 1) ^ fr) * 2 + ((n & 3) >> 1)) * 16) + (n & 1) * 8; PIN(ta0); PIN(ta1)

constexpr int C_V = 41, C_WO = 47;

__global__ void __launch_bounds__(512) __attribute__((amdgpu_waves_per_eu(2, 2)))
fused_decode(const u16* x_, const u16* gw_, char* ws_, const u16* wq_, const u16* wg_, const u16* wu_, const u16* kc_, const u16* vc_, const u16* wo_,
             const u16* wd_, u16* out_) {
  asm volatile("s_load_dwordx16 s[76:91], %0, 0x0\n\ts_load_dwordx4 s[92:95], %0, 0x40\n\ts_load_dwordx2 s[96:97], %0, 0x50 ; ARGS_LOAD"
               :: "s"(__builtin_amdgcn_kernarg_segment_ptr()) : "s76", "s77", "s78", "s79", "s80", "s81", "s82", "s83", "s84", "s85", "s86", "s87", "s88", "s89", "s90", "s91", "s92", "s93", "s94", "s95", "s96", "s97");
#define ARGS(EXTRA) v16u ka; v4u kb; v2u kd; asm volatile("s_waitcnt lgkmcnt(0) ; ARGS_WAIT" EXTRA : "={s[76:91]}"(ka), "={s[92:95]}"(kb), "={s[96:97]}"(kd) :: "s98", "s99"); \
  const char* x = P64(ka[0], ka[1]); const char* gw = P64(ka[2], ka[3]); char* ws = P64(ka[4], ka[5]); const char* wq = P64(ka[6], ka[7]); \
  const char* wg = P64(ka[8], ka[9]); const char* wu = P64(ka[10], ka[11]); const char* kc = P64(ka[12], ka[13]); const char* vc = P64(ka[14], ka[15]); \
  const char* wo = P64(kb[0], kb[1]); const char* wd = P64(kb[2], kb[3]); char* out = P64(kd[0], kd[1])
#define T0() v2u tt; asm volatile("s_waitcnt lgkmcnt(0) ; T0_WAIT" : "={s[98:99]}"(tt)::"memory"); const unsigned t0 = tt[0]
  extern __shared__ __attribute__((aligned(16))) char smem[];
  LAS char* L = (LAS char*)smem;
  const unsigned lbase = (unsigned)(uintptr_t)L;
  const int c = blockIdx.x, lane = threadIdx.x & 63, wv = __builtin_amdgcn_readfirstlane(threadIdx.x >> 6);
  const int xc = c & 7, j = c >> 3;
  const int kvh = xc, b = j & 1, sp = j >> 1;
  const int rg = 8 * xc + (j >> 2), cg = j & 3;
  const int qp = xc >> 1, qc = 32 * (xc & 1) + j;
  const bool has_f = j < (xc < 4 ? 32 : 24);
  const int ft = (xc < 4 ? 32 * xc : 32 + 24 * xc) + j, fp = ft / 28, fc = ft - 28 * fp;
  const int sk = has_f ? 0 : 4;
  const int g = lane >> 4, n = lane & 15;
  const int rot = (j >> 2) * 2 + (j & 1), stp = 2 * xc + 1;
  const size_t kvoff = ((size_t)(b * 8 + kvh) * 16384 + 12288 + sp * 256) * 256;
  if (wv == 4) {
    const char* pc;
    asm volatile("s_getpc_b64 %0" : "=s"(pc));
    asm volatile("s_mov_b32 m0, %2\n\ts_nop 0\n\tglobal_load_lds_dword %0, %1" ::"v"(min(j, CODE_PF) * 512 + (lane & 7) * 64), "s"((const char*)((uintptr_t)pc & ~(uintptr_t)255)), "s"(lbase + L_OB) : "memory");
  }

  if (wv >= 4) {
    const int hw = wv - 4;
    const unsigned fh = L_FH + 4 * hw;
    __builtin_amdgcn_s_setprio(2);
    ARGS("");
    __builtin_amdgcn_s_barrier();
    const unsigned t0 = __builtin_amdgcn_readfirstlane(*(volatile LAS unsigned*)(L + L_T0));
    wait4<15, 0>(L, L_FS, 1);
    const unsigned t32 = __builtin_amdgcn_readfirstlane(*(LAS unsigned*)(L + L_P + 1024)) + 1, t12 = t32 & 0xFFFu, t8 = t32 & 0xFFu;
    if (hw < 2) {
      v4u xq[4];
#pragma unroll
      for (int s = 0; s < 4; ++s) xq[s] = *(LAS v4u*)(L + L_X + (n & 1) * 1024 + qp * 256 + s * 64 + g * 16);
      const int col = 64 * qc + 32 * hw + 16 * ((n >> 1) & 1) + 4 * g;
      const unsigned qoff = WS_Q + (((((col >> 9) * 2 + (n & 1)) * 4 + ((col >> 7) & 3)) * 4 + qp) * 128 + (col & 127)) * 4;
      LAS char* a = L + L_WQ + (8 * g + (n >> 2)) * 128 + hw * 64 + (n & 3) * 8;
      wait4<15, 0>(L, L_FS, 5);
      v4u af[8];
#pragma unroll
      for (int s = 0; s < 4; ++s) { af[2 * s] = tr2(a + s * 4096, 512); af[2 * s + 1] = tr2(a + s * 4096 + 32, 512); }
      SB();
      f32x4 d0 = {0, 0, 0, 0}, d1 = {0, 0, 0, 0};
#pragma unroll
      for (int s = 0; s < 4; ++s) { d0 = mfma(af[2 * s], xq[s], d0); d1 = mfma(af[2 * s + 1], xq[s], d1); }
      v4u gr;
#pragma unroll
      for (int r = 0; r < 4; ++r) gr[r] = tagf<8>((n & 2) ? d1[r] : d0[r], t8);
      st_sc1<0x000F000F000F000Full>(qoff, gr, ws);
    } else {
      if (hw == 2 && c < 8) st_sc1<0xFF>(WS_T + c * 128 + n * 16, v4u{t0, t32, t0, t32}, ws);
      {
        const v4u gwv = *(LAS v4u*)(L + L_P + lane * 16), xv = *(LAS v4u*)(L + L_X + (hw & 1) * 1024 + lane * 16);
        float s = 0.f;
#pragma unroll
        for (int k = 0; k < 4; ++k) {
          const unsigned pr = pk_bf16(asf(xv[k] << 16) * asf(gwv[k] << 16), asf(xv[k] & 0xFFFF0000u) * asf(gwv[k] & 0xFFFF0000u));
          s += asf(pr << 16) + asf(pr & 0xFFFF0000u);
        }
        s += dppf<XOR1>(s); s += dppf<XOR2>(s); s += dppf<ROR4>(s); s += dppf<ROR8>(s); s = sw16(s, s); s = sw32(s, s);
        const float gs = rbf(frcp(1.f + fexp(-rbf(s))));
        if (lane == 0) *(LAS f32x2*)(L + L_GATE + (hw & 1) * 8) = f32x2{gs, rbf(1.f - gs)};
        lds_set(L, L_TAG, t32);
      }
      v4u xg[2];
#pragma unroll
      for (int s = 0; s < 2; ++s) xg[s] = *(LAS v4u*)(L + L_X + (n & 1) * 1024 + (fp & 7) * 128 + s * 64 + g * 16);
      const unsigned hoff = WS_H + ((((n & 1) * 2 + (hw & 1)) * 8 + fp) * 448 + 16 * fc + 4 * ((n >> 1) & 3) + g) * 16;
      LAS char* a = L + L_GU + (hw & 1) * 8192 + (8 * g + (n >> 2)) * 128 + (n & 3) * 8;
      wait4<15, 0>(L, L_FS, 9 - sk);
      v4u af[8];
#pragma unroll
      for (int s = 0; s < 2; ++s)
#pragma unroll
        for (int t = 0; t < 4; ++t) af[4 * s + t] = tr2(a + s * 4096 + t * 32, 512);
      SB();
      f32x4 d[4];
#pragma unroll
      for (int t = 0; t < 4; ++t) d[t] = mfma(af[4 + t], xg[1], mfma(af[t], xg[0], f32x4{0, 0, 0, 0}));
      v4u gr;
#pragma unroll
      for (int r = 0; r < 4; ++r) { const float lo = (n & 2) ? d[1][r] : d[0][r], hi = (n & 2) ? d[3][r] : d[2][r]; gr[r] = tagf<12>((n & 4) ? hi : lo, t12); }
      PIN(gr);
      if (has_f) st_sc1<0x00FF00FF00FF00FFull>(hoff, gr, ws);
    }
    lds_set(L, fh, 1);
    if (hw == 1) {
      wait_until(L, t0, TT);
      v4u tv;
      ld1w(tv, WS_T + (lane & 7) * 128, ws);
      const bool ok = tv.y == t32 && tv.w == t32;
      float d = ok ? (float)(int)(tv.x - t0) : 0.f, k = ok ? 1.f : 0.f;
      d += dppf<XOR1>(d); k += dppf<XOR1>(k); d += dppf<XOR2>(d); k += dppf<XOR2>(k); d += dppf<HMIR>(d); k += dppf<HMIR>(k);
      lds_set(L, L_DT, (int)fminf(fmaxf(d * 16.f * frcp(fmaxf(k, 1.f)), -1600.f), 1600.f));
    }
    {
      const unsigned qo = WS_Q + ((kvh * 2 + b) * 4 + hw) * 2048 + lane * 16;
      wait_until(L, t0, TQ);
      v4u q0, q1;
#pragma unroll 1
      for (int it = 0; it < SPIN_CAP; ++it) { ld2w(q0, q1, qo, qo + 1024, ws); if (!anybad<8>(neq(q0, t8) | neq(q1, t8))) break; }
      float v[4];
#pragma unroll
      for (int r = 0; r < 4; ++r) { v[r] = untag<8>(q0[r]) + untag<8>(q1[r]); v[r] = sw32(v[r], v[r]); }
      if (lane < 32) *(LAS v2u*)(L + L_Q + hw * QSTR + lane * 8) = v2u{pk_bf16(v[0], v[1]), pk_bf16(v[2], v[3])};
      LGKM0(); lds_set(L, fh, 2);
    }
    wait4<15, 1>(L, L_FH, 2);
    v4u qf[4], kf[16];
#pragma unroll
    for (int s = 0; s < 4; ++s) qf[s] = *(LAS v4u*)(L + L_Q + (n & 3) * QSTR + s * 64 + g * 16);
    wait4<15, 0>(L, L_FS, 13 + 4 * hw - sk);
#pragma unroll
    for (int s = 0; s < 4; ++s)
#pragma unroll
      for (int t = 0; t < 4; ++t) kf[s * 4 + t] = *(LAS v4u*)(L + L_K + (4 * hw + t) * 4096 + n * 256 + (((4 * s + g) ^ n) * 16));
    SB();
    float sc[4];
    {
      f32x4 d[4];
#pragma unroll
      for (int t = 0; t < 4; ++t) d[t] = f32x4{0, 0, 0, 0};
#pragma unroll
      for (int s = 0; s < 4; ++s)
#pragma unroll
        for (int t = 0; t < 4; ++t) d[t] = mfma(kf[s * 4 + t], qf[s], d[t]);
      constexpr float SC = 0.08838834764831845f;
      float v[4];
#pragma unroll
      for (int r = 0; r < 4; ++r) { const float lo = (n & 4) ? d[1][r] : d[0][r], hi = (n & 4) ? d[3][r] : d[2][r]; v[r] = (n & 8) ? hi : lo; }
      const unsigned a = pk_bf16(v[0], v[1]), bq = pk_bf16(v[2], v[3]);
      const unsigned a2 = pk_bf16(asf(a << 16) * SC, asf(a & 0xFFFF0000u) * SC), b2 = pk_bf16(asf(bq << 16) * SC, asf(bq & 0xFFFF0000u) * SC);
      sc[0] = asf(a2 << 16); sc[1] = asf(a2 & 0xFFFF0000u); sc[2] = asf(b2 << 16); sc[3] = asf(b2 & 0xFFFF0000u);
    }
    float m = vmax(vmax(sc[0], sc[1]), vmax(sc[2], sc[3]));
    m = vmax(m, dppf<ROR4>(m)); m = vmax(m, dppf<ROR8>(m)); m = x16max(m); m = x32max(m);
    if (lane < 4) *(LAS float*)(L + L_MX + lane * 16 + hw * 4) = m;
    LGKM0(); lds_set(L, fh, 3);
    wait4<15, 0>(L, L_FH, 3);
    { const f32x4 mm = *(LAS f32x4*)(L + L_MX + (n & 3) * 16); m = vmax(vmax(mm[0], mm[1]), vmax(mm[2], mm[3])); }
    {
      float p[4];
#pragma unroll
      for (int r = 0; r < 4; ++r) p[r] = fexp(sc[r] - m);
      *(LAS v2u*)(L + L_P + (n & 3) * PSTR + (64 * hw + 16 * (n >> 2) + 4 * g) * 2) = v2u{pk_bf16(p[0], p[1]), pk_bf16(p[2], p[3])};
      float l = (p[0] + p[1]) + (p[2] + p[3]);
      l += dppf<ROR4>(l); l += dppf<ROR8>(l); l = sw16(l, l); l = sw32(l, l);
      if (lane < 4) *(LAS float*)(L + L_SUM + lane * 16 + hw * 4) = l;
    }
    LGKM0(); lds_set(L, fh, 4);
    TR_ADDR(hw);
    unsigned ooff = WS_O + (((2 * (4 * kvh + (n & 3)) + (hw >> 1)) * 2 + b) * 16 + sp) * 256 + (hw & 1) * 128 + ((n >> 2) & 1) * 64 + g * 16;
    unsigned tv = t12;
    PIN(ooff); PIN(tv);
    const int tabs = t0 + (clock_offset(L) >> 4);
    wait4<15, 0>(L, L_FH, 4);
    v4u pa[8], pb[8];
    {
      const unsigned pp = L_P + (n & 3) * PSTR + g * 16, aa = n < 4 ? pp : L_Z, ab = (n >> 2) == 1 ? pp : L_Z;
#pragma unroll
      for (int u = 0; u < 8; ++u) { pa[u] = *(LAS v4u*)(L + aa + u * 64); pb[u] = *(LAS v4u*)(L + ab + u * 64); }
    }
    v4u sg;
    { const f32x4 ls = *(LAS f32x4*)(L + L_SUM + (n & 3) * 16); sg = v4u{tagt<12>(m, tv), tagf<12>((ls[0] + ls[1]) + (ls[2] + ls[3]), tv), tv, tv}; }
    unsigned soff = WS_ST + ((kvh * 2 + b) * 16 + sp) * 128 + n * 16;
    PIN(sg); PIN(soff);
    f32x4 o = {0, 0, 0, 0};
#define PVN(u0, nu) do { v4u va[nu], vb[nu]; _Pragma("unroll") for (int u = 0; u < nu; ++u) { va[u] = tr2(L + L_V + ta0 + (u0 + u) * 8192, 1024); vb[u] = tr2(L + L_V + ta1 + (u0 + u) * 8192, 1024); } SB(); \
      _Pragma("unroll") for (int u = 0; u < nu; ++u) { o = mfma(va[u], pa[u0 + u], o); o = mfma(vb[u], pb[u0 + u], o); } } while (0)
    if (wait4<15, 1>(L, L_FS, 33 - sk) >= C_V - sk) {
      PVN(0, 8);
    } else {
      PVN(0, 4); PIN(o);
      if (wait4<15, 0>(L, L_FS, 39 - sk) >= C_V - sk) {
        PVN(4, 4);
      } else {
        PVN(4, 3); PIN(o);
        wait4<15, 0>(L, L_FS, C_V - sk);
        PVN(7, 1);
      }
    }
    st_sc1<0x00FF00FF00FF00FFull>(ooff, v4u{tagt<12>(o[0], tv), tagt<12>(o[1], tv), tagt<12>(o[2], tv), tagt<12>(o[3], tv)}, ws);
    if (hw == 2) st_sc1<0xFF>(soff, sg, ws);
    if (hw >= 2) {
      const int e = lane & 7;
      const unsigned ho = WS_H + (((hw & 1) * 16 + (lane >> 3)) * 448 + 7 * rg + (e < 7 ? e : 6)) * 16;
      v4u h0, h1;
#pragma unroll 1
      for (int it = 0; it < SPIN_CAP; ++it) { ld2w(h0, h1, ho, ho + 8 * 448 * 16, ws); if (!anybad<12>(neq(h0, t12) | neq(h1, t12))) break; }
      float v[4];
#pragma unroll
      for (int r = 0; r < 4; ++r) v[r] = sw32(untag<12>(h0[r]), untag<12>(h1[r]));
      const float a0 = sw16(v[0], v[1]), a1 = sw16(v[2], v[3]);
      const float b0 = a0 + dppf<ROR8>(a0), b1 = a1 + dppf<ROR8>(a1);
      const float gg = rbf((lane & 8) ? b1 : b0);
      const auto sw = __builtin_amdgcn_permlane32_swap(asu(gg), asu(gg), false, false);
      const float hv = rbf(gg * frcp(1.f + fexp(-gg))) * asf(sw[1]);
      if (lane < 32) *(LAS u16*)(L + L_HB + (hw & 1) * 64 + (4 * e + ((lane >> 2) & 2) + (lane >> 4)) * 2) = e < 7 ? (u16)(pk_bf16(hv, hv) & 0xFFFFu) : (u16)0;
      LGKM0(); lds_set(L, fh, 5);
    }
    if (hw < 2) {
      unsigned oo = WS_O + (rg * 2 + hw) * 4096 + lane * 16, oba = L_OB + hw * 128 + (4 * n + (g & 1) * 2 + (g >> 1)) * 2;
      const float gate = *(LAS float*)(L + L_GATE + hw * 8);
      unsigned so = WS_ST + ((xc * 2 + hw) * 16 + 4 * (n & 3) + g) * 128 + (j >> 3) * 16;
      PIN(oo); PIN(oba); PIN(so);
      spin_until(tabs + (TO >> 4));
#pragma unroll 1
      for (int it = 0; it < SPIN_CAP; ++it) {
        v4u st, a0, a1, a2, a3;
        asm volatile("global_load_dwordx4 %0, %5, %7 sc1\n\tglobal_load_dwordx4 %1, %6, %7 sc1\n\tglobal_load_dwordx4 %2, %6, %7 offset:1024 sc1\n\t"
                     "global_load_dwordx4 %3, %6, %7 offset:2048 sc1\n\tglobal_load_dwordx4 %4, %6, %7 offset:3072 sc1\n\ts_waitcnt vmcnt(0)"
                     : "=&v"(st), "=&v"(a0), "=&v"(a1), "=&v"(a2), "=&v"(a3) : "v"(so), "v"(oo), "s"(ws) : "memory");
        const float ms = untag<12>(st.x), ls = untag<12>(st.y);
        float M = vmax(ms, dppf<XOR1>(ms));
#define UT2(a, i) f32x2{untag<12>(a[i]), untag<12>(a[i + 1])}
        f32x2 u0 = UT2(a0, 0), u1 = UT2(a0, 2);
        PIN(u0); PIN(u1);
        M = vmax(M, dppf<XOR2>(M));
        f32x2 u2 = UT2(a1, 0), u3 = UT2(a1, 2);
        PIN(u2); PIN(u3);
        M = x16max(M);
        f32x2 u4 = UT2(a2, 0), u5 = UT2(a2, 2);
        PIN(u4); PIN(u5);
        M = x32max(M);
        f32x2 u6 = UT2(a3, 0), u7 = UT2(a3, 2);
        PIN(u6); PIN(u7);
        const float ex = fexp(ms - M);
        unsigned bad = neq(st, t12) | neq(a0, t12);
        PIN(bad);
        const float wt0 = dppf<0x00>(ex), wt1 = dppf<0x55>(ex), wt2 = dppf<0xAA>(ex), wt3 = dppf<0xFF>(ex);
        float Ls = ex * ls; Ls += dppf<XOR1>(Ls);
        const f32x2 w0 = {wt0, wt0}, w1 = {wt1, wt1}, w2 = {wt2, wt2}, w3 = {wt3, wt3};
        f32x2 lo = __builtin_elementwise_fma(w0, u0, w1 * u2);
        PIN(lo);
        Ls += dppf<XOR2>(Ls);
        f32x2 hi = __builtin_elementwise_fma(w0, u1, w1 * u3);
        PIN(hi);
        Ls = sw16(Ls, Ls);
        lo = __builtin_elementwise_fma(w3, u6, __builtin_elementwise_fma(w2, u4, lo));
        PIN(lo);
        Ls = sw32(Ls, Ls);
        hi = __builtin_elementwise_fma(w3, u7, __builtin_elementwise_fma(w2, u5, hi));
        const float t = sw16(sw32(lo[0], lo[1]), sw32(hi[0], hi[1])) * (gate * frcp(Ls));
        *(volatile LAS u16*)(L + oba) = (u16)(pk_bf16(t, t) & 0xFFFFu);
        SB();
        if (!anybad<12>(bad | neq(a1, t12) | neq(a2, t12) | neq(a3, t12))) break;
      }
      LGKM0();
      __builtin_amdgcn_s_barrier();
    } else if (hw == 2) {
      __builtin_amdgcn_s_barrier();
    } else {
      unsigned yo = WS_Y + (((xc >> 1) * 64 + lane) * 2 + (xc & 1)) * 512 + j * 16;
      unsigned op = ((xc & 1) * 512 + (xc >> 1) * 128 + 4 * j + (g & 1) * 2 + (g >> 1)) * 2;
      PIN(yo); PIN(op);
      __builtin_amdgcn_s_barrier();
      __builtin_amdgcn_s_sleep(4);
      spin_until(tabs + (TY >> 4));
      float t;
#pragma unroll 1
      for (int it = 0; it < SPIN_CAP; ++it) {
        v4u y;
        ld1w(y, yo, ws);
        t = sw16(sw32(untag<12>(y.x), untag<12>(y.y)), sw32(untag<12>(y.z), untag<12>(y.w)));
        t += dppf<XOR1>(t); t += dppf<XOR2>(t); t += dppf<ROR4>(t); t += dppf<ROR8>(t);
        if (!anybad<12>(neq(y, t12))) break;
      }
      asm volatile("s_mov_b64 exec, %3\n\tglobal_store_short %0, %1, %2\n\ts_mov_b64 exec, -1" ::"v"(op), "v"(pk_bf16(t, t)), "s"(out), "s"(0x0001000100010001ull) : "memory");
      if (c == 0) asm volatile("s_mov_b64 exec, 1\n\tglobal_store_dword %0, %1, %2\n\ts_mov_b64 exec, -1" ::"v"(0), "v"(t32), "s"(ws) : "memory");
    }
  } else {
    const int w = wv;
    const unsigned fs = L_FS + 4 * w;
    int issued = 5, landed = 0, due = TK0 + w * (TAU / 4), seen = 0, dt = 0;
#define PUBLISH(vm_) do { if (issued - (vm_) != landed) { landed = issued - (vm_); lds_set(L, fs, landed); } } while (0)
#define PACE() do { \
      const int v0_ = vm_inflight(); PUBLISH(v0_); \
      if (seen < due || v0_ >= WIN) { \
        const int d_ = *(volatile LAS int*)(L + L_DT); \
        _Pragma("unroll 1") for (int it_ = 0; it_ < SPIN_CAP; ++it_) { \
          seen = now16(t0) - dt; \
          const int vm_ = vm_inflight(); PUBLISH(vm_); \
          if (seen >= due && vm_ < WIN) break; } \
        const int dn_ = __builtin_amdgcn_readfirstlane(d_); seen += dt - dn_; dt = dn_; } \
      vmcnt<WIN - 1>(); due += TAU; ++issued; } while (0)
    unsigned v0 = w == 3 ? 0 : lane * 16 + (w == 1 ? 1024 : 0), vq = (lane >> 3) * 8192 + (lane & 7) * 16;
    unsigned l0 = lbase + (w < 2 ? L_X : L_P - 2048) + w * 1024, lq = lbase + L_WQ + w * 1024, oq = (128 * qp + 8 * w) * 8192 + qc * 128;
    PIN(v0); PIN(vq); PINS(l0); PINS(lq); PINS(oq);
    ARGS("\n\ts_memrealtime s[98:99] ; T0_LOAD");
    DMA_S(" sc1", v0, P64(w < 2 ? ka[0] : w == 2 ? ka[2] : ka[4], w < 2 ? ka[1] : w == 2 ? ka[3] : ka[5]), l0);
#pragma unroll
    for (int i = 0; i < 4; ++i) DMA_S(" nt", vq, wq + oq + i * (32 * 8192), lq + i * 4096);
    if (w == 0 && lane < 9) *(volatile LAS int*)(L + L_FS + 4 * lane) = lane < 8 ? -64 : 0;
    if (w == 1) *(volatile LAS v2u*)(L + L_Z + 8 * lane) = v2u{0, 0};
    if (w == 3) *(volatile LAS v4u*)(L + L_WD + 7168 + 16 * lane) = v4u{0, 0, 0, 0};
    T0();
    if (w == 0) { lds_set(L, L_T0, t0); LGKM0(); }
    __builtin_amdgcn_s_barrier();
    if (has_f) {
      const unsigned voff = (lane >> 3) * 3584 + (lane & 7) * 16;
      const size_t off = (size_t)(64 * fp + 8 * w) * 3584 + fc * 128;
#pragma unroll 1
      for (int i = 0; i < 4; ++i) { PACE(); DMA_S(" nt", voff, (const char*)(i < 2 ? wg : wu) + off + (i & 1) * (32 * 3584), lbase + L_GU + (4 * i + w) * 1024); }
    } else {
      due += 4 * TAU;
    }
    {
      const char* kb = (const char*)kc + kvoff + w * 1024;
      const unsigned voff = g * 256 + ((n ^ (4 * w + g)) * 16);
#pragma unroll 1
      for (int i = 0; i < 16; ++i) { PACE(); DMA_S(" nt", voff, kb + (((i * stp + rot) & 15) << 12), lbase + L_K + i * 4096 + w * 1024); }
    }
    {
      const unsigned voff = g * 256 + ((n ^ (2 * (g | ((w >> 1) << 2)))) * 16);
      const char* vb = (const char*)vc + kvoff + w * 1024;
#pragma unroll 1
      for (int i = 0; i < 16; ++i) {
        if (i == 8) wait4<3, 0>(L, L_FH, 1);
        PACE(); DMA_S(" nt", voff, vb + (((i * stp + rot) & 15) << 12), lbase + L_V + i * 4096 + w * 1024);
      }
    }
    wait4<12, 0>(L, L_FH, 1);
    {
      const unsigned vo = g * 1024 + ((n ^ (2 * (g | ((w >> 1) << 2)))) * 16);
      const char* ob = (const char*)wo + (size_t)(64 * rg + 4 * w) * 1024 + cg * 256;
      const char* db = (const char*)wd + (size_t)(28 * rg) * 1024 + cg * 256;
#pragma unroll 1
      for (int i = 0; i < 6; ++i) {
        if (i >= 2) due += TAU3 - TAU;
        if (i == 1 && w == 3) { due += TAU; continue; }
        const char* src = i >= 2 ? ob + (i & 3) * 16384 : db + (16 * i + 4 * w) * 1024;
        PACE();
        DMA_S(" nt", vo, src, lbase + L_WO + (i >= 2 ? (i & 3) : i + 4) * 4096 + w * 1024);
      }
    }
#pragma unroll 1
    for (int it = 0; it < LDS_CAP && landed < C_V - sk; ++it) { const int vm = vm_inflight(); PUBLISH(vm); }
    unsigned t12 = *(volatile LAS unsigned*)(L + L_TAG) & 0xFFFu;
    TR_ADDR(w);
    unsigned yoff = WS_Y + ((cg * 64 + rg) * 2 + (n & 1)) * 512 + w * 128 + ((n >> 1) & 1) * 64 + g * 16;
    unsigned oa = lbase + (n < 2 ? L_OB + n * 128 + g * 16 : L_Z), ob = lbase + ((n >> 1) == 1 ? L_OB + (n & 1) * 128 + g * 16 : L_Z);
    const float g1 = *(LAS float*)(L + L_GATE + (n & 1) * 8 + 4);
    unsigned ha = lbase + (n < 2 ? L_HB + n * 64 + g * 16 : L_Z), hb = lbase + ((n >> 1) == 1 ? L_HB + (n & 1) * 64 + g * 16 : L_Z);
    vmcnt<0>(); lds_set(L, fs, C_WO - sk);
    PIN(ha); PIN(hb);
#pragma unroll 1
    for (int it = 0; it < LDS_CAP; ++it) {
      const v4i f = *(volatile LAS v4i*)(L + L_FS);
      const v2u fh2 = *(volatile LAS v2u*)(L + L_FH + 8);
      const int m = min(min(min(f.x, f.y), min(f.z, f.w)), min((int)fh2.x, (int)fh2.y) + (C_WO - sk - 5));
      if (__builtin_amdgcn_readfirstlane(m) >= C_WO - sk) break;
    }
    asm volatile("" ::: "memory");
    const v4u df0 = tr2(L + L_WD + ta0, 1024), h0 = *(LAS v4u*)(uintptr_t)ha, df1 = tr2(L + L_WD + ta1, 1024), h1 = *(LAS v4u*)(uintptr_t)hb;
    v4u wf[4];
    wf[0] = tr2(L + L_WO + ta0, 1024); wf[1] = tr2(L + L_WO + ta1, 1024); wf[2] = tr2(L + L_WO + ta0 + 8192, 1024); wf[3] = tr2(L + L_WO + ta1 + 8192, 1024);
    SB();
    f32x4 y = mfma(df1, h1, mfma(df0, h0, f32x4{0, 0, 0, 0})) * g1;
    PIN(y); PIN(wf[0]); PIN(wf[1]); PIN(wf[2]); PIN(wf[3]); PIN(oa); PIN(ob); PIN(yoff); PIN(t12);
    __builtin_amdgcn_s_setprio(3);
    __builtin_amdgcn_s_barrier();
    asm volatile("" ::: "memory");
    {
      const v4u a0 = *(LAS v4u*)(uintptr_t)oa, b0 = *(LAS v4u*)(uintptr_t)ob, a1 = *(LAS v4u*)(uintptr_t)(oa + 64), b1 = *(LAS v4u*)(uintptr_t)(ob + 64);
      SB();
      y = mfma(wf[0], a0, y); y = mfma(wf[1], b0, y); y = mfma(wf[2], a1, y); y = mfma(wf[3], b1, y);
      st_sc1<0x000F000F000F000Full>(yoff, v4u{tagt<12>(y[0], t12), tagt<12>(y[1], t12), tagt<12>(y[2], t12), tagt<12>(y[3], t12)}, ws);
    }
  }
}

extern "C" void kernel_launch(void* const* d_in, const int* in_sizes, int n_in, void* d_out, int out_size, void* d_ws, size_t ws_size, hipStream_t stream) {
  (void)hipFuncSetAttribute((const void*)fused_decode, hipFuncAttributeMaxDynamicSharedMemorySize, L_END);
  fused_decode<<<256, 512, L_END, stream>>>((const u16*)d_in[0], (const u16*)d_in[9], (char*)d_ws, (const u16*)d_in[3], (const u16*)d_in[6], (const u16*)d_in[7],
                                            (const u16*)d_in[1], (const u16*)d_in[2], (const u16*)d_in[4], (const u16*)d_in[8], (u16*)d_out);
}
```
